```python
import torch

def _softmax(x, axis=-1):
    return torch.softmax(x, dim=axis)
def _gelu(x):
    return torch.nn.functional.gelu(x, approximate="tanh")
def _silu(x):
    return torch.nn.functional.silu(x)
def _sigmoid(x):
    return torch.sigmoid(x)
def _layernorm(x, axis=-1, eps=1e-5):
    m = x.mean(dim=axis, keepdim=True)
    v = x.var(dim=axis, keepdim=True, unbiased=False)
    return (x - m) / torch.sqrt(v + eps)
def _scatter_add(tgt, idx, src, axis=0):
    return tgt.clone().index_add_(axis, idx, src)
def _segment_sum(src, seg, n):
    out = torch.zeros((n,) + src.shape[1:], dtype=src.dtype)
    return out.index_add_(0, seg, src)
def _pad(x, pw):
    flat = tuple(v for pair in reversed(pw) for v in pair)
    return torch.nn.functional.pad(x, flat)

kernel_name = 'concat_heads__gqa_block_cached__gqa_block_cached__near_ridge'

def setup_inputs():

    _w = (lambda a: (lambda t: t.to(torch.bfloat16) if t.is_floating_point() else t)(torch.as_tensor(a)))
    def _A_setup():
        B, Sq, D = 16, 96, 640
        Hq, Hkv, Sk, Dh = 32, 32, 512, 128
        assert Hq % Hkv == 0, (Hq, Hkv)
        return {
            "x": _w((lambda *s: torch.randn(*s, dtype=torch.bfloat16) * 0.8)(B, Sq, D)),
            "k_cache": _w((lambda *s: torch.randn(*s, dtype=torch.bfloat16) * 0.8)(B, Hkv, Sk, Dh)),
            "v_cache": _w((lambda *s: torch.randn(*s, dtype=torch.bfloat16) * 0.05)(B, Hkv, Sk, Dh)),
            "wq": _w((lambda *s: torch.randn(*s, dtype=torch.bfloat16) * 0.8)(D, Hq * Dh) * (D ** -0.5)),
            "wo": _w((lambda *s: torch.randn(*s, dtype=torch.bfloat16) * 0.05)(Hq * Dh, D) * ((Hq * Dh) ** -0.5)),
        }
    _A_in = _A_setup()

    def _B_setup():
        B, Sq, D = 16, 96, 640
        Hq, Hkv, Sk, Dh = 32, 32, 512, 128
        assert Hq % Hkv == 0, (Hq, Hkv)
        return {
            "x": _w((lambda *s: torch.randn(*s, dtype=torch.bfloat16) * 0.8)(B, Sq, D)),
            "k_cache": _w((lambda *s: torch.randn(*s, dtype=torch.bfloat16) * 0.8)(B, Hkv, Sk, Dh)),
            "v_cache": _w((lambda *s: torch.randn(*s, dtype=torch.bfloat16) * 0.05)(B, Hkv, Sk, Dh)),
            "wq": _w((lambda *s: torch.randn(*s, dtype=torch.bfloat16) * 0.8)(D, Hq * Dh) * (D ** -0.5)),
            "wo": _w((lambda *s: torch.randn(*s, dtype=torch.bfloat16) * 0.05)(Hq * Dh, D) * ((Hq * Dh) ** -0.5)),
        }
    _B_in = _B_setup()

    return {
        "A_x": _A_in["x"],
        "A_k_cache": _A_in["k_cache"],
        "A_v_cache": _A_in["v_cache"],
        "A_wq": _A_in["wq"],
        "A_wo": _A_in["wo"],
        "B_x": _B_in["x"],
        "B_k_cache": _B_in["k_cache"],
        "B_v_cache": _B_in["v_cache"],
        "B_wq": _B_in["wq"],
        "B_wo": _B_in["wo"],
    }

def reference(A_x, A_k_cache, A_v_cache, A_wq, A_wo, B_x, B_k_cache, B_v_cache, B_wq, B_wo):
    def _A_ref(x, k_cache, v_cache, wq, wo):
        B, Sq, D = x.shape
        Hq, Dh = 32, 128
        Hkv = k_cache.shape[1]
        rep = Hq // Hkv
        q = (lambda x, *d: x.permute(*d))((x @ wq).reshape(B, Sq, Hq, Dh), 0, 2, 1, 3)
        k = (lambda x, n, axis: torch.repeat_interleave(x, n, dim=axis))(k_cache, rep, axis=1)
        v = (lambda x, n, axis: torch.repeat_interleave(x, n, dim=axis))(v_cache, rep, axis=1)
        scores = torch.einsum("bhqd,bhkd->bhqk", q, k) * (Dh ** -0.5)
        p = _softmax(scores, axis=-1)
        o = torch.einsum("bhqk,bhkd->bhqd", p, v)
        return (lambda x, *d: x.permute(*d))(o, 0, 2, 1, 3).reshape(B, Sq, Hq * Dh) @ wo

    def _B_ref(x, k_cache, v_cache, wq, wo):
        B, Sq, D = x.shape
        Hq, Dh = 32, 128
        Hkv = k_cache.shape[1]
        rep = Hq // Hkv
        q = (lambda x, *d: x.permute(*d))((x @ wq).reshape(B, Sq, Hq, Dh), 0, 2, 1, 3)
        k = (lambda x, n, axis: torch.repeat_interleave(x, n, dim=axis))(k_cache, rep, axis=1)
        v = (lambda x, n, axis: torch.repeat_interleave(x, n, dim=axis))(v_cache, rep, axis=1)
        scores = torch.einsum("bhqd,bhkd->bhqk", q, k) * (Dh ** -0.5)
        p = _softmax(scores, axis=-1)
        o = torch.einsum("bhqk,bhkd->bhqd", p, v)
        return (lambda x, *d: x.permute(*d))(o, 0, 2, 1, 3).reshape(B, Sq, Hq * Dh) @ wo

    a = _A_ref(A_x, A_k_cache, A_v_cache, A_wq, A_wo)
    b = _B_ref(B_x, B_k_cache, B_v_cache, B_wq, B_wo)
    return torch.concatenate([a, b], axis=-1)
```

```cpp
#include <hip/hip_runtime.h>
#include <hip/hip_bf16.h>
#include <stdint.h>

typedef __attribute__((ext_vector_type(8))) __bf16 bf16x8;
typedef __attribute__((ext_vector_type(16))) float f32x16;
typedef unsigned short u16;

constexpr int NB = 16, SQ = 96, DM = 640, NH = 32, SK = 512, DH = 128, HD = NH * DH;

__device__ inline float bf2f(u16 v) { return __uint_as_float(((unsigned)v) << 16); }
__device__ inline u16 f2bf(float f) { __bf16 b = (__bf16)f; return __builtin_bit_cast(u16, b); }

__global__ __launch_bounds__(64) void gemm_naive(const u16* A, const u16* B, u16* C, int K, int N, int ldc) {
  const int lane = threadIdx.x, r = lane & 31, h = lane >> 5;
  const int m0 = blockIdx.y * 32, n0 = blockIdx.x * 32;
  f32x16 acc = {};
  for (int k = 0; k < K; k += 16) {
    bf16x8 a = *(const bf16x8*)(A + (size_t)(m0 + r) * K + k + 8 * h);
    bf16x8 b;
    for (int j = 0; j < 8; ++j) b[j] = __builtin_bit_cast(__bf16, B[(size_t)(k + 8 * h + j) * N + n0 + r]);
    acc = __builtin_amdgcn_mfma_f32_32x32x16_bf16(a, b, acc, 0, 0, 0);
  }
  for (int i = 0; i < 16; ++i) {
    int row = (i & 3) + 8 * (i >> 2) + 4 * h;
    C[(size_t)(m0 + row) * ldc + n0 + r] = f2bf(acc[i]);
  }
}

__global__ __launch_bounds__(64) void attn_naive(const u16* Q, const u16* Kc, const u16* Vc, u16* O) {
  const int lane = threadIdx.x, r = lane & 31, h = lane >> 5;
  const int qt = blockIdx.x, hd = blockIdx.y, b = blockIdx.z;
  const u16* q = Q + (size_t)(b * SQ + qt * 32 + r) * HD + hd * DH;
  const u16* K = Kc + (size_t)(b * NH + hd) * SK * DH;
  const u16* V = Vc + (size_t)(b * NH + hd) * SK * DH;
  bf16x8 qf[8];
  for (int s = 0; s < 8; ++s) qf[s] = *(const bf16x8*)(q + 16 * s + 8 * h);
  f32x16 o[4] = {};
  float m = -1e30f, l = 0.f;
  const float c = 0.08838834764831845f * 1.4426950408889634f;
  for (int kt = 0; kt < SK; kt += 32) {
    f32x16 s = {};
    for (int d = 0; d < 8; ++d) {
      bf16x8 kf = *(const bf16x8*)(K + (size_t)(kt + r) * DH + 16 * d + 8 * h);
      s = __builtin_amdgcn_mfma_f32_32x32x16_bf16(kf, qf[d], s, 0, 0, 0);
    }
    float mx = s[0];
    for (int i = 1; i < 16; ++i) mx = fmaxf(mx, s[i]);
    mx = fmaxf(mx, __shfl_xor(mx, 32));
    float mn = fmaxf(m, mx * c);
    float al = exp2f(m - mn);
    m = mn;
    float ps = 0.f;
    bf16x8 p[2];
    for (int i = 0; i < 16; ++i) { float e = exp2f(s[i] * c - mn); ps += e; p[i >> 3][i & 7] = (__bf16)e; }
    ps += __shfl_xor(ps, 32);
    l = l * al + ps;
    for (int d = 0; d < 4; ++d) {
      for (int i = 0; i < 16; ++i) o[d][i] *= al;
      for (int ks = 0; ks < 2; ++ks) {
        bf16x8 vf;
        for (int j = 0; j < 8; ++j) {
          int key = kt + 16 * ks + 8 * (j >> 2) + 4 * h + (j & 3);
          vf[j] = __builtin_bit_cast(__bf16, V[(size_t)key * DH + 32 * d + r]);
        }
        o[d] = __builtin_amdgcn_mfma_f32_32x32x16_bf16(vf, p[ks], o[d], 0, 0, 0);
      }
    }
  }
  float inv = 1.f / l;
  u16* op = O + (size_t)(b * SQ + qt * 32 + r) * HD + hd * DH;
  for (int d = 0; d < 4; ++d)
    for (int i = 0; i < 16; ++i) op[32 * d + (i & 3) + 8 * (i >> 2) + 4 * h] = f2bf(o[d][i] * inv);
}

extern "C" void kernel_launch(void* const* d_in, const int* in_sizes, int n_in, void* d_out, int out_size,
                              void* d_ws, size_t ws_size, hipStream_t stream) {
  u16* ws = (u16*)d_ws;
  for (int br = 0; br < 2; ++br) {
    const u16* x = (const u16*)d_in[5 * br + 0];
    const u16* kc = (const u16*)d_in[5 * br + 1];
    const u16* vc = (const u16*)d_in[5 * br + 2];
    const u16* wq = (const u16*)d_in[5 * br + 3];
    const u16* wo = (const u16*)d_in[5 * br + 4];
    u16* Q = ws + (size_t)br * 2 * NB * SQ * HD;
    u16* O = Q + (size_t)NB * SQ * HD;
    gemm_naive<<<dim3(HD / 32, NB * SQ / 32), 64, 0, stream>>>(x, wq, Q, DM, HD, HD);
    attn_naive<<<dim3(3, NH, NB), 64, 0, stream>>>(Q, kc, vc, O);
    gemm_naive<<<dim3(DM / 32, NB * SQ / 32), 64, 0, stream>>>(O, wo, (u16*)d_out + br * DM, HD, DM, 2 * DM);
  }
}
```

```cpp
#include <hip/hip_runtime.h>
#include <hip/hip_bf16.h>
#include <stdint.h>

#define STAMPS 0

struct Args {
  const char* x[2];
  const char* k[2];
  const char* v[2];
  const char* wq[2];
  const char* wo[2];
  char* out;
  char* ws;
};

constexpr size_t PART_BYTES = (size_t)256 * 122880, DBG_BYTES = 1024 * 256;

__global__ __launch_bounds__(256, 1) void fused_k(Args a) {
  __shared__ __attribute__((aligned(16))) char smem[163840];
  const unsigned bid = blockIdx.x, xcd = bid & 7, loc = bid >> 3;
  const unsigned br = xcd >> 2, hq = xcd & 3, b = loc >> 1, hg = hq * 2 + (loc & 1), grp = br * 16 + b;
  const unsigned w = __builtin_amdgcn_readfirstlane(threadIdx.x >> 6);
  const unsigned head0 = (hg & 3) | ((hg >> 2) << 4), head = head0 + 4 * w;
  const char* px = (br ? a.x[1] : a.x[0]) + (size_t)b * (96 * 1280);
  const char* pw = (br ? a.wq[1] : a.wq[0]) + (size_t)head * 256;
  const char* pk = (br ? a.k[1] : a.k[0]) + (size_t)(b * 32 + head) * 131072;
  const char* pv = (br ? a.v[1] : a.v[0]) + (size_t)(b * 32 + head) * 131072;
  const char* po = (br ? a.wo[1] : a.wo[0]) + (size_t)(head0 * 128 + w * 32) * 1280;
  char* pg = a.ws + (size_t)grp * (8 * 122880);
  char* pout = a.out + (size_t)b * (96 * 2560) + br * 1280;
  char* pc = a.ws + PART_BYTES + DBG_BYTES + (size_t)grp * 1024;
  const unsigned rot_c = __builtin_amdgcn_readfirstlane((b % 10 * 1 + 0 * (loc & 1)) % 10);
  const unsigned hsh = (bid * 4 + w) * 2654435761u;
  const unsigned rot_kv = ((hsh >> 20) & 31) << 12, str_kv = 4096;
  const unsigned rot_b = b % 5, rot_h = (b / 5) & 3;
  char* pd = a.ws + PART_BYTES + (size_t)(bid * 4 + w) * 256;
  asm volatile(
      "s_mov_b64 s[36:37], %0\n"
      "s_and_b32 s37, s37, 0xffff\n"
      "s_mov_b32 s38, 122880\n"
      "s_mov_b32 s39, 0x20000\n"
      "s_mov_b64 s[40:41], %1\n"
      "s_and_b32 s41, s41, 0xffff\n"
      "s_mov_b32 s42, 5234944\n"
      "s_mov_b32 s43, 0x20000\n"
      "s_mov_b64 s[64:65], %7\n"
      "s_and_b32 s65, s65, 0xffff\n"
      "s_mov_b32 s66, 1024\n"
      "s_mov_b32 s67, 0x20000\n"
      "s_lshl_b32 s72, %8, 13\n"
      "s_mul_i32 s68, %9, 524288\n"
      "s_lshl_b32 s69, %9, 7\n"
      "s_lshl_b32 s81, %8, 10\n"
      "v_mbcnt_lo_u32_b32 v192, -1, 0\n"
      "v_mbcnt_hi_u32_b32 v192, -1, v192\n"
      "v_bfe_u32 v193, v192, 2, 2\n"
      "v_lshrrev_b32 v194, 4, v192\n"
      "v_and_b32 v195, 3, v192\n"
      "s_getpc_b64 s[92:93]\n"
      "PC0_%=:\n"
      "s_lshl_b32 s73, %15, 9\n"
      "v_lshl_add_u32 v196, v195, 7, s73\n"
      "v_min_u32 v196, LEND_%=-PC0_%=-4, v196\n"
      "global_load_dword v200, v196, s[92:93]\n"
      "s_and_b32 s73, %8, 1\n"
      "s_lshl_b32 s73, s73, 1\n"
      "s_mul_i32 s74, %8, 10240\n"
      "v_xor_b32 v196, s73, v195\n"
      "v_lshlrev_b32 v196, 4, v196\n"
      "v_lshrrev_b32 v197, 5, v192\n"
      "v_lshl_add_u32 v196, v197, 6, v196\n"
      "v_bfe_u32 v197, v192, 2, 3\n"
      "v_mul_u32_u24 v197, 1280, v197\n"
      "v_add3_u32 v252, v196, v197, s74\n"
      "s_add_u32 m0, s81, 139264\n"
      "s_add_u32 s73, s69, 0\n"
      "buffer_load_dwordx4 v252, s[36:39], s73 offen offset:0  lds\n"
      "s_add_u32 m0, s81, 143360\n"
      "s_add_u32 s73, s69, 40960\n"
      "buffer_load_dwordx4 v252, s[36:39], s73 offen offset:0  lds\n"
      "s_add_u32 m0, s81, 147456\n"
      "s_add_u32 s73, s69, 81920\n"
      "buffer_load_dwordx4 v252, s[36:39], s73 offen offset:0  lds\n"
      "s_add_u32 s69, s69, 128\n"
      "s_cmp_eq_u32 s69, 1280\n"
      "s_cselect_b32 s69, 0, s69\n"
      "v_xor_b32 v196, 0, v195\n"
      "v_lshlrev_b32 v196, 4, v196\n"
      "v_lshl_add_u32 v196, v194, 6, v196\n"
      "v_mul_u32_u24 v197, 8192, v193\n"
      "v_add_u32 v196, v196, v197\n"
      "v_add_u32 v247, 0, v196\n"
      "v_xor_b32 v196, 0, v195\n"
      "v_lshlrev_b32 v196, 4, v196\n"
      "v_lshl_add_u32 v196, v194, 6, v196\n"
      "v_mul_u32_u24 v197, 8192, v193\n"
      "v_add_u32 v196, v196, v197\n"
      "v_add_u32 v248, 31744, v196\n"
      "v_xor_b32 v196, 2, v195\n"
      "v_lshlrev_b32 v196, 4, v196\n"
      "v_lshl_add_u32 v196, v194, 6, v196\n"
      "v_mul_u32_u24 v197, 8192, v193\n"
      "v_add_u32 v196, v196, v197\n"
      "v_add_u32 v249, 63488, v196\n"
      "v_xor_b32 v196, 2, v195\n"
      "v_lshlrev_b32 v196, 4, v196\n"
      "v_lshl_add_u32 v196, v194, 6, v196\n"
      "v_mul_u32_u24 v197, 8192, v193\n"
      "v_add_u32 v196, v196, v197\n"
      "v_add_u32 v250, 95232, v196\n"
      "s_add_u32 m0, s72, 32768\n"
      "s_nop 0\n"
      "buffer_load_dwordx4 v247, s[40:43], s68 offen offset:0  lds\n"
      "buffer_load_dwordx4 v248, s[40:43], s68 offen offset:1024  lds\n"
      "buffer_load_dwordx4 v249, s[40:43], s68 offen offset:2048  lds\n"
      "buffer_load_dwordx4 v250, s[40:43], s68 offen offset:3072  lds\n"
      "s_add_u32 m0, s72, 36864\n"
      "s_add_u32 s68, s68, 131072\n"
      "buffer_load_dwordx4 v247, s[40:43], s68 offen offset:0  lds\n"
      "buffer_load_dwordx4 v248, s[40:43], s68 offen offset:1024  lds\n"
      "buffer_load_dwordx4 v249, s[40:43], s68 offen offset:2048  lds\n"
      "buffer_load_dwordx4 v250, s[40:43], s68 offen offset:3072  lds\n"
      "s_add_u32 s68, s68, 131072\n"
      "s_cmp_eq_u32 s68, 5242880\n"
      "s_cselect_b32 s68, 0, s68\n"
      "s_lshl_b32 s73, %13, 2\n"
      "s_add_u32 s73, s73, 256\n"
      "v_and_b32 v199, 7, v192\n"
      "v_lshl_add_u32 v199, v199, 6, s73\n"
      "s_lshl_b32 s73, %13, 2\n"
      "v_mov_b32 v196, s73\n"
      "v_cmp_gt_u32 vcc, 8, v192\n"
      "s_nop 1\n"
      "v_cndmask_b32 v199, v196, v199, vcc\n"
      "buffer_load_dword v198, v199, s[64:67], 0 offen offset:0 sc1\n"
      "s_mov_b64 s[44:45], %2\n"
      "s_and_b32 s45, s45, 0xffff\n"
      "s_mov_b32 s46, 131072\n"
      "s_mov_b32 s47, 0x20000\n"
      "s_mov_b64 s[48:49], %3\n"
      "s_and_b32 s49, s49, 0xffff\n"
      "s_mov_b32 s50, 131072\n"
      "s_mov_b32 s51, 0x20000\n"
      "s_mov_b64 s[52:53], %4\n"
      "s_and_b32 s53, s53, 0xffff\n"
      "s_mov_b32 s54, 2007040\n"
      "s_mov_b32 s55, 0x20000\n"
      "s_mov_b64 s[56:57], %5\n"
      "s_and_b32 s57, s57, 0xffff\n"
      "s_mov_b32 s58, 983040\n"
      "s_mov_b32 s59, 0x20000\n"
      "s_mov_b64 s[60:61], %6\n"
      "s_and_b32 s61, s61, 0xffff\n"
      "s_mov_b32 s62, 244480\n"
      "s_mov_b32 s63, 0x20000\n"
      "s_mov_b32 s70, %10\n"
      "s_mov_b32 s71, %10\n"
      "s_mov_b32 s82, %11\n"
      "s_mov_b32 s86, %11\n"
      "s_mov_b32 s83, %12\n"
      "s_mov_b32 s87, %12\n"
      "s_mov_b32 s76, 0x3e0293ee\n"
      "s_mov_b32 s77, 0x41000000\n"
      "s_mov_b32 s79, s72\n"
      "s_add_u32 s80, s72, 32768\n"
      "v_lshlrev_b32 v242, 4, v192\n"
      "s_lshl_b32 s78, %8, 12\n"
      "s_add_u32 s78, s78, 98304\n"
      "s_sub_u32 s74, s78, s72\n"
      "v_and_b32 v196, 1, v194\n"
      "v_xor_b32 v196, 0, v196\n"
      "v_lshlrev_b32 v196, 5, v196\n"
      "v_lshl_add_u32 v196, v193, 6, v196\n"
      "v_lshl_add_u32 v196, v195, 3, v196\n"
      "v_mul_u32_u24 v197, 2048, v194\n"
      "v_add3_u32 v196, v196, v197, s72\n"
      "v_add_u32 v243, 0, v196\n"
      "v_add_u32 v245, 65536, v243\n"
      "v_and_b32 v196, 1, v194\n"
      "v_xor_b32 v196, 0, v196\n"
      "v_lshlrev_b32 v196, 5, v196\n"
      "v_lshl_add_u32 v196, v193, 6, v196\n"
      "v_lshl_add_u32 v196, v195, 3, v196\n"
      "v_mul_u32_u24 v197, 1024, v194\n"
      "v_add3_u32 v196, v196, v197, s72\n"
      "v_add_u32 v232, 32768, v196\n"
      "v_add_u32 v202, s74, v196\n"
      "v_and_b32 v196, 1, v194\n"
      "v_xor_b32 v196, 1, v196\n"
      "v_lshlrev_b32 v196, 5, v196\n"
      "v_lshl_add_u32 v196, v193, 6, v196\n"
      "v_lshl_add_u32 v196, v195, 3, v196\n"
      "v_mul_u32_u24 v197, 2048, v194\n"
      "v_add3_u32 v196, v196, v197, s72\n"
      "v_add_u32 v244, 0, v196\n"
      "v_add_u32 v246, 65536, v244\n"
      "v_and_b32 v196, 1, v194\n"
      "v_xor_b32 v196, 1, v196\n"
      "v_lshlrev_b32 v196, 5, v196\n"
      "v_lshl_add_u32 v196, v193, 6, v196\n"
      "v_lshl_add_u32 v196, v195, 3, v196\n"
      "v_mul_u32_u24 v197, 1024, v194\n"
      "v_add3_u32 v196, v196, v197, s72\n"
      "v_add_u32 v233, 32768, v196\n"
      "v_add_u32 v203, s74, v196\n"
      "v_bfe_u32 v197, v192, 3, 1\n"
      "v_lshlrev_b32 v196, 1, v197\n"
      "v_xor_b32 v196, v196, v194\n"
      "v_lshlrev_b32 v196, 4, v196\n"
      "v_lshl_add_u32 v196, v197, 10, v196\n"
      "v_and_b32 v197, 7, v192\n"
      "v_lshl_add_u32 v196, v197, 6, v196\n"
      "v_add_u32 v251, 114688, v196\n"
      "v_and_b32 v196, 1, v194\n"
      "v_lshlrev_b32 v196, 1, v196\n"
      "v_lshrrev_b32 v197, 1, v194\n"
      "v_or_b32 v196, v196, v197\n"
      "v_bfe_u32 v197, v192, 3, 1\n"
      "v_mul_u32_u24 v197, 3, v197\n"
      "v_xor_b32 v196, v196, v197\n"
      "v_lshlrev_b32 v196, 4, v196\n"
      "v_lshl_add_u32 v196, v193, 10, v196\n"
      "v_lshl_add_u32 v196, v195, 6, v196\n"
      "v_add_u32 v231, s72, v196\n"
      "v_add_u32 v201, s78, v196\n"
      "v_xor_b32 v196, 0, v195\n"
      "v_lshlrev_b32 v196, 4, v196\n"
      "v_lshl_add_u32 v196, v194, 6, v196\n"
      "v_mul_u32_u24 v197, 256, v193\n"
      "v_add_u32 v196, v196, v197\n"
      "v_add_u32 v234, 0, v196\n"
      "v_xor_b32 v196, 3, v195\n"
      "v_lshlrev_b32 v196, 4, v196\n"
      "v_lshl_add_u32 v196, v194, 6, v196\n"
      "v_mul_u32_u24 v197, 256, v193\n"
      "v_add_u32 v196, v196, v197\n"
      "v_add_u32 v235, 0, v196\n"
      "v_xor_b32 v196, 2, v195\n"
      "v_lshlrev_b32 v196, 4, v196\n"
      "v_lshl_add_u32 v196, v194, 6, v196\n"
      "v_mul_u32_u24 v197, 256, v193\n"
      "v_add_u32 v196, v196, v197\n"
      "v_add_u32 v236, 0, v196\n"
      "v_mov_b32 v196, 0x3f803f80\n"
      "v_accvgpr_write_b32 a248, v196\n"
      "v_accvgpr_write_b32 a249, v196\n"
      "v_accvgpr_write_b32 a250, v196\n"
      "v_accvgpr_write_b32 a251, v196\n"
      "s_waitcnt vmcnt(9)\n"
      "s_barrier\n"
      "ds_read_b128 a[32:35], v251 offset:24576\n"
      "ds_read_b128 a[36:39], v251 offset:26624\n"
      "ds_read_b128 a[40:43], v251 offset:28672\n"
      "ds_read_b128 a[44:47], v251 offset:30720\n"
      "ds_read_b128 a[48:51], v251 offset:32768\n"
      "ds_read_b128 a[52:55], v251 offset:34816\n"
      "s_add_u32 m0, s72, 65536\n"
      "s_nop 0\n"
      "buffer_load_dwordx4 v247, s[40:43], s68 offen offset:0  lds\n"
      "buffer_load_dwordx4 v248, s[40:43], s68 offen offset:1024  lds\n"
      "buffer_load_dwordx4 v249, s[40:43], s68 offen offset:2048  lds\n"
      "buffer_load_dwordx4 v250, s[40:43], s68 offen offset:3072  lds\n"
      "s_add_u32 m0, s72, 69632\n"
      "s_add_u32 s68, s68, 131072\n"
      "buffer_load_dwordx4 v247, s[40:43], s68 offen offset:0  lds\n"
      "buffer_load_dwordx4 v248, s[40:43], s68 offen offset:1024  lds\n"
      "buffer_load_dwordx4 v249, s[40:43], s68 offen offset:2048  lds\n"
      "buffer_load_dwordx4 v250, s[40:43], s68 offen offset:3072  lds\n"
      "s_add_u32 s68, s68, 131072\n"
      "s_cmp_eq_u32 s68, 5242880\n"
      "s_cselect_b32 s68, 0, s68\n"
      "s_waitcnt vmcnt(9)\n"
      "ds_read_b64_tr_b16 a[0:1], v243 offset:32768\n"
      "ds_read_b64_tr_b16 a[2:3], v243 offset:33792\n"
      "ds_read_b64_tr_b16 a[4:5], v244 offset:32768\n"
      "ds_read_b64_tr_b16 a[6:7], v244 offset:33792\n"
      "ds_read_b64_tr_b16 a[8:9], v243 offset:33024\n"
      "ds_read_b64_tr_b16 a[10:11], v243 offset:34048\n"
      "ds_read_b64_tr_b16 a[12:13], v244 offset:33024\n"
      "ds_read_b64_tr_b16 a[14:15], v244 offset:34048\n"
      "ds_read_b64_tr_b16 a[16:17], v243 offset:33280\n"
      "ds_read_b64_tr_b16 a[18:19], v243 offset:34304\n"
      "ds_read_b64_tr_b16 a[20:21], v244 offset:33280\n"
      "ds_read_b64_tr_b16 a[22:23], v244 offset:34304\n"
      "ds_read_b64_tr_b16 a[24:25], v243 offset:33536\n"
      "ds_read_b64_tr_b16 a[26:27], v243 offset:34560\n"
      "s_waitcnt lgkmcnt(12)\n"
      "v_mfma_f32_16x16x32_bf16 v[0:3], a[0:3], a[32:35], 0\n"
      "s_add_u32 m0, s81, 151552\n"
      "ds_read_b64_tr_b16 a[28:29], v244 offset:33536\n"
      "v_mfma_f32_16x16x32_bf16 v[32:35], a[0:3], a[36:39], 0\n"
      "s_add_u32 s73, s69, 0\n"
      "ds_read_b64_tr_b16 a[30:31], v244 offset:34560\n"
      "v_mfma_f32_16x16x32_bf16 v[64:67], a[0:3], a[40:43], 0\n"
      "buffer_load_dwordx4 v252, s[36:39], s73 offen offset:0  lds\n"
      "v_mfma_f32_16x16x32_bf16 v[96:99], a[0:3], a[44:47], 0\n"
      "s_add_u32 m0, s81, 155648\n"
      "v_mfma_f32_16x16x32_bf16 v[128:131], a[0:3], a[48:51], 0\n"
      "s_add_u32 s73, s69, 40960\n"
      "ds_read_b128 a[56:59], v251 offset:25088\n"
      "v_mfma_f32_16x16x32_bf16 v[160:163], a[0:3], a[52:55], 0\n"
      "s_waitcnt lgkmcnt(13)\n"
      "buffer_load_dwordx4 v252, s[36:39], s73 offen offset:0  lds\n"
      "v_mfma_f32_16x16x32_bf16 v[4:7], a[4:7], a[32:35], 0\n"
      "s_add_u32 m0, s81, 159744\n"
      "v_mfma_f32_16x16x32_bf16 v[36:39], a[4:7], a[36:39], 0\n"
      "s_add_u32 s73, s69, 81920\n"
      "v_mfma_f32_16x16x32_bf16 v[68:71], a[4:7], a[40:43], 0\n"
      "buffer_load_dwordx4 v252, s[36:39], s73 offen offset:0  lds\n"
      "v_mfma_f32_16x16x32_bf16 v[100:103], a[4:7], a[44:47], 0\n"
      "s_add_u32 s69, s69, 128\n"
      "v_mfma_f32_16x16x32_bf16 v[132:135], a[4:7], a[48:51], 0\n"
      "s_cmp_eq_u32 s69, 1280\n"
      "s_cselect_b32 s69, 0, s69\n"
      "ds_read_b128 a[60:63], v251 offset:27136\n"
      "v_mfma_f32_16x16x32_bf16 v[164:167], a[4:7], a[52:55], 0\n"
      "s_waitcnt lgkmcnt(12)\n"
      "v_mfma_f32_16x16x32_bf16 v[8:11], a[8:11], a[32:35], 0\n"
      "s_add_u32 m0, s72, 0\n"
      "v_mfma_f32_16x16x32_bf16 v[40:43], a[8:11], a[36:39], 0\n"
      "v_mfma_f32_16x16x32_bf16 v[72:75], a[8:11], a[40:43], 0\n"
      "v_mfma_f32_16x16x32_bf16 v[104:107], a[8:11], a[44:47], 0\n"
      "buffer_load_dwordx4 v247, s[40:43], s68 offen offset:0  lds\n"
      "v_mfma_f32_16x16x32_bf16 v[136:139], a[8:11], a[48:51], 0\n"
      "ds_read_b128 a[64:67], v251 offset:29184\n"
      "v_mfma_f32_16x16x32_bf16 v[168:171], a[8:11], a[52:55], 0\n"
      "s_waitcnt lgkmcnt(11)\n"
      "v_mfma_f32_16x16x32_bf16 v[12:15], a[12:15], a[32:35], 0\n"
      "buffer_load_dwordx4 v248, s[40:43], s68 offen offset:1024  lds\n"
      "v_mfma_f32_16x16x32_bf16 v[44:47], a[12:15], a[36:39], 0\n"
      "v_mfma_f32_16x16x32_bf16 v[76:79], a[12:15], a[40:43], 0\n"
      "buffer_load_dwordx4 v249, s[40:43], s68 offen offset:2048  lds\n"
      "v_mfma_f32_16x16x32_bf16 v[108:111], a[12:15], a[44:47], 0\n"
      "v_mfma_f32_16x16x32_bf16 v[140:143], a[12:15], a[48:51], 0\n"
      "ds_read_b128 a[68:71], v251 offset:31232\n"
      "v_mfma_f32_16x16x32_bf16 v[172:175], a[12:15], a[52:55], 0\n"
      "s_waitcnt lgkmcnt(10)\n"
      "buffer_load_dwordx4 v250, s[40:43], s68 offen offset:3072  lds\n"
      "v_mfma_f32_16x16x32_bf16 v[16:19], a[16:19], a[32:35], 0\n"
      "v_mfma_f32_16x16x32_bf16 v[48:51], a[16:19], a[36:39], 0\n"
      "v_mfma_f32_16x16x32_bf16 v[80:83], a[16:19], a[40:43], 0\n"
      "s_add_u32 m0, s72, 4096\n"
      "s_add_u32 s68, s68, 131072\n"
      "buffer_load_dwordx4 v247, s[40:43], s68 offen offset:0  lds\n"
      "v_mfma_f32_16x16x32_bf16 v[112:115], a[16:19], a[44:47], 0\n"
      "buffer_load_dwordx4 v248, s[40:43], s68 offen offset:1024  lds\n"
      "v_mfma_f32_16x16x32_bf16 v[144:147], a[16:19], a[48:51], 0\n"
      "ds_read_b128 a[72:75], v251 offset:33280\n"
      "v_mfma_f32_16x16x32_bf16 v[176:179], a[16:19], a[52:55], 0\n"
      "s_waitcnt lgkmcnt(9)\n"
      "ds_read_b128 a[76:79], v251 offset:35328\n"
      "v_mfma_f32_16x16x32_bf16 v[20:23], a[20:23], a[32:35], 0\n"
      "s_waitcnt vmcnt(9)\n"
      "ds_read_b64_tr_b16 a[0:1], v245 offset:0\n"
      "v_mfma_f32_16x16x32_bf16 v[52:55], a[20:23], a[36:39], 0\n"
      "ds_read_b64_tr_b16 a[2:3], v245 offset:1024\n"
      "v_mfma_f32_16x16x32_bf16 v[84:87], a[20:23], a[40:43], 0\n"
      "ds_read_b64_tr_b16 a[4:5], v246 offset:0\n"
      "v_mfma_f32_16x16x32_bf16 v[116:119], a[20:23], a[44:47], 0\n"
      "ds_read_b64_tr_b16 a[6:7], v246 offset:1024\n"
      "v_mfma_f32_16x16x32_bf16 v[148:151], a[20:23], a[48:51], 0\n"
      "ds_read_b64_tr_b16 a[8:9], v245 offset:256\n"
      "v_mfma_f32_16x16x32_bf16 v[180:183], a[20:23], a[52:55], 0\n"
      "s_waitcnt lgkmcnt(13)\n"
      "ds_read_b64_tr_b16 a[10:11], v245 offset:1280\n"
      "v_mfma_f32_16x16x32_bf16 v[24:27], a[24:27], a[32:35], 0\n"
      "ds_read_b64_tr_b16 a[12:13], v246 offset:256\n"
      "v_mfma_f32_16x16x32_bf16 v[56:59], a[24:27], a[36:39], 0\n"
      "ds_read_b64_tr_b16 a[14:15], v246 offset:1280\n"
      "v_mfma_f32_16x16x32_bf16 v[88:91], a[24:27], a[40:43], 0\n"
      "ds_read_b64_tr_b16 a[16:17], v245 offset:512\n"
      "v_mfma_f32_16x16x32_bf16 v[120:123], a[24:27], a[44:47], 0\n"
      "ds_read_b64_tr_b16 a[18:19], v245 offset:1536\n"
      "v_mfma_f32_16x16x32_bf16 v[152:155], a[24:27], a[48:51], 0\n"
      "ds_read_b64_tr_b16 a[20:21], v246 offset:512\n"
      "v_mfma_f32_16x16x32_bf16 v[184:187], a[24:27], a[52:55], 0\n"
      "s_waitcnt lgkmcnt(14)\n"
      "ds_read_b64_tr_b16 a[22:23], v246 offset:1536\n"
      "v_mfma_f32_16x16x32_bf16 v[28:31], a[28:31], a[32:35], 0\n"
      "ds_read_b64_tr_b16 a[24:25], v245 offset:768\n"
      "v_mfma_f32_16x16x32_bf16 v[60:63], a[28:31], a[36:39], 0\n"
      "ds_read_b64_tr_b16 a[26:27], v245 offset:1792\n"
      "v_mfma_f32_16x16x32_bf16 v[92:95], a[28:31], a[40:43], 0\n"
      "buffer_load_dwordx4 v249, s[40:43], s68 offen offset:2048  lds\n"
      "v_mfma_f32_16x16x32_bf16 v[124:127], a[28:31], a[44:47], 0\n"
      "buffer_load_dwordx4 v250, s[40:43], s68 offen offset:3072  lds\n"
      "s_add_u32 s68, s68, 131072\n"
      "v_mfma_f32_16x16x32_bf16 v[156:159], a[28:31], a[48:51], 0\n"
      "s_cmp_eq_u32 s68, 5242880\n"
      "s_cselect_b32 s68, 0, s68\n"
      "v_mfma_f32_16x16x32_bf16 v[188:191], a[28:31], a[52:55], 0\n"
      "s_waitcnt lgkmcnt(12)\n"
      "v_mfma_f32_16x16x32_bf16 v[0:3], a[0:3], a[56:59], v[0:3]\n"
      "s_waitcnt vmcnt(8)\n"
      "s_barrier\n"
      "ds_read_b64_tr_b16 a[28:29], v246 offset:768\n"
      "v_mfma_f32_16x16x32_bf16 v[32:35], a[0:3], a[60:63], v[32:35]\n"
      "s_mov_b32 m0, s78\n"
      "ds_read_b64_tr_b16 a[30:31], v246 offset:1792\n"
      "v_mfma_f32_16x16x32_bf16 v[64:67], a[0:3], a[64:67], v[64:67]\n"
      "buffer_load_dwordx4 v234, s[44:47], s70 offen offset:0 nt lds\n"
      "buffer_load_dwordx4 v234, s[44:47], s70 offen offset:1024 nt lds\n"
      "v_mfma_f32_16x16x32_bf16 v[96:99], a[0:3], a[68:71], v[96:99]\n"
      "buffer_load_dwordx4 v235, s[44:47], s70 offen offset:2048 nt lds\n"
      "v_mfma_f32_16x16x32_bf16 v[128:131], a[0:3], a[72:75], v[128:131]\n"
      "ds_read_b128 a[32:35], v251 offset:36864\n"
      "v_mfma_f32_16x16x32_bf16 v[160:163], a[0:3], a[76:79], v[160:163]\n"
      "s_waitcnt lgkmcnt(13)\n"
      "buffer_load_dwordx4 v235, s[44:47], s70 offen offset:3072 nt lds\n"
      "s_add_u32 s70, s70, %16\n"
      "v_mfma_f32_16x16x32_bf16 v[4:7], a[4:7], a[56:59], v[4:7]\n"
      "s_waitcnt vmcnt(4)\n"
      "s_and_b32 s70, s70, 0x1ffff\n"
      "ds_read_b64_tr_b16 a[0:1], v243 offset:0\n"
      "v_mfma_f32_16x16x32_bf16 v[36:39], a[4:7], a[60:63], v[36:39]\n"
      "s_add_u32 m0, s72, 32768\n"
      "ds_read_b64_tr_b16 a[2:3], v243 offset:1024\n"
      "v_mfma_f32_16x16x32_bf16 v[68:71], a[4:7], a[64:67], v[68:71]\n"
      "v_mfma_f32_16x16x32_bf16 v[100:103], a[4:7], a[68:71], v[100:103]\n"
      "buffer_load_dwordx4 v247, s[40:43], s68 offen offset:0  lds\n"
      "v_mfma_f32_16x16x32_bf16 v[132:135], a[4:7], a[72:75], v[132:135]\n"
      "ds_read_b128 a[36:39], v251 offset:38912\n"
      "v_mfma_f32_16x16x32_bf16 v[164:167], a[4:7], a[76:79], v[164:167]\n"
      "s_waitcnt lgkmcnt(14)\n"
      "buffer_load_dwordx4 v248, s[40:43], s68 offen offset:1024  lds\n"
      "v_mfma_f32_16x16x32_bf16 v[8:11], a[8:11], a[56:59], v[8:11]\n"
      "ds_read_b64_tr_b16 a[4:5], v244 offset:0\n"
      "v_mfma_f32_16x16x32_bf16 v[40:43], a[8:11], a[60:63], v[40:43]\n"
      "ds_read_b64_tr_b16 a[6:7], v244 offset:1024\n"
      "v_mfma_f32_16x16x32_bf16 v[72:75], a[8:11], a[64:67], v[72:75]\n"
      "buffer_load_dwordx4 v249, s[40:43], s68 offen offset:2048  lds\n"
      "v_mfma_f32_16x16x32_bf16 v[104:107], a[8:11], a[68:71], v[104:107]\n"
      "buffer_load_dwordx4 v250, s[40:43], s68 offen offset:3072  lds\n"
      "v_mfma_f32_16x16x32_bf16 v[136:139], a[8:11], a[72:75], v[136:139]\n"
      "s_add_u32 m0, s72, 36864\n"
      "ds_read_b128 a[40:43], v251 offset:40960\n"
      "v_mfma_f32_16x16x32_bf16 v[168:171], a[8:11], a[76:79], v[168:171]\n"
      "s_waitcnt lgkmcnt(14)\n"
      "v_mfma_f32_16x16x32_bf16 v[12:15], a[12:15], a[56:59], v[12:15]\n"
      "s_add_u32 s68, s68, 131072\n"
      "ds_read_b64_tr_b16 a[8:9], v243 offset:256\n"
      "v_mfma_f32_16x16x32_bf16 v[44:47], a[12:15], a[60:63], v[44:47]\n"
      "ds_read_b64_tr_b16 a[10:11], v243 offset:1280\n"
      "v_mfma_f32_16x16x32_bf16 v[76:79], a[12:15], a[64:67], v[76:79]\n"
      "buffer_load_dwordx4 v247, s[40:43], s68 offen offset:0  lds\n"
      "v_mfma_f32_16x16x32_bf16 v[108:111], a[12:15], a[68:71], v[108:111]\n"
      "buffer_load_dwordx4 v248, s[40:43], s68 offen offset:1024  lds\n"
      "v_mfma_f32_16x16x32_bf16 v[140:143], a[12:15], a[72:75], v[140:143]\n"
      "ds_read_b128 a[44:47], v251 offset:43008\n"
      "v_mfma_f32_16x16x32_bf16 v[172:175], a[12:15], a[76:79], v[172:175]\n"
      "s_waitcnt lgkmcnt(14)\n"
      "buffer_load_dwordx4 v249, s[40:43], s68 offen offset:2048  lds\n"
      "v_mfma_f32_16x16x32_bf16 v[16:19], a[16:19], a[56:59], v[16:19]\n"
      "ds_read_b64_tr_b16 a[12:13], v244 offset:256\n"
      "v_mfma_f32_16x16x32_bf16 v[48:51], a[16:19], a[60:63], v[48:51]\n"
      "ds_read_b64_tr_b16 a[14:15], v244 offset:1280\n"
      "v_mfma_f32_16x16x32_bf16 v[80:83], a[16:19], a[64:67], v[80:83]\n"
      "buffer_load_dwordx4 v250, s[40:43], s68 offen offset:3072  lds\n"
      "s_add_u32 s68, s68, 131072\n"
      "v_mfma_f32_16x16x32_bf16 v[112:115], a[16:19], a[68:71], v[112:115]\n"
      "v_mfma_f32_16x16x32_bf16 v[144:147], a[16:19], a[72:75], v[144:147]\n"
      "s_cmp_eq_u32 s68, 5242880\n"
      "s_cselect_b32 s68, 0, s68\n"
      "ds_read_b128 a[48:51], v251 offset:45056\n"
      "v_mfma_f32_16x16x32_bf16 v[176:179], a[16:19], a[76:79], v[176:179]\n"
      "v_mfma_f32_16x16x32_bf16 v[20:23], a[20:23], a[56:59], v[20:23]\n"
      "s_add_u32 m0, s81, 114688\n"
      "ds_read_b64_tr_b16 a[16:17], v243 offset:512\n"
      "v_mfma_f32_16x16x32_bf16 v[52:55], a[20:23], a[60:63], v[52:55]\n"
      "s_add_u32 s73, s69, 0\n"
      "ds_read_b64_tr_b16 a[18:19], v243 offset:1536\n"
      "v_mfma_f32_16x16x32_bf16 v[84:87], a[20:23], a[64:67], v[84:87]\n"
      "v_mfma_f32_16x16x32_bf16 v[116:119], a[20:23], a[68:71], v[116:119]\n"
      "buffer_load_dwordx4 v252, s[36:39], s73 offen offset:0  lds\n"
      "v_mfma_f32_16x16x32_bf16 v[148:151], a[20:23], a[72:75], v[148:151]\n"
      "ds_read_b128 a[52:55], v251 offset:47104\n"
      "v_mfma_f32_16x16x32_bf16 v[180:183], a[20:23], a[76:79], v[180:183]\n"
      "s_waitcnt lgkmcnt(14)\n"
      "s_add_u32 m0, s81, 118784\n"
      "v_mfma_f32_16x16x32_bf16 v[24:27], a[24:27], a[56:59], v[24:27]\n"
      "s_add_u32 s73, s69, 40960\n"
      "ds_read_b64_tr_b16 a[20:21], v244 offset:512\n"
      "v_mfma_f32_16x16x32_bf16 v[56:59], a[24:27], a[60:63], v[56:59]\n"
      "ds_read_b64_tr_b16 a[22:23], v244 offset:1536\n"
      "v_mfma_f32_16x16x32_bf16 v[88:91], a[24:27], a[64:67], v[88:91]\n"
      "buffer_load_dwordx4 v252, s[36:39], s73 offen offset:0  lds\n"
      "v_mfma_f32_16x16x32_bf16 v[120:123], a[24:27], a[68:71], v[120:123]\n"
      "v_mfma_f32_16x16x32_bf16 v[152:155], a[24:27], a[72:75], v[152:155]\n"
      "s_add_u32 m0, s81, 122880\n"
      "v_mfma_f32_16x16x32_bf16 v[184:187], a[24:27], a[76:79], v[184:187]\n"
      "v_mfma_f32_16x16x32_bf16 v[28:31], a[28:31], a[56:59], v[28:31]\n"
      "s_add_u32 s73, s69, 81920\n"
      "ds_read_b64_tr_b16 a[24:25], v243 offset:768\n"
      "v_mfma_f32_16x16x32_bf16 v[60:63], a[28:31], a[60:63], v[60:63]\n"
      "ds_read_b64_tr_b16 a[26:27], v243 offset:1792\n"
      "v_mfma_f32_16x16x32_bf16 v[92:95], a[28:31], a[64:67], v[92:95]\n"
      "buffer_load_dwordx4 v252, s[36:39], s73 offen offset:0  lds\n"
      "v_mfma_f32_16x16x32_bf16 v[124:127], a[28:31], a[68:71], v[124:127]\n"
      "s_add_u32 s69, s69, 128\n"
      "v_mfma_f32_16x16x32_bf16 v[156:159], a[28:31], a[72:75], v[156:159]\n"
      "v_mfma_f32_16x16x32_bf16 v[188:191], a[28:31], a[76:79], v[188:191]\n"
      "s_waitcnt lgkmcnt(14)\n"
      "s_cmp_eq_u32 s69, 1280\n"
      "s_cselect_b32 s69, 0, s69\n"
      "v_mfma_f32_16x16x32_bf16 v[0:3], a[0:3], a[32:35], v[0:3]\n"
      "s_add_u32 m0, s72, 65536\n"
      "ds_read_b64_tr_b16 a[28:29], v244 offset:768\n"
      "v_mfma_f32_16x16x32_bf16 v[32:35], a[0:3], a[36:39], v[32:35]\n"
      "s_waitcnt lgkmcnt(14)\n"
      "ds_read_b64_tr_b16 a[30:31], v244 offset:1792\n"
      "v_mfma_f32_16x16x32_bf16 v[64:67], a[0:3], a[40:43], v[64:67]\n"
      "s_waitcnt lgkmcnt(12)\n"
      "buffer_load_dwordx4 v247, s[40:43], s68 offen offset:0  lds\n"
      "v_mfma_f32_16x16x32_bf16 v[96:99], a[0:3], a[44:47], v[96:99]\n"
      "s_waitcnt lgkmcnt(9)\n"
      "buffer_load_dwordx4 v248, s[40:43], s68 offen offset:1024  lds\n"
      "v_lshrrev_b32 v198, v192, v198\n"
      "v_mfma_f32_16x16x32_bf16 v[128:131], a[0:3], a[48:51], v[128:131]\n"
      "s_waitcnt lgkmcnt(6)\n"
      "ds_read_b128 a[56:59], v251 offset:37376\n"
      "v_mfma_f32_16x16x32_bf16 v[160:163], a[0:3], a[52:55], v[160:163]\n"
      "buffer_load_dwordx4 v249, s[40:43], s68 offen offset:2048  lds\n"
      "v_mfma_f32_16x16x32_bf16 v[4:7], a[4:7], a[32:35], v[4:7]\n"
      "s_waitcnt vmcnt(6)\n"
      "ds_read_b64_tr_b16 a[0:1], v243 offset:32768\n"
      "v_and_b32 v198, 1, v198\n"
      "v_mfma_f32_16x16x32_bf16 v[36:39], a[4:7], a[36:39], v[36:39]\n"
      "ds_read_b64_tr_b16 a[2:3], v243 offset:33792\n"
      "v_mfma_f32_16x16x32_bf16 v[68:71], a[4:7], a[40:43], v[68:71]\n"
      "buffer_load_dwordx4 v250, s[40:43], s68 offen offset:3072  lds\n"
      "s_add_u32 m0, s72, 69632\n"
      "s_add_u32 s68, s68, 131072\n"
      "v_mfma_f32_16x16x32_bf16 v[100:103], a[4:7], a[44:47], v[100:103]\n"
      "buffer_load_dwordx4 v247, s[40:43], s68 offen offset:0  lds\n"
      "v_cmp_eq_u32 vcc, 0, v198\n"
      "v_mfma_f32_16x16x32_bf16 v[132:135], a[4:7], a[48:51], v[132:135]\n"
      "ds_read_b128 a[60:63], v251 offset:39424\n"
      "v_mfma_f32_16x16x32_bf16 v[164:167], a[4:7], a[52:55], v[164:167]\n"
      "buffer_load_dwordx4 v248, s[40:43], s68 offen offset:1024  lds\n"
      "v_mfma_f32_16x16x32_bf16 v[8:11], a[8:11], a[32:35], v[8:11]\n"
      "s_and_b32 s89, vcc_lo, 0x1ff\n"
      "ds_read_b64_tr_b16 a[4:5], v244 offset:32768\n"
      "v_mfma_f32_16x16x32_bf16 v[40:43], a[8:11], a[36:39], v[40:43]\n"
      "ds_read_b64_tr_b16 a[6:7], v244 offset:33792\n"
      "v_mfma_f32_16x16x32_bf16 v[72:75], a[8:11], a[40:43], v[72:75]\n"
      "buffer_load_dwordx4 v249, s[40:43], s68 offen offset:2048  lds\n"
      "v_mfma_f32_16x16x32_bf16 v[104:107], a[8:11], a[44:47], v[104:107]\n"
      "s_lshl_b32 s90, %13, 2\n"
      "buffer_load_dwordx4 v250, s[40:43], s68 offen offset:3072  lds\n"
      "s_add_u32 s68, s68, 131072\n"
      "v_mfma_f32_16x16x32_bf16 v[136:139], a[8:11], a[48:51], v[136:139]\n"
      "s_cmp_eq_u32 s68, 5242880\n"
      "s_cselect_b32 s68, 0, s68\n"
      "ds_read_b128 a[64:67], v251 offset:41472\n"
      "v_mfma_f32_16x16x32_bf16 v[168:171], a[8:11], a[52:55], v[168:171]\n"
      "s_add_u32 m0, s81, 126976\n"
      "v_mfma_f32_16x16x32_bf16 v[12:15], a[12:15], a[32:35], v[12:15]\n"
      "s_add_u32 s73, s69, 0\n"
      "ds_read_b64_tr_b16 a[8:9], v243 offset:33024\n"
      "v_lshl_add_u32 v199, v192, 7, s90\n"
      "v_mfma_f32_16x16x32_bf16 v[44:47], a[12:15], a[36:39], v[44:47]\n"
      "ds_read_b64_tr_b16 a[10:11], v243 offset:34048\n"
      "v_mfma_f32_16x16x32_bf16 v[76:79], a[12:15], a[40:43], v[76:79]\n"
      "buffer_load_dwordx4 v252, s[36:39], s73 offen offset:0  lds\n"
      "v_add_u32 v196, s72, v192\n"
      "v_mfma_f32_16x16x32_bf16 v[108:111], a[12:15], a[44:47], v[108:111]\n"
      "s_add_u32 m0, s81, 131072\n"
      "s_add_u32 s73, s69, 40960\n"
      "buffer_load_dwordx4 v252, s[36:39], s73 offen offset:0  lds\n"
      "v_mfma_f32_16x16x32_bf16 v[140:143], a[12:15], a[48:51], v[140:143]\n"
      "ds_read_b128 a[68:71], v251 offset:43520\n"
      "v_mfma_f32_16x16x32_bf16 v[172:175], a[12:15], a[52:55], v[172:175]\n"
      "s_add_u32 m0, s81, 135168\n"
      "s_add_u32 s73, s69, 81920\n"
      "buffer_load_dwordx4 v252, s[36:39], s73 offen offset:0  lds\n"
      "v_mfma_f32_16x16x32_bf16 v[16:19], a[16:19], a[32:35], v[16:19]\n"
      "ds_read_b64_tr_b16 a[12:13], v244 offset:33024\n"
      "v_cmp_gt_u32 vcc, 2, v196\n"
      "v_mfma_f32_16x16x32_bf16 v[48:51], a[16:19], a[36:39], v[48:51]\n"
      "s_add_u32 s69, s69, 128\n"
      "ds_read_b64_tr_b16 a[14:15], v244 offset:34048\n"
      "v_mfma_f32_16x16x32_bf16 v[80:83], a[16:19], a[40:43], v[80:83]\n"
      "s_cmp_eq_u32 s69, 1280\n"
      "s_cselect_b32 s69, 0, s69\n"
      "s_add_u32 m0, s72, 0\n"
      "v_mfma_f32_16x16x32_bf16 v[112:115], a[16:19], a[44:47], v[112:115]\n"
      "s_waitcnt lgkmcnt(12)\n"
      "buffer_load_dwordx4 v247, s[40:43], s68 offen offset:0  lds\n"
      "v_mov_b32 v196, 0x40000000\n"
      "v_mfma_f32_16x16x32_bf16 v[144:147], a[16:19], a[48:51], v[144:147]\n"
      "ds_read_b128 a[72:75], v251 offset:45568\n"
      "v_mfma_f32_16x16x32_bf16 v[176:179], a[16:19], a[52:55], v[176:179]\n"
      "ds_read_b128 a[160:163], v201 offset:0\n"
      "v_cndmask_b32 v199, v196, v199, vcc\n"
      "v_mfma_f32_16x16x32_bf16 v[20:23], a[20:23], a[32:35], v[20:23]\n"
      "ds_read_b64_tr_b16 a[16:17], v243 offset:33280\n"
      "v_mfma_f32_16x16x32_bf16 v[52:55], a[20:23], a[36:39], v[52:55]\n"
      "ds_read_b64_tr_b16 a[18:19], v243 offset:34304\n"
      "v_mfma_f32_16x16x32_bf16 v[84:87], a[20:23], a[40:43], v[84:87]\n"
      "buffer_load_dwordx4 v248, s[40:43], s68 offen offset:1024  lds\n"
      "v_mov_b32 v196, s89\n"
      "v_mfma_f32_16x16x32_bf16 v[116:119], a[20:23], a[44:47], v[116:119]\n"
      "ds_read_b128 a[164:167], v201 offset:256\n"
      "v_mfma_f32_16x16x32_bf16 v[148:151], a[20:23], a[48:51], v[148:151]\n"
      "ds_read_b128 a[76:79], v251 offset:47616\n"
      "v_mfma_f32_16x16x32_bf16 v[180:183], a[20:23], a[52:55], v[180:183]\n"
      "buffer_load_dwordx4 v249, s[40:43], s68 offen offset:2048  lds\n"
      "v_cmp_eq_u32 vcc, 0, v192\n"
      "v_mfma_f32_16x16x32_bf16 v[24:27], a[24:27], a[32:35], v[24:27]\n"
      "ds_read_b64_tr_b16 a[20:21], v244 offset:33280\n"
      "v_mfma_f32_16x16x32_bf16 v[56:59], a[24:27], a[36:39], v[56:59]\n"
      "ds_read_b64_tr_b16 a[22:23], v244 offset:34304\n"
      "v_mfma_f32_16x16x32_bf16 v[88:91], a[24:27], a[40:43], v[88:91]\n"
      "ds_read_b128 a[168:171], v201 offset:512\n"
      "v_mov_b32 v197, 0\n"
      "v_mfma_f32_16x16x32_bf16 v[120:123], a[24:27], a[44:47], v[120:123]\n"
      "buffer_load_dwordx4 v250, s[40:43], s68 offen offset:3072  lds\n"
      "s_add_u32 m0, s72, 4096\n"
      "s_add_u32 s68, s68, 131072\n"
      "v_mfma_f32_16x16x32_bf16 v[152:155], a[24:27], a[48:51], v[152:155]\n"
      "buffer_load_dwordx4 v247, s[40:43], s68 offen offset:0  lds\n"
      "v_mfma_f32_16x16x32_bf16 v[184:187], a[24:27], a[52:55], v[184:187]\n"
      "ds_read_b128 a[172:175], v201 offset:768\n"
      "v_cndmask_b32 v198, v197, v196, vcc\n"
      "v_mfma_f32_16x16x32_bf16 v[28:31], a[28:31], a[32:35], v[28:31]\n"
      "ds_read_b64_tr_b16 a[24:25], v243 offset:33536\n"
      "v_mfma_f32_16x16x32_bf16 v[60:63], a[28:31], a[36:39], v[60:63]\n"
      "ds_read_b64_tr_b16 a[26:27], v243 offset:34560\n"
      "v_mfma_f32_16x16x32_bf16 v[92:95], a[28:31], a[40:43], v[92:95]\n"
      "buffer_load_dwordx4 v248, s[40:43], s68 offen offset:1024  lds\n"
      "buffer_store_dword v198, v199, s[64:67], 0 offen offset:0 sc1\n"
      "v_mfma_f32_16x16x32_bf16 v[124:127], a[28:31], a[44:47], v[124:127]\n"
      "buffer_load_dwordx4 v249, s[40:43], s68 offen offset:2048  lds\n"
      "v_mfma_f32_16x16x32_bf16 v[156:159], a[28:31], a[48:51], v[156:159]\n"
      "buffer_load_dwordx4 v250, s[40:43], s68 offen offset:3072  lds\n"
      "s_add_u32 s68, s68, 131072\n"
      "v_mfma_f32_16x16x32_bf16 v[188:191], a[28:31], a[52:55], v[188:191]\n"
      "s_waitcnt lgkmcnt(14)\n"
      "s_cmp_eq_u32 s68, 5242880\n"
      "s_cselect_b32 s68, 0, s68\n"
      "v_mfma_f32_16x16x32_bf16 v[0:3], a[0:3], a[56:59], v[0:3]\n"
      "s_waitcnt vmcnt(20)\n"
      "s_barrier\n"
      "ds_read_b64_tr_b16 a[28:29], v244 offset:33536\n"
      "v_mfma_f32_16x16x32_bf16 v[32:35], a[0:3], a[60:63], v[32:35]\n"
      "s_add_u32 m0, s81, 139264\n"
      "s_add_u32 s73, s69, 0\n"
      "ds_read_b64_tr_b16 a[30:31], v244 offset:34560\n"
      "v_mfma_f32_16x16x32_bf16 v[64:67], a[0:3], a[64:67], v[64:67]\n"
      "buffer_load_dwordx4 v252, s[36:39], s73 offen offset:0  lds\n"
      "v_mfma_f32_16x16x32_bf16 v[96:99], a[0:3], a[68:71], v[96:99]\n"
      "s_waitcnt lgkmcnt(13)\n"
      "s_add_u32 m0, s81, 143360\n"
      "v_mfma_f32_16x16x32_bf16 v[128:131], a[0:3], a[72:75], v[128:131]\n"
      "s_waitcnt lgkmcnt(8)\n"
      "s_add_u32 s73, s69, 40960\n"
      "ds_read_b128 a[32:35], v251 offset:0\n"
      "v_mfma_f32_16x16x32_bf16 v[160:163], a[0:3], a[76:79], v[160:163]\n"
      "buffer_load_dwordx4 v252, s[36:39], s73 offen offset:0  lds\n"
      "v_mfma_f32_16x16x32_bf16 v[4:7], a[4:7], a[56:59], v[4:7]\n"
      "s_waitcnt vmcnt(14)\n"
      "s_add_u32 m0, s81, 147456\n"
      "ds_read_b64_tr_b16 a[0:1], v245 offset:0\n"
      "v_mfma_f32_16x16x32_bf16 v[36:39], a[4:7], a[60:63], v[36:39]\n"
      "s_add_u32 s73, s69, 81920\n"
      "ds_read_b64_tr_b16 a[2:3], v245 offset:1024\n"
      "v_mfma_f32_16x16x32_bf16 v[68:71], a[4:7], a[64:67], v[68:71]\n"
      "buffer_load_dwordx4 v252, s[36:39], s73 offen offset:0  lds\n"
      "v_mfma_f32_16x16x32_bf16 v[100:103], a[4:7], a[68:71], v[100:103]\n"
      "s_add_u32 s69, s69, 128\n"
      "s_cmp_eq_u32 s69, 1280\n"
      "s_cselect_b32 s69, 0, s69\n"
      "v_mfma_f32_16x16x32_bf16 v[132:135], a[4:7], a[72:75], v[132:135]\n"
      "s_mov_b32 m0, s78\n"
      "ds_read_b128 a[36:39], v251 offset:2048\n"
      "v_mfma_f32_16x16x32_bf16 v[164:167], a[4:7], a[76:79], v[164:167]\n"
      "s_waitcnt lgkmcnt(8)\n"
      "buffer_load_dwordx4 v234, s[44:47], s70 offen offset:0 nt lds\n"
      "v_mfma_f32_16x16x32_bf16 v[8:11], a[8:11], a[56:59], v[8:11]\n"
      "ds_read_b64_tr_b16 a[4:5], v246 offset:0\n"
      "v_mfma_f32_16x16x32_bf16 v[40:43], a[8:11], a[60:63], v[40:43]\n"
      "ds_read_b64_tr_b16 a[6:7], v246 offset:1024\n"
      "v_mfma_f32_16x16x32_bf16 v[72:75], a[8:11], a[64:67], v[72:75]\n"
      "buffer_load_dwordx4 v234, s[44:47], s70 offen offset:1024 nt lds\n"
      "v_mfma_f32_16x16x32_bf16 v[104:107], a[8:11], a[68:71], v[104:107]\n"
      "buffer_load_dwordx4 v235, s[44:47], s70 offen offset:2048 nt lds\n"
      "v_mfma_f32_16x16x32_bf16 v[136:139], a[8:11], a[72:75], v[136:139]\n"
      "ds_read_b128 a[40:43], v251 offset:4096\n"
      "v_mfma_f32_16x16x32_bf16 v[168:171], a[8:11], a[76:79], v[168:171]\n"
      "buffer_load_dwordx4 v235, s[44:47], s70 offen offset:3072 nt lds\n"
      "s_add_u32 s70, s70, %16\n"
      "s_and_b32 s70, s70, 0x1ffff\n"
      "v_mfma_f32_16x16x32_bf16 v[12:15], a[12:15], a[56:59], v[12:15]\n"
      "ds_read_b64_tr_b16 a[8:9], v245 offset:256\n"
      "v_mfma_f32_16x16x32_bf16 v[44:47], a[12:15], a[60:63], v[44:47]\n"
      "s_add_u32 m0, s72, 32768\n"
      "ds_read_b64_tr_b16 a[10:11], v245 offset:1280\n"
      "v_mfma_f32_16x16x32_bf16 v[76:79], a[12:15], a[64:67], v[76:79]\n"
      "v_mfma_f32_16x16x32_bf16 v[108:111], a[12:15], a[68:71], v[108:111]\n"
      "s_waitcnt lgkmcnt(9)\n"
      "buffer_load_dwordx4 v247, s[40:43], s68 offen offset:0  lds\n"
      "v_mfma_f32_16x16x32_bf16 v[140:143], a[12:15], a[72:75], v[140:143]\n"
      "ds_read_b128 a[44:47], v251 offset:6144\n"
      "v_mfma_f32_16x16x32_bf16 v[172:175], a[12:15], a[76:79], v[172:175]\n"
      "buffer_load_dwordx4 v248, s[40:43], s68 offen offset:1024  lds\n"
      "v_mfma_f32_16x16x32_bf16 v[16:19], a[16:19], a[56:59], v[16:19]\n"
      "ds_read_b64_tr_b16 a[12:13], v246 offset:256\n"
      "v_mfma_f32_16x16x32_bf16 v[48:51], a[16:19], a[60:63], v[48:51]\n"
      "ds_read_b64_tr_b16 a[14:15], v246 offset:1280\n"
      "v_mfma_f32_16x16x32_bf16 v[80:83], a[16:19], a[64:67], v[80:83]\n"
      "buffer_load_dwordx4 v249, s[40:43], s68 offen offset:2048  lds\n"
      "v_mfma_f32_16x16x32_bf16 v[112:115], a[16:19], a[68:71], v[112:115]\n"
      "buffer_load_dwordx4 v250, s[40:43], s68 offen offset:3072  lds\n"
      "v_mfma_f32_16x16x32_bf16 v[144:147], a[16:19], a[72:75], v[144:147]\n"
      "ds_read_b128 a[48:51], v251 offset:8192\n"
      "v_mfma_f32_16x16x32_bf16 v[176:179], a[16:19], a[76:79], v[176:179]\n"
      "v_mfma_f32_16x16x32_bf16 v[20:23], a[20:23], a[56:59], v[20:23]\n"
      "s_add_u32 m0, s72, 36864\n"
      "ds_read_b64_tr_b16 a[16:17], v245 offset:512\n"
      "v_mfma_f32_16x16x32_bf16 v[52:55], a[20:23], a[60:63], v[52:55]\n"
      "ds_read_b64_tr_b16 a[18:19], v245 offset:1536\n"
      "v_mfma_f32_16x16x32_bf16 v[84:87], a[20:23], a[64:67], v[84:87]\n"
      "s_add_u32 s68, s68, 131072\n"
      "v_mfma_f32_16x16x32_bf16 v[116:119], a[20:23], a[68:71], v[116:119]\n"
      "v_mfma_f32_16x16x32_bf16 v[148:151], a[20:23], a[72:75], v[148:151]\n"
      "ds_read_b128 a[52:55], v251 offset:10240\n"
      "v_mfma_f32_16x16x32_bf16 v[180:183], a[20:23], a[76:79], v[180:183]\n"
      "buffer_load_dwordx4 v247, s[40:43], s68 offen offset:0  lds\n"
      "v_mfma_f32_16x16x32_bf16 v[24:27], a[24:27], a[56:59], v[24:27]\n"
      "ds_read_b64_tr_b16 a[20:21], v246 offset:512\n"
      "v_mfma_f32_16x16x32_bf16 v[56:59], a[24:27], a[60:63], v[56:59]\n"
      "ds_read_b64_tr_b16 a[22:23], v246 offset:1536\n"
      "v_mfma_f32_16x16x32_bf16 v[88:91], a[24:27], a[64:67], v[88:91]\n"
      "buffer_load_dwordx4 v248, s[40:43], s68 offen offset:1024  lds\n"
      "v_mfma_f32_16x16x32_bf16 v[120:123], a[24:27], a[68:71], v[120:123]\n"
      "buffer_load_dwordx4 v249, s[40:43], s68 offen offset:2048  lds\n"
      "v_mfma_f32_16x16x32_bf16 v[152:155], a[24:27], a[72:75], v[152:155]\n"
      "v_mfma_f32_16x16x32_bf16 v[184:187], a[24:27], a[76:79], v[184:187]\n"
      "buffer_load_dwordx4 v250, s[40:43], s68 offen offset:3072  lds\n"
      "v_mfma_f32_16x16x32_bf16 v[28:31], a[28:31], a[56:59], v[28:31]\n"
      "ds_read_b64_tr_b16 a[24:25], v245 offset:768\n"
      "v_mfma_f32_16x16x32_bf16 v[60:63], a[28:31], a[60:63], v[60:63]\n"
      "ds_read_b64_tr_b16 a[26:27], v245 offset:1792\n"
      "v_mfma_f32_16x16x32_bf16 v[92:95], a[28:31], a[64:67], v[92:95]\n"
      "s_add_u32 s68, s68, 131072\n"
      "v_mfma_f32_16x16x32_bf16 v[124:127], a[28:31], a[68:71], v[124:127]\n"
      "v_mfma_f32_16x16x32_bf16 v[156:159], a[28:31], a[72:75], v[156:159]\n"
      "s_cmp_eq_u32 s68, 5242880\n"
      "s_cselect_b32 s68, 0, s68\n"
      "v_mfma_f32_16x16x32_bf16 v[188:191], a[28:31], a[76:79], v[188:191]\n"
      "s_waitcnt lgkmcnt(14)\n"
      "v_mfma_f32_16x16x32_bf16 v[0:3], a[0:3], a[32:35], v[0:3]\n"
      "ds_read_b64_tr_b16 a[28:29], v246 offset:768\n"
      "v_mfma_f32_16x16x32_bf16 v[32:35], a[0:3], a[36:39], v[32:35]\n"
      "s_waitcnt lgkmcnt(14)\n"
      "ds_read_b64_tr_b16 a[30:31], v246 offset:1792\n"
      "v_mfma_f32_16x16x32_bf16 v[64:67], a[0:3], a[40:43], v[64:67]\n"
      "s_waitcnt lgkmcnt(12)\n"
      "v_mfma_f32_16x16x32_bf16 v[96:99], a[0:3], a[44:47], v[96:99]\n"
      "s_waitcnt lgkmcnt(9)\n"
      "v_mfma_f32_16x16x32_bf16 v[128:131], a[0:3], a[48:51], v[128:131]\n"
      "s_waitcnt lgkmcnt(6)\n"
      "ds_read_b128 a[56:59], v251 offset:512\n"
      "v_mfma_f32_16x16x32_bf16 v[160:163], a[0:3], a[52:55], v[160:163]\n"
      "v_mfma_f32_16x16x32_bf16 v[4:7], a[4:7], a[32:35], v[4:7]\n"
      "s_waitcnt vmcnt(15)\n"
      "ds_read_b64_tr_b16 a[0:1], v243 offset:0\n"
      "v_mfma_f32_16x16x32_bf16 v[36:39], a[4:7], a[36:39], v[36:39]\n"
      "ds_read_b64_tr_b16 a[2:3], v243 offset:1024\n"
      "v_mfma_f32_16x16x32_bf16 v[68:71], a[4:7], a[40:43], v[68:71]\n"
      "v_mfma_f32_16x16x32_bf16 v[100:103], a[4:7], a[44:47], v[100:103]\n"
      "s_add_u32 m0, s72, 65536\n"
      "v_mfma_f32_16x16x32_bf16 v[132:135], a[4:7], a[48:51], v[132:135]\n"
      "ds_read_b128 a[60:63], v251 offset:2560\n"
      "v_mfma_f32_16x16x32_bf16 v[164:167], a[4:7], a[52:55], v[164:167]\n"
      "s_waitcnt lgkmcnt(4)\n"
      "buffer_load_dwordx4 v247, s[40:43], s68 offen offset:0  lds\n"
      "v_mfma_f32_16x16x32_bf16 v[8:11], a[8:11], a[32:35], v[8:11]\n"
      "ds_read_b64_tr_b16 a[4:5], v244 offset:0\n"
      "v_mfma_f32_16x16x32_bf16 v[40:43], a[8:11], a[36:39], v[40:43]\n"
      "ds_read_b64_tr_b16 a[6:7], v244 offset:1024\n"
      "v_mfma_f32_16x16x32_bf16 v[72:75], a[8:11], a[40:43], v[72:75]\n"
      "v_mfma_f32_16x16x32_bf16 v[104:107], a[8:11], a[44:47], v[104:107]\n"
      "buffer_load_dwordx4 v248, s[40:43], s68 offen offset:1024  lds\n"
      "v_mfma_f32_16x16x32_bf16 v[136:139], a[8:11], a[48:51], v[136:139]\n"
      "ds_read_b128 a[64:67], v251 offset:4608\n"
      "v_mfma_f32_16x16x32_bf16 v[168:171], a[8:11], a[52:55], v[168:171]\n"
      "buffer_load_dwordx4 v249, s[40:43], s68 offen offset:2048  lds\n"
      "v_mfma_f32_16x16x32_bf16 v[12:15], a[12:15], a[32:35], v[12:15]\n"
      "ds_read_b64_tr_b16 a[8:9], v243 offset:256\n"
      "v_mfma_f32_16x16x32_bf16 v[44:47], a[12:15], a[36:39], v[44:47]\n"
      "ds_read_b64_tr_b16 a[10:11], v243 offset:1280\n"
      "v_mfma_f32_16x16x32_bf16 v[76:79], a[12:15], a[40:43], v[76:79]\n"
      "v_mfma_f32_16x16x32_bf16 v[108:111], a[12:15], a[44:47], v[108:111]\n"
      "buffer_load_dwordx4 v250, s[40:43], s68 offen offset:3072  lds\n"
      "v_mfma_f32_16x16x32_bf16 v[140:143], a[12:15], a[48:51], v[140:143]\n"
      "ds_read_b128 a[68:71], v251 offset:6656\n"
      "v_mfma_f32_16x16x32_bf16 v[172:175], a[12:15], a[52:55], v[172:175]\n"
      "v_mfma_f32_16x16x32_bf16 v[16:19], a[16:19], a[32:35], v[16:19]\n"
      "s_add_u32 m0, s72, 69632\n"
      "ds_read_b64_tr_b16 a[12:13], v244 offset:256\n"
      "v_mfma_f32_16x16x32_bf16 v[48:51], a[16:19], a[36:39], v[48:51]\n"
      "ds_read_b64_tr_b16 a[14:15], v244 offset:1280\n"
      "v_mfma_f32_16x16x32_bf16 v[80:83], a[16:19], a[40:43], v[80:83]\n"
      "v_mfma_f32_16x16x32_bf16 v[112:115], a[16:19], a[44:47], v[112:115]\n"
      "s_add_u32 s68, s68, 131072\n"
      "v_mfma_f32_16x16x32_bf16 v[144:147], a[16:19], a[48:51], v[144:147]\n"
      "ds_read_b128 a[72:75], v251 offset:8704\n"
      "v_mfma_f32_16x16x32_bf16 v[176:179], a[16:19], a[52:55], v[176:179]\n"
      "s_waitcnt vmcnt(12)\n"
      "ds_read_b128 a[176:179], v201 offset:0\n"
      "v_mfma_f32_16x16x32_bf16 v[20:23], a[20:23], a[32:35], v[20:23]\n"
      "ds_read_b64_tr_b16 a[16:17], v243 offset:512\n"
      "v_mfma_f32_16x16x32_bf16 v[52:55], a[20:23], a[36:39], v[52:55]\n"
      "ds_read_b64_tr_b16 a[18:19], v243 offset:1536\n"
      "v_mfma_f32_16x16x32_bf16 v[84:87], a[20:23], a[40:43], v[84:87]\n"
      "buffer_load_dwordx4 v247, s[40:43], s68 offen offset:0  lds\n"
      "v_mfma_f32_16x16x32_bf16 v[116:119], a[20:23], a[44:47], v[116:119]\n"
      "ds_read_b128 a[180:183], v201 offset:256\n"
      "v_mfma_f32_16x16x32_bf16 v[148:151], a[20:23], a[48:51], v[148:151]\n"
      "ds_read_b128 a[76:79], v251 offset:10752\n"
      "v_mfma_f32_16x16x32_bf16 v[180:183], a[20:23], a[52:55], v[180:183]\n"
      "buffer_load_dwordx4 v248, s[40:43], s68 offen offset:1024  lds\n"
      "v_mfma_f32_16x16x32_bf16 v[24:27], a[24:27], a[32:35], v[24:27]\n"
      "ds_read_b64_tr_b16 a[20:21], v244 offset:512\n"
      "v_mfma_f32_16x16x32_bf16 v[56:59], a[24:27], a[36:39], v[56:59]\n"
      "ds_read_b64_tr_b16 a[22:23], v244 offset:1536\n"
      "v_mfma_f32_16x16x32_bf16 v[88:91], a[24:27], a[40:43], v[88:91]\n"
      "ds_read_b128 a[184:187], v201 offset:512\n"
      "v_mfma_f32_16x16x32_bf16 v[120:123], a[24:27], a[44:47], v[120:123]\n"
      "buffer_load_dwordx4 v249, s[40:43], s68 offen offset:2048  lds\n"
      "v_mfma_f32_16x16x32_bf16 v[152:155], a[24:27], a[48:51], v[152:155]\n"
      "buffer_load_dwordx4 v250, s[40:43], s68 offen offset:3072  lds\n"
      "v_mfma_f32_16x16x32_bf16 v[184:187], a[24:27], a[52:55], v[184:187]\n"
      "ds_read_b128 a[188:191], v201 offset:768\n"
      "v_mfma_f32_16x16x32_bf16 v[28:31], a[28:31], a[32:35], v[28:31]\n"
      "ds_read_b64_tr_b16 a[24:25], v243 offset:768\n"
      "v_mfma_f32_16x16x32_bf16 v[60:63], a[28:31], a[36:39], v[60:63]\n"
      "s_add_u32 s68, s68, 131072\n"
      "ds_read_b64_tr_b16 a[26:27], v243 offset:1792\n"
      "v_mfma_f32_16x16x32_bf16 v[92:95], a[28:31], a[40:43], v[92:95]\n"
      "v_mfma_f32_16x16x32_bf16 v[124:127], a[28:31], a[44:47], v[124:127]\n"
      "v_mfma_f32_16x16x32_bf16 v[156:159], a[28:31], a[48:51], v[156:159]\n"
      "s_cmp_eq_u32 s68, 5242880\n"
      "s_cselect_b32 s68, 0, s68\n"
      "v_mfma_f32_16x16x32_bf16 v[188:191], a[28:31], a[52:55], v[188:191]\n"
      "s_waitcnt lgkmcnt(14)\n"
      "v_mfma_f32_16x16x32_bf16 v[0:3], a[0:3], a[56:59], v[0:3]\n"
      "s_barrier\n"
      "ds_read_b64_tr_b16 a[28:29], v244 offset:768\n"
      "v_mfma_f32_16x16x32_bf16 v[32:35], a[0:3], a[60:63], v[32:35]\n"
      "s_add_u32 m0, s81, 151552\n"
      "s_add_u32 s73, s69, 0\n"
      "ds_read_b64_tr_b16 a[30:31], v244 offset:1792\n"
      "v_mfma_f32_16x16x32_bf16 v[64:67], a[0:3], a[64:67], v[64:67]\n"
      "buffer_load_dwordx4 v252, s[36:39], s73 offen offset:0  lds\n"
      "v_mfma_f32_16x16x32_bf16 v[96:99], a[0:3], a[68:71], v[96:99]\n"
      "s_waitcnt lgkmcnt(13)\n"
      "s_add_u32 m0, s81, 155648\n"
      "v_mfma_f32_16x16x32_bf16 v[128:131], a[0:3], a[72:75], v[128:131]\n"
      "s_waitcnt lgkmcnt(8)\n"
      "s_add_u32 s73, s69, 40960\n"
      "ds_read_b128 a[32:35], v251 offset:12288\n"
      "v_mfma_f32_16x16x32_bf16 v[160:163], a[0:3], a[76:79], v[160:163]\n"
      "buffer_load_dwordx4 v252, s[36:39], s73 offen offset:0  lds\n"
      "v_mfma_f32_16x16x32_bf16 v[4:7], a[4:7], a[56:59], v[4:7]\n"
      "s_waitcnt vmcnt(10)\n"
      "s_add_u32 m0, s81, 159744\n"
      "ds_read_b64_tr_b16 a[0:1], v243 offset:32768\n"
      "v_mfma_f32_16x16x32_bf16 v[36:39], a[4:7], a[60:63], v[36:39]\n"
      "s_add_u32 s73, s69, 81920\n"
      "ds_read_b64_tr_b16 a[2:3], v243 offset:33792\n"
      "v_mfma_f32_16x16x32_bf16 v[68:71], a[4:7], a[64:67], v[68:71]\n"
      "buffer_load_dwordx4 v252, s[36:39], s73 offen offset:0  lds\n"
      "v_mfma_f32_16x16x32_bf16 v[100:103], a[4:7], a[68:71], v[100:103]\n"
      "s_add_u32 s69, s69, 128\n"
      "s_cmp_eq_u32 s69, 1280\n"
      "s_cselect_b32 s69, 0, s69\n"
      "v_mfma_f32_16x16x32_bf16 v[132:135], a[4:7], a[72:75], v[132:135]\n"
      "s_mov_b32 m0, s78\n"
      "ds_read_b128 a[36:39], v251 offset:14336\n"
      "v_mfma_f32_16x16x32_bf16 v[164:167], a[4:7], a[76:79], v[164:167]\n"
      "s_waitcnt lgkmcnt(8)\n"
      "buffer_load_dwordx4 v234, s[48:51], s71 offen offset:0 nt lds\n"
      "v_mfma_f32_16x16x32_bf16 v[8:11], a[8:11], a[56:59], v[8:11]\n"
      "ds_read_b64_tr_b16 a[4:5], v244 offset:32768\n"
      "v_mfma_f32_16x16x32_bf16 v[40:43], a[8:11], a[60:63], v[40:43]\n"
      "ds_read_b64_tr_b16 a[6:7], v244 offset:33792\n"
      "v_mfma_f32_16x16x32_bf16 v[72:75], a[8:11], a[64:67], v[72:75]\n"
      "buffer_load_dwordx4 v236, s[48:51], s71 offen offset:1024 nt lds\n"
      "v_mfma_f32_16x16x32_bf16 v[104:107], a[8:11], a[68:71], v[104:107]\n"
      "buffer_load_dwordx4 v234, s[48:51], s71 offen offset:2048 nt lds\n"
      "v_mfma_f32_16x16x32_bf16 v[136:139], a[8:11], a[72:75], v[136:139]\n"
      "ds_read_b128 a[40:43], v251 offset:16384\n"
      "v_mfma_f32_16x16x32_bf16 v[168:171], a[8:11], a[76:79], v[168:171]\n"
      "buffer_load_dwordx4 v236, s[48:51], s71 offen offset:3072 nt lds\n"
      "s_add_u32 s71, s71, %16\n"
      "s_and_b32 s71, s71, 0x1ffff\n"
      "v_mfma_f32_16x16x32_bf16 v[12:15], a[12:15], a[56:59], v[12:15]\n"
      "ds_read_b64_tr_b16 a[8:9], v243 offset:33024\n"
      "v_mfma_f32_16x16x32_bf16 v[44:47], a[12:15], a[60:63], v[44:47]\n"
      "s_add_u32 m0, s72, 0\n"
      "ds_read_b64_tr_b16 a[10:11], v243 offset:34048\n"
      "v_mfma_f32_16x16x32_bf16 v[76:79], a[12:15], a[64:67], v[76:79]\n"
      "v_mfma_f32_16x16x32_bf16 v[108:111], a[12:15], a[68:71], v[108:111]\n"
      "s_waitcnt lgkmcnt(9)\n"
      "buffer_load_dwordx4 v247, s[40:43], s68 offen offset:0  lds\n"
      "v_mfma_f32_16x16x32_bf16 v[140:143], a[12:15], a[72:75], v[140:143]\n"
      "ds_read_b128 a[44:47], v251 offset:18432\n"
      "v_mfma_f32_16x16x32_bf16 v[172:175], a[12:15], a[76:79], v[172:175]\n"
      "buffer_load_dwordx4 v248, s[40:43], s68 offen offset:1024  lds\n"
      "v_mfma_f32_16x16x32_bf16 v[16:19], a[16:19], a[56:59], v[16:19]\n"
      "ds_read_b64_tr_b16 a[12:13], v244 offset:33024\n"
      "v_mfma_f32_16x16x32_bf16 v[48:51], a[16:19], a[60:63], v[48:51]\n"
      "ds_read_b64_tr_b16 a[14:15], v244 offset:34048\n"
      "v_mfma_f32_16x16x32_bf16 v[80:83], a[16:19], a[64:67], v[80:83]\n"
      "buffer_load_dwordx4 v249, s[40:43], s68 offen offset:2048  lds\n"
      "v_mfma_f32_16x16x32_bf16 v[112:115], a[16:19], a[68:71], v[112:115]\n"
      "buffer_load_dwordx4 v250, s[40:43], s68 offen offset:3072  lds\n"
      "v_mfma_f32_16x16x32_bf16 v[144:147], a[16:19], a[72:75], v[144:147]\n"
      "ds_read_b128 a[48:51], v251 offset:20480\n"
      "v_mfma_f32_16x16x32_bf16 v[176:179], a[16:19], a[76:79], v[176:179]\n"
      "v_mfma_f32_16x16x32_bf16 v[20:23], a[20:23], a[56:59], v[20:23]\n"
      "s_add_u32 m0, s72, 4096\n"
      "ds_read_b64_tr_b16 a[16:17], v243 offset:33280\n"
      "v_mfma_f32_16x16x32_bf16 v[52:55], a[20:23], a[60:63], v[52:55]\n"
      "ds_read_b64_tr_b16 a[18:19], v243 offset:34304\n"
      "v_mfma_f32_16x16x32_bf16 v[84:87], a[20:23], a[64:67], v[84:87]\n"
      "s_add_u32 s68, s68, 131072\n"
      "v_mfma_f32_16x16x32_bf16 v[116:119], a[20:23], a[68:71], v[116:119]\n"
      "v_mfma_f32_16x16x32_bf16 v[148:151], a[20:23], a[72:75], v[148:151]\n"
      "ds_read_b128 a[52:55], v251 offset:22528\n"
      "v_mfma_f32_16x16x32_bf16 v[180:183], a[20:23], a[76:79], v[180:183]\n"
      "buffer_load_dwordx4 v247, s[40:43], s68 offen offset:0  lds\n"
      "v_mfma_f32_16x16x32_bf16 v[24:27], a[24:27], a[56:59], v[24:27]\n"
      "ds_read_b64_tr_b16 a[20:21], v244 offset:33280\n"
      "v_mfma_f32_16x16x32_bf16 v[56:59], a[24:27], a[60:63], v[56:59]\n"
      "ds_read_b64_tr_b16 a[22:23], v244 offset:34304\n"
      "v_mfma_f32_16x16x32_bf16 v[88:91], a[24:27], a[64:67], v[88:91]\n"
      "buffer_load_dwordx4 v248, s[40:43], s68 offen offset:1024  lds\n"
      "v_mfma_f32_16x16x32_bf16 v[120:123], a[24:27], a[68:71], v[120:123]\n"
      "buffer_load_dwordx4 v249, s[40:43], s68 offen offset:2048  lds\n"
      "v_mfma_f32_16x16x32_bf16 v[152:155], a[24:27], a[72:75], v[152:155]\n"
      "v_mfma_f32_16x16x32_bf16 v[184:187], a[24:27], a[76:79], v[184:187]\n"
      "buffer_load_dwordx4 v250, s[40:43], s68 offen offset:3072  lds\n"
      "v_mfma_f32_16x16x32_bf16 v[28:31], a[28:31], a[56:59], v[28:31]\n"
      "ds_read_b64_tr_b16 a[24:25], v243 offset:33536\n"
      "v_mfma_f32_16x16x32_bf16 v[60:63], a[28:31], a[60:63], v[60:63]\n"
      "ds_read_b64_tr_b16 a[26:27], v243 offset:34560\n"
      "v_mfma_f32_16x16x32_bf16 v[92:95], a[28:31], a[64:67], v[92:95]\n"
      "s_add_u32 s68, s68, 131072\n"
      "v_mfma_f32_16x16x32_bf16 v[124:127], a[28:31], a[68:71], v[124:127]\n"
      "v_mfma_f32_16x16x32_bf16 v[156:159], a[28:31], a[72:75], v[156:159]\n"
      "s_cmp_eq_u32 s68, 5242880\n"
      "s_cselect_b32 s68, 0, s68\n"
      "v_mfma_f32_16x16x32_bf16 v[188:191], a[28:31], a[76:79], v[188:191]\n"
      "s_waitcnt lgkmcnt(14)\n"
      "v_mfma_f32_16x16x32_bf16 v[0:3], a[0:3], a[32:35], v[0:3]\n"
      "ds_read_b64_tr_b16 a[28:29], v244 offset:33536\n"
      "v_mfma_f32_16x16x32_bf16 v[32:35], a[0:3], a[36:39], v[32:35]\n"
      "s_waitcnt lgkmcnt(14)\n"
      "ds_read_b64_tr_b16 a[30:31], v244 offset:34560\n"
      "v_mfma_f32_16x16x32_bf16 v[64:67], a[0:3], a[40:43], v[64:67]\n"
      "s_waitcnt lgkmcnt(12)\n"
      "v_mfma_f32_16x16x32_bf16 v[96:99], a[0:3], a[44:47], v[96:99]\n"
      "s_waitcnt lgkmcnt(9)\n"
      "v_mfma_f32_16x16x32_bf16 v[128:131], a[0:3], a[48:51], v[128:131]\n"
      "s_waitcnt lgkmcnt(6)\n"
      "ds_read_b128 a[56:59], v251 offset:12800\n"
      "v_mfma_f32_16x16x32_bf16 v[160:163], a[0:3], a[52:55], v[160:163]\n"
      "v_mfma_f32_16x16x32_bf16 v[4:7], a[4:7], a[32:35], v[4:7]\n"
      "s_waitcnt vmcnt(15)\n"
      "ds_read_b64_tr_b16 a[0:1], v245 offset:0\n"
      "v_mfma_f32_16x16x32_bf16 v[36:39], a[4:7], a[36:39], v[36:39]\n"
      "ds_read_b64_tr_b16 a[2:3], v245 offset:1024\n"
      "v_mfma_f32_16x16x32_bf16 v[68:71], a[4:7], a[40:43], v[68:71]\n"
      "v_mfma_f32_16x16x32_bf16 v[100:103], a[4:7], a[44:47], v[100:103]\n"
      "s_add_u32 m0, s72, 32768\n"
      "v_and_b32 v199, 7, v192\n"
      "v_mfma_f32_16x16x32_bf16 v[132:135], a[4:7], a[48:51], v[132:135]\n"
      "ds_read_b128 a[60:63], v251 offset:14848\n"
      "v_mfma_f32_16x16x32_bf16 v[164:167], a[4:7], a[52:55], v[164:167]\n"
      "s_waitcnt lgkmcnt(4)\n"
      "buffer_load_dwordx4 v247, s[40:43], s68 offen offset:0  lds\n"
      "v_mfma_f32_16x16x32_bf16 v[8:11], a[8:11], a[32:35], v[8:11]\n"
      "ds_read_b64_tr_b16 a[4:5], v246 offset:0\n"
      "v_mfma_f32_16x16x32_bf16 v[40:43], a[8:11], a[36:39], v[40:43]\n"
      "ds_read_b64_tr_b16 a[6:7], v246 offset:1024\n"
      "v_mfma_f32_16x16x32_bf16 v[72:75], a[8:11], a[40:43], v[72:75]\n"
      "v_mfma_f32_16x16x32_bf16 v[104:107], a[8:11], a[44:47], v[104:107]\n"
      "buffer_load_dwordx4 v248, s[40:43], s68 offen offset:1024  lds\n"
      "v_mfma_f32_16x16x32_bf16 v[136:139], a[8:11], a[48:51], v[136:139]\n"
      "ds_read_b128 a[64:67], v251 offset:16896\n"
      "v_mfma_f32_16x16x32_bf16 v[168:171], a[8:11], a[52:55], v[168:171]\n"
      "buffer_load_dwordx4 v249, s[40:43], s68 offen offset:2048  lds\n"
      "v_mfma_f32_16x16x32_bf16 v[12:15], a[12:15], a[32:35], v[12:15]\n"
      "ds_read_b64_tr_b16 a[8:9], v245 offset:256\n"
      "v_mfma_f32_16x16x32_bf16 v[44:47], a[12:15], a[36:39], v[44:47]\n"
      "ds_read_b64_tr_b16 a[10:11], v245 offset:1280\n"
      "v_mfma_f32_16x16x32_bf16 v[76:79], a[12:15], a[40:43], v[76:79]\n"
      "v_mfma_f32_16x16x32_bf16 v[108:111], a[12:15], a[44:47], v[108:111]\n"
      "buffer_load_dwordx4 v250, s[40:43], s68 offen offset:3072  lds\n"
      "v_mfma_f32_16x16x32_bf16 v[140:143], a[12:15], a[48:51], v[140:143]\n"
      "ds_read_b128 a[68:71], v251 offset:18944\n"
      "v_lshlrev_b32 v199, 2, v199\n"
      "v_mfma_f32_16x16x32_bf16 v[172:175], a[12:15], a[52:55], v[172:175]\n"
      "buffer_load_dword v198, v199, s[64:67], 0 offen offset:0 sc1\n"
      "v_mfma_f32_16x16x32_bf16 v[16:19], a[16:19], a[32:35], v[16:19]\n"
      "s_add_u32 m0, s72, 36864\n"
      "ds_read_b64_tr_b16 a[12:13], v246 offset:256\n"
      "v_mfma_f32_16x16x32_bf16 v[48:51], a[16:19], a[36:39], v[48:51]\n"
      "ds_read_b64_tr_b16 a[14:15], v246 offset:1280\n"
      "v_mfma_f32_16x16x32_bf16 v[80:83], a[16:19], a[40:43], v[80:83]\n"
      "s_add_u32 s68, s68, 131072\n"
      "buffer_load_dwordx4 v247, s[40:43], s68 offen offset:0  lds\n"
      "v_mfma_f32_16x16x32_bf16 v[112:115], a[16:19], a[44:47], v[112:115]\n"
      "buffer_load_dwordx4 v248, s[40:43], s68 offen offset:1024  lds\n"
      "v_mfma_f32_16x16x32_bf16 v[144:147], a[16:19], a[48:51], v[144:147]\n"
      "ds_read_b128 a[72:75], v251 offset:20992\n"
      "v_mfma_f32_16x16x32_bf16 v[176:179], a[16:19], a[52:55], v[176:179]\n"
      "s_waitcnt vmcnt(15)\n"
      "ds_read_b64_tr_b16 a[192:193], v202 offset:0\n"
      "v_mfma_f32_16x16x32_bf16 v[20:23], a[20:23], a[32:35], v[20:23]\n"
      "ds_read_b64_tr_b16 a[16:17], v245 offset:512\n"
      "v_mfma_f32_16x16x32_bf16 v[52:55], a[20:23], a[36:39], v[52:55]\n"
      "ds_read_b64_tr_b16 a[18:19], v245 offset:1536\n"
      "v_mfma_f32_16x16x32_bf16 v[84:87], a[20:23], a[40:43], v[84:87]\n"
      "ds_read_b64_tr_b16 a[196:197], v203 offset:0\n"
      "v_mfma_f32_16x16x32_bf16 v[116:119], a[20:23], a[44:47], v[116:119]\n"
      "ds_read_b64_tr_b16 a[200:201], v202 offset:256\n"
      "v_mfma_f32_16x16x32_bf16 v[148:151], a[20:23], a[48:51], v[148:151]\n"
      "ds_read_b128 a[76:79], v251 offset:23040\n"
      "v_mfma_f32_16x16x32_bf16 v[180:183], a[20:23], a[52:55], v[180:183]\n"
      "ds_read_b64_tr_b16 a[204:205], v203 offset:256\n"
      "v_mfma_f32_16x16x32_bf16 v[24:27], a[24:27], a[32:35], v[24:27]\n"
      "ds_read_b64_tr_b16 a[20:21], v246 offset:512\n"
      "v_mfma_f32_16x16x32_bf16 v[56:59], a[24:27], a[36:39], v[56:59]\n"
      "ds_read_b64_tr_b16 a[22:23], v246 offset:1536\n"
      "v_mfma_f32_16x16x32_bf16 v[88:91], a[24:27], a[40:43], v[88:91]\n"
      "ds_read_b64_tr_b16 a[208:209], v202 offset:512\n"
      "v_mfma_f32_16x16x32_bf16 v[120:123], a[24:27], a[44:47], v[120:123]\n"
      "ds_read_b64_tr_b16 a[212:213], v203 offset:512\n"
      "v_mfma_f32_16x16x32_bf16 v[152:155], a[24:27], a[48:51], v[152:155]\n"
      "ds_read_b64_tr_b16 a[216:217], v202 offset:768\n"
      "v_mfma_f32_16x16x32_bf16 v[184:187], a[24:27], a[52:55], v[184:187]\n"
      "ds_read_b64_tr_b16 a[220:221], v203 offset:768\n"
      "v_mfma_f32_16x16x32_bf16 v[28:31], a[28:31], a[32:35], v[28:31]\n"
      "ds_read_b64_tr_b16 a[24:25], v245 offset:768\n"
      "v_mfma_f32_16x16x32_bf16 v[60:63], a[28:31], a[36:39], v[60:63]\n"
      "ds_read_b64_tr_b16 a[26:27], v245 offset:1792\n"
      "v_mfma_f32_16x16x32_bf16 v[92:95], a[28:31], a[40:43], v[92:95]\n"
      "buffer_load_dwordx4 v249, s[40:43], s68 offen offset:2048  lds\n"
      "v_mfma_f32_16x16x32_bf16 v[124:127], a[28:31], a[44:47], v[124:127]\n"
      "buffer_load_dwordx4 v250, s[40:43], s68 offen offset:3072  lds\n"
      "s_add_u32 s68, s68, 131072\n"
      "v_mfma_f32_16x16x32_bf16 v[156:159], a[28:31], a[48:51], v[156:159]\n"
      "s_cmp_eq_u32 s68, 5242880\n"
      "s_cselect_b32 s68, 0, s68\n"
      "v_mfma_f32_16x16x32_bf16 v[188:191], a[28:31], a[52:55], v[188:191]\n"
      "s_waitcnt lgkmcnt(14)\n"
      "v_mfma_f32_16x16x32_bf16 v[0:3], a[0:3], a[56:59], v[0:3]\n"
      "s_barrier\n"
      "ds_read_b64_tr_b16 a[28:29], v246 offset:768\n"
      "v_mfma_f32_16x16x32_bf16 v[32:35], a[0:3], a[60:63], v[32:35]\n"
      "s_add_u32 m0, s81, 114688\n"
      "s_add_u32 s73, s69, 0\n"
      "ds_read_b64_tr_b16 a[30:31], v246 offset:1792\n"
      "v_mfma_f32_16x16x32_bf16 v[64:67], a[0:3], a[64:67], v[64:67]\n"
      "buffer_load_dwordx4 v252, s[36:39], s73 offen offset:0  lds\n"
      "v_mfma_f32_16x16x32_bf16 v[96:99], a[0:3], a[68:71], v[96:99]\n"
      "s_add_u32 m0, s81, 118784\n"
      "v_mfma_f32_16x16x32_bf16 v[128:131], a[0:3], a[72:75], v[128:131]\n"
      "s_waitcnt lgkmcnt(11)\n"
      "s_add_u32 s73, s69, 40960\n"
      "ds_read_b128 a[32:35], v251 offset:24576\n"
      "v_mfma_f32_16x16x32_bf16 v[160:163], a[0:3], a[76:79], v[160:163]\n"
      "buffer_load_dwordx4 v252, s[36:39], s73 offen offset:0  lds\n"
      "v_mfma_f32_16x16x32_bf16 v[4:7], a[4:7], a[56:59], v[4:7]\n"
      "s_waitcnt vmcnt(11)\n"
      "s_add_u32 m0, s81, 122880\n"
      "ds_read_b64_tr_b16 a[0:1], v243 offset:0\n"
      "v_mfma_f32_16x16x32_bf16 v[36:39], a[4:7], a[60:63], v[36:39]\n"
      "s_add_u32 s73, s69, 81920\n"
      "ds_read_b64_tr_b16 a[2:3], v243 offset:1024\n"
      "v_mfma_f32_16x16x32_bf16 v[68:71], a[4:7], a[64:67], v[68:71]\n"
      "buffer_load_dwordx4 v252, s[36:39], s73 offen offset:0  lds\n"
      "v_mfma_f32_16x16x32_bf16 v[100:103], a[4:7], a[68:71], v[100:103]\n"
      "s_add_u32 s69, s69, 128\n"
      "s_cmp_eq_u32 s69, 1280\n"
      "s_cselect_b32 s69, 0, s69\n"
      "v_mfma_f32_16x16x32_bf16 v[132:135], a[4:7], a[72:75], v[132:135]\n"
      "s_mov_b32 m0, s78\n"
      "ds_read_b128 a[36:39], v251 offset:26624\n"
      "v_mfma_f32_16x16x32_bf16 v[164:167], a[4:7], a[76:79], v[164:167]\n"
      "s_waitcnt lgkmcnt(8)\n"
      "buffer_load_dwordx4 v234, s[48:51], s71 offen offset:0 nt lds\n"
      "v_mfma_f32_16x16x32_bf16 v[8:11], a[8:11], a[56:59], v[8:11]\n"
      "ds_read_b64_tr_b16 a[4:5], v244 offset:0\n"
      "v_mfma_f32_16x16x32_bf16 v[40:43], a[8:11], a[60:63], v[40:43]\n"
      "ds_read_b64_tr_b16 a[6:7], v244 offset:1024\n"
      "v_mfma_f32_16x16x32_bf16 v[72:75], a[8:11], a[64:67], v[72:75]\n"
      "buffer_load_dwordx4 v236, s[48:51], s71 offen offset:1024 nt lds\n"
      "v_mfma_f32_16x16x32_bf16 v[104:107], a[8:11], a[68:71], v[104:107]\n"
      "buffer_load_dwordx4 v234, s[48:51], s71 offen offset:2048 nt lds\n"
      "v_mfma_f32_16x16x32_bf16 v[136:139], a[8:11], a[72:75], v[136:139]\n"
      "ds_read_b128 a[40:43], v251 offset:28672\n"
      "v_mfma_f32_16x16x32_bf16 v[168:171], a[8:11], a[76:79], v[168:171]\n"
      "buffer_load_dwordx4 v236, s[48:51], s71 offen offset:3072 nt lds\n"
      "s_add_u32 s71, s71, %16\n"
      "s_and_b32 s71, s71, 0x1ffff\n"
      "v_mfma_f32_16x16x32_bf16 v[12:15], a[12:15], a[56:59], v[12:15]\n"
      "ds_read_b64_tr_b16 a[8:9], v243 offset:256\n"
      "v_mfma_f32_16x16x32_bf16 v[44:47], a[12:15], a[60:63], v[44:47]\n"
      "s_add_u32 m0, s72, 65536\n"
      "ds_read_b64_tr_b16 a[10:11], v243 offset:1280\n"
      "v_mfma_f32_16x16x32_bf16 v[76:79], a[12:15], a[64:67], v[76:79]\n"
      "v_mfma_f32_16x16x32_bf16 v[108:111], a[12:15], a[68:71], v[108:111]\n"
      "s_waitcnt lgkmcnt(9)\n"
      "buffer_load_dwordx4 v247, s[40:43], s68 offen offset:0  lds\n"
      "v_mfma_f32_16x16x32_bf16 v[140:143], a[12:15], a[72:75], v[140:143]\n"
      "ds_read_b128 a[44:47], v251 offset:30720\n"
      "v_mfma_f32_16x16x32_bf16 v[172:175], a[12:15], a[76:79], v[172:175]\n"
      "buffer_load_dwordx4 v248, s[40:43], s68 offen offset:1024  lds\n"
      "v_mfma_f32_16x16x32_bf16 v[16:19], a[16:19], a[56:59], v[16:19]\n"
      "ds_read_b64_tr_b16 a[12:13], v244 offset:256\n"
      "v_mfma_f32_16x16x32_bf16 v[48:51], a[16:19], a[60:63], v[48:51]\n"
      "ds_read_b64_tr_b16 a[14:15], v244 offset:1280\n"
      "v_mfma_f32_16x16x32_bf16 v[80:83], a[16:19], a[64:67], v[80:83]\n"
      "buffer_load_dwordx4 v249, s[40:43], s68 offen offset:2048  lds\n"
      "v_mfma_f32_16x16x32_bf16 v[112:115], a[16:19], a[68:71], v[112:115]\n"
      "buffer_load_dwordx4 v250, s[40:43], s68 offen offset:3072  lds\n"
      "v_mfma_f32_16x16x32_bf16 v[144:147], a[16:19], a[72:75], v[144:147]\n"
      "ds_read_b128 a[48:51], v251 offset:32768\n"
      "v_mfma_f32_16x16x32_bf16 v[176:179], a[16:19], a[76:79], v[176:179]\n"
      "v_mfma_f32_16x16x32_bf16 v[20:23], a[20:23], a[56:59], v[20:23]\n"
      "s_add_u32 m0, s72, 69632\n"
      "ds_read_b64_tr_b16 a[16:17], v243 offset:512\n"
      "v_mfma_f32_16x16x32_bf16 v[52:55], a[20:23], a[60:63], v[52:55]\n"
      "ds_read_b64_tr_b16 a[18:19], v243 offset:1536\n"
      "v_mfma_f32_16x16x32_bf16 v[84:87], a[20:23], a[64:67], v[84:87]\n"
      "s_add_u32 s68, s68, 131072\n"
      "v_mfma_f32_16x16x32_bf16 v[116:119], a[20:23], a[68:71], v[116:119]\n"
      "v_mfma_f32_16x16x32_bf16 v[148:151], a[20:23], a[72:75], v[148:151]\n"
      "ds_read_b128 a[52:55], v251 offset:34816\n"
      "v_mfma_f32_16x16x32_bf16 v[180:183], a[20:23], a[76:79], v[180:183]\n"
      "buffer_load_dwordx4 v247, s[40:43], s68 offen offset:0  lds\n"
      "v_mfma_f32_16x16x32_bf16 v[24:27], a[24:27], a[56:59], v[24:27]\n"
      "ds_read_b64_tr_b16 a[20:21], v244 offset:512\n"
      "v_mfma_f32_16x16x32_bf16 v[56:59], a[24:27], a[60:63], v[56:59]\n"
      "ds_read_b64_tr_b16 a[22:23], v244 offset:1536\n"
      "v_mfma_f32_16x16x32_bf16 v[88:91], a[24:27], a[64:67], v[88:91]\n"
      "buffer_load_dwordx4 v248, s[40:43], s68 offen offset:1024  lds\n"
      "v_mfma_f32_16x16x32_bf16 v[120:123], a[24:27], a[68:71], v[120:123]\n"
      "buffer_load_dwordx4 v249, s[40:43], s68 offen offset:2048  lds\n"
      "v_mfma_f32_16x16x32_bf16 v[152:155], a[24:27], a[72:75], v[152:155]\n"
      "v_mfma_f32_16x16x32_bf16 v[184:187], a[24:27], a[76:79], v[184:187]\n"
      "buffer_load_dwordx4 v250, s[40:43], s68 offen offset:3072  lds\n"
      "v_mfma_f32_16x16x32_bf16 v[28:31], a[28:31], a[56:59], v[28:31]\n"
      "ds_read_b64_tr_b16 a[24:25], v243 offset:768\n"
      "v_mfma_f32_16x16x32_bf16 v[60:63], a[28:31], a[60:63], v[60:63]\n"
      "ds_read_b64_tr_b16 a[26:27], v243 offset:1792\n"
      "v_mfma_f32_16x16x32_bf16 v[92:95], a[28:31], a[64:67], v[92:95]\n"
      "s_add_u32 s68, s68, 131072\n"
      "v_mfma_f32_16x16x32_bf16 v[124:127], a[28:31], a[68:71], v[124:127]\n"
      "v_mfma_f32_16x16x32_bf16 v[156:159], a[28:31], a[72:75], v[156:159]\n"
      "s_cmp_eq_u32 s68, 5242880\n"
      "s_cselect_b32 s68, 0, s68\n"
      "v_mfma_f32_16x16x32_bf16 v[188:191], a[28:31], a[76:79], v[188:191]\n"
      "s_waitcnt lgkmcnt(14)\n"
      "v_mfma_f32_16x16x32_bf16 v[0:3], a[0:3], a[32:35], v[0:3]\n"
      "ds_read_b64_tr_b16 a[28:29], v244 offset:768\n"
      "v_mfma_f32_16x16x32_bf16 v[32:35], a[0:3], a[36:39], v[32:35]\n"
      "s_waitcnt lgkmcnt(14)\n"
      "ds_read_b64_tr_b16 a[30:31], v244 offset:1792\n"
      "v_mfma_f32_16x16x32_bf16 v[64:67], a[0:3], a[40:43], v[64:67]\n"
      "s_waitcnt lgkmcnt(12)\n"
      "v_mfma_f32_16x16x32_bf16 v[96:99], a[0:3], a[44:47], v[96:99]\n"
      "s_waitcnt lgkmcnt(9)\n"
      "v_mfma_f32_16x16x32_bf16 v[128:131], a[0:3], a[48:51], v[128:131]\n"
      "s_waitcnt lgkmcnt(6)\n"
      "s_lshl_b32 s90, %13, 6\n"
      "ds_read_b128 a[56:59], v251 offset:25088\n"
      "v_mfma_f32_16x16x32_bf16 v[160:163], a[0:3], a[52:55], v[160:163]\n"
      "v_mfma_f32_16x16x32_bf16 v[4:7], a[4:7], a[32:35], v[4:7]\n"
      "s_waitcnt vmcnt(15)\n"
      "ds_read_b64_tr_b16 a[0:1], v243 offset:32768\n"
      "v_mfma_f32_16x16x32_bf16 v[36:39], a[4:7], a[36:39], v[36:39]\n"
      "ds_read_b64_tr_b16 a[2:3], v243 offset:33792\n"
      "v_mfma_f32_16x16x32_bf16 v[68:71], a[4:7], a[40:43], v[68:71]\n"
      "v_mfma_f32_16x16x32_bf16 v[100:103], a[4:7], a[44:47], v[100:103]\n"
      "s_add_u32 m0, s72, 0\n"
      "v_mfma_f32_16x16x32_bf16 v[132:135], a[4:7], a[48:51], v[132:135]\n"
      "s_add_u32 s90, s90, 256\n"
      "ds_read_b128 a[60:63], v251 offset:27136\n"
      "v_mfma_f32_16x16x32_bf16 v[164:167], a[4:7], a[52:55], v[164:167]\n"
      "s_waitcnt lgkmcnt(4)\n"
      "buffer_load_dwordx4 v247, s[40:43], s68 offen offset:0  lds\n"
      "v_mfma_f32_16x16x32_bf16 v[8:11], a[8:11], a[32:35], v[8:11]\n"
      "ds_read_b64_tr_b16 a[4:5], v244 offset:32768\n"
      "v_mfma_f32_16x16x32_bf16 v[40:43], a[8:11], a[36:39], v[40:43]\n"
      "ds_read_b64_tr_b16 a[6:7], v244 offset:33792\n"
      "v_mfma_f32_16x16x32_bf16 v[72:75], a[8:11], a[40:43], v[72:75]\n"
      "v_mfma_f32_16x16x32_bf16 v[104:107], a[8:11], a[44:47], v[104:107]\n"
      "buffer_load_dwordx4 v248, s[40:43], s68 offen offset:1024  lds\n"
      "v_lshl_add_u32 v199, v192, 2, s90\n"
      "v_mfma_f32_16x16x32_bf16 v[136:139], a[8:11], a[48:51], v[136:139]\n"
      "ds_read_b128 a[64:67], v251 offset:29184\n"
      "v_mfma_f32_16x16x32_bf16 v[168:171], a[8:11], a[52:55], v[168:171]\n"
      "buffer_load_dwordx4 v249, s[40:43], s68 offen offset:2048  lds\n"
      "v_mfma_f32_16x16x32_bf16 v[12:15], a[12:15], a[32:35], v[12:15]\n"
      "ds_read_b64_tr_b16 a[8:9], v243 offset:33024\n"
      "v_mfma_f32_16x16x32_bf16 v[44:47], a[12:15], a[36:39], v[44:47]\n"
      "ds_read_b64_tr_b16 a[10:11], v243 offset:34048\n"
      "v_mfma_f32_16x16x32_bf16 v[76:79], a[12:15], a[40:43], v[76:79]\n"
      "v_add_u32 v196, s72, v192\n"
      "v_cmp_gt_u32 vcc, 8, v196\n"
      "v_mfma_f32_16x16x32_bf16 v[108:111], a[12:15], a[44:47], v[108:111]\n"
      "buffer_load_dwordx4 v250, s[40:43], s68 offen offset:3072  lds\n"
      "v_mov_b32 v196, 0x40000000\n"
      "v_mfma_f32_16x16x32_bf16 v[140:143], a[12:15], a[48:51], v[140:143]\n"
      "ds_read_b128 a[68:71], v251 offset:31232\n"
      "v_cndmask_b32 v199, v196, v199, vcc\n"
      "v_mfma_f32_16x16x32_bf16 v[172:175], a[12:15], a[52:55], v[172:175]\n"
      "buffer_store_dword v198, v199, s[64:67], 0 offen offset:0 sc1\n"
      "v_mfma_f32_16x16x32_bf16 v[16:19], a[16:19], a[32:35], v[16:19]\n"
      "s_add_u32 m0, s72, 4096\n"
      "ds_read_b64_tr_b16 a[12:13], v244 offset:33024\n"
      "v_mfma_f32_16x16x32_bf16 v[48:51], a[16:19], a[36:39], v[48:51]\n"
      "ds_read_b64_tr_b16 a[14:15], v244 offset:34048\n"
      "v_mfma_f32_16x16x32_bf16 v[80:83], a[16:19], a[40:43], v[80:83]\n"
      "s_add_u32 s68, s68, 131072\n"
      "buffer_load_dwordx4 v247, s[40:43], s68 offen offset:0  lds\n"
      "v_mfma_f32_16x16x32_bf16 v[112:115], a[16:19], a[44:47], v[112:115]\n"
      "buffer_load_dwordx4 v248, s[40:43], s68 offen offset:1024  lds\n"
      "v_mfma_f32_16x16x32_bf16 v[144:147], a[16:19], a[48:51], v[144:147]\n"
      "ds_read_b128 a[72:75], v251 offset:33280\n"
      "v_mfma_f32_16x16x32_bf16 v[176:179], a[16:19], a[52:55], v[176:179]\n"
      "s_waitcnt vmcnt(15)\n"
      "ds_read_b64_tr_b16 a[194:195], v202 offset:0\n"
      "v_mfma_f32_16x16x32_bf16 v[20:23], a[20:23], a[32:35], v[20:23]\n"
      "ds_read_b64_tr_b16 a[16:17], v243 offset:33280\n"
      "v_mfma_f32_16x16x32_bf16 v[52:55], a[20:23], a[36:39], v[52:55]\n"
      "ds_read_b64_tr_b16 a[18:19], v243 offset:34304\n"
      "v_mfma_f32_16x16x32_bf16 v[84:87], a[20:23], a[40:43], v[84:87]\n"
      "ds_read_b64_tr_b16 a[198:199], v203 offset:0\n"
      "v_mfma_f32_16x16x32_bf16 v[116:119], a[20:23], a[44:47], v[116:119]\n"
      "ds_read_b64_tr_b16 a[202:203], v202 offset:256\n"
      "v_mfma_f32_16x16x32_bf16 v[148:151], a[20:23], a[48:51], v[148:151]\n"
      "ds_read_b128 a[76:79], v251 offset:35328\n"
      "v_mfma_f32_16x16x32_bf16 v[180:183], a[20:23], a[52:55], v[180:183]\n"
      "ds_read_b64_tr_b16 a[206:207], v203 offset:256\n"
      "v_mfma_f32_16x16x32_bf16 v[24:27], a[24:27], a[32:35], v[24:27]\n"
      "ds_read_b64_tr_b16 a[20:21], v244 offset:33280\n"
      "v_mfma_f32_16x16x32_bf16 v[56:59], a[24:27], a[36:39], v[56:59]\n"
      "ds_read_b64_tr_b16 a[22:23], v244 offset:34304\n"
      "v_mfma_f32_16x16x32_bf16 v[88:91], a[24:27], a[40:43], v[88:91]\n"
      "ds_read_b64_tr_b16 a[210:211], v202 offset:512\n"
      "v_mfma_f32_16x16x32_bf16 v[120:123], a[24:27], a[44:47], v[120:123]\n"
      "ds_read_b64_tr_b16 a[214:215], v203 offset:512\n"
      "v_mfma_f32_16x16x32_bf16 v[152:155], a[24:27], a[48:51], v[152:155]\n"
      "ds_read_b64_tr_b16 a[218:219], v202 offset:768\n"
      "v_mfma_f32_16x16x32_bf16 v[184:187], a[24:27], a[52:55], v[184:187]\n"
      "ds_read_b64_tr_b16 a[222:223], v203 offset:768\n"
      "v_mfma_f32_16x16x32_bf16 v[28:31], a[28:31], a[32:35], v[28:31]\n"
      "ds_read_b64_tr_b16 a[24:25], v243 offset:33536\n"
      "v_mfma_f32_16x16x32_bf16 v[60:63], a[28:31], a[36:39], v[60:63]\n"
      "ds_read_b64_tr_b16 a[26:27], v243 offset:34560\n"
      "v_mfma_f32_16x16x32_bf16 v[92:95], a[28:31], a[40:43], v[92:95]\n"
      "buffer_load_dwordx4 v249, s[40:43], s68 offen offset:2048  lds\n"
      "v_mfma_f32_16x16x32_bf16 v[124:127], a[28:31], a[44:47], v[124:127]\n"
      "buffer_load_dwordx4 v250, s[40:43], s68 offen offset:3072  lds\n"
      "s_add_u32 s68, s68, 131072\n"
      "v_mfma_f32_16x16x32_bf16 v[156:159], a[28:31], a[48:51], v[156:159]\n"
      "s_cmp_eq_u32 s68, 5242880\n"
      "s_cselect_b32 s68, 0, s68\n"
      "v_mfma_f32_16x16x32_bf16 v[188:191], a[28:31], a[52:55], v[188:191]\n"
      "s_waitcnt lgkmcnt(14)\n"
      "v_mfma_f32_16x16x32_bf16 v[0:3], a[0:3], a[56:59], v[0:3]\n"
      "s_barrier\n"
      "ds_read_b64_tr_b16 a[28:29], v244 offset:33536\n"
      "v_mfma_f32_16x16x32_bf16 v[32:35], a[0:3], a[60:63], v[32:35]\n"
      "s_add_u32 m0, s81, 126976\n"
      "s_add_u32 s73, s69, 0\n"
      "ds_read_b64_tr_b16 a[30:31], v244 offset:34560\n"
      "v_mfma_f32_16x16x32_bf16 v[64:67], a[0:3], a[64:67], v[64:67]\n"
      "buffer_load_dwordx4 v252, s[36:39], s73 offen offset:0  lds\n"
      "v_mfma_f32_16x16x32_bf16 v[96:99], a[0:3], a[68:71], v[96:99]\n"
      "s_add_u32 m0, s81, 131072\n"
      "v_mfma_f32_16x16x32_bf16 v[128:131], a[0:3], a[72:75], v[128:131]\n"
      "s_waitcnt lgkmcnt(11)\n"
      "s_add_u32 s73, s69, 40960\n"
      "ds_read_b128 a[32:35], v251 offset:36864\n"
      "v_mfma_f32_16x16x32_bf16 v[160:163], a[0:3], a[76:79], v[160:163]\n"
      "buffer_load_dwordx4 v252, s[36:39], s73 offen offset:0  lds\n"
      "v_mfma_f32_16x16x32_bf16 v[4:7], a[4:7], a[56:59], v[4:7]\n"
      "s_waitcnt vmcnt(11)\n"
      "s_add_u32 m0, s81, 135168\n"
      "ds_read_b64_tr_b16 a[0:1], v245 offset:0\n"
      "v_mfma_f32_16x16x32_bf16 v[36:39], a[4:7], a[60:63], v[36:39]\n"
      "s_add_u32 s73, s69, 81920\n"
      "ds_read_b64_tr_b16 a[2:3], v245 offset:1024\n"
      "v_mfma_f32_16x16x32_bf16 v[68:71], a[4:7], a[64:67], v[68:71]\n"
      "buffer_load_dwordx4 v252, s[36:39], s73 offen offset:0  lds\n"
      "v_mfma_f32_16x16x32_bf16 v[100:103], a[4:7], a[68:71], v[100:103]\n"
      "s_add_u32 s69, s69, 128\n"
      "s_cmp_eq_u32 s69, 1280\n"
      "s_cselect_b32 s69, 0, s69\n"
      "v_mfma_f32_16x16x32_bf16 v[132:135], a[4:7], a[72:75], v[132:135]\n"
      "s_mov_b32 m0, s78\n"
      "ds_read_b128 a[36:39], v251 offset:38912\n"
      "v_mfma_f32_16x16x32_bf16 v[164:167], a[4:7], a[76:79], v[164:167]\n"
      "s_waitcnt lgkmcnt(8)\n"
      "buffer_load_dwordx4 v234, s[44:47], s70 offen offset:0 nt lds\n"
      "v_mfma_f32_16x16x32_bf16 v[8:11], a[8:11], a[56:59], v[8:11]\n"
      "ds_read_b64_tr_b16 a[4:5], v246 offset:0\n"
      "v_mfma_f32_16x16x32_bf16 v[40:43], a[8:11], a[60:63], v[40:43]\n"
      "ds_read_b64_tr_b16 a[6:7], v246 offset:1024\n"
      "v_mfma_f32_16x16x32_bf16 v[72:75], a[8:11], a[64:67], v[72:75]\n"
      "buffer_load_dwordx4 v234, s[44:47], s70 offen offset:1024 nt lds\n"
      "v_mfma_f32_16x16x32_bf16 v[104:107], a[8:11], a[68:71], v[104:107]\n"
      "buffer_load_dwordx4 v235, s[44:47], s70 offen offset:2048 nt lds\n"
      "v_mfma_f32_16x16x32_bf16 v[136:139], a[8:11], a[72:75], v[136:139]\n"
      "ds_read_b128 a[40:43], v251 offset:40960\n"
      "v_mfma_f32_16x16x32_bf16 v[168:171], a[8:11], a[76:79], v[168:171]\n"
      "buffer_load_dwordx4 v235, s[44:47], s70 offen offset:3072 nt lds\n"
      "s_add_u32 s70, s70, %16\n"
      "s_and_b32 s70, s70, 0x1ffff\n"
      "v_mfma_f32_16x16x32_bf16 v[12:15], a[12:15], a[56:59], v[12:15]\n"
      "ds_read_b64_tr_b16 a[8:9], v245 offset:256\n"
      "v_mfma_f32_16x16x32_bf16 v[44:47], a[12:15], a[60:63], v[44:47]\n"
      "s_add_u32 m0, s72, 32768\n"
      "ds_read_b64_tr_b16 a[10:11], v245 offset:1280\n"
      "v_mfma_f32_16x16x32_bf16 v[76:79], a[12:15], a[64:67], v[76:79]\n"
      "v_mfma_f32_16x16x32_bf16 v[108:111], a[12:15], a[68:71], v[108:111]\n"
      "s_waitcnt lgkmcnt(9)\n"
      "buffer_load_dwordx4 v247, s[40:43], s68 offen offset:0  lds\n"
      "v_mfma_f32_16x16x32_bf16 v[140:143], a[12:15], a[72:75], v[140:143]\n"
      "ds_read_b128 a[44:47], v251 offset:43008\n"
      "v_mfma_f32_16x16x32_bf16 v[172:175], a[12:15], a[76:79], v[172:175]\n"
      "buffer_load_dwordx4 v248, s[40:43], s68 offen offset:1024  lds\n"
      "v_mfma_f32_16x16x32_bf16 v[16:19], a[16:19], a[56:59], v[16:19]\n"
      "ds_read_b64_tr_b16 a[12:13], v246 offset:256\n"
      "v_mfma_f32_16x16x32_bf16 v[48:51], a[16:19], a[60:63], v[48:51]\n"
      "ds_read_b64_tr_b16 a[14:15], v246 offset:1280\n"
      "v_mfma_f32_16x16x32_bf16 v[80:83], a[16:19], a[64:67], v[80:83]\n"
      "buffer_load_dwordx4 v249, s[40:43], s68 offen offset:2048  lds\n"
      "v_mfma_f32_16x16x32_bf16 v[112:115], a[16:19], a[68:71], v[112:115]\n"
      "buffer_load_dwordx4 v250, s[40:43], s68 offen offset:3072  lds\n"
      "v_mfma_f32_16x16x32_bf16 v[144:147], a[16:19], a[72:75], v[144:147]\n"
      "ds_read_b128 a[48:51], v251 offset:45056\n"
      "v_mfma_f32_16x16x32_bf16 v[176:179], a[16:19], a[76:79], v[176:179]\n"
      "v_mfma_f32_16x16x32_bf16 v[20:23], a[20:23], a[56:59], v[20:23]\n"
      "s_add_u32 m0, s72, 36864\n"
      "ds_read_b64_tr_b16 a[16:17], v245 offset:512\n"
      "v_mfma_f32_16x16x32_bf16 v[52:55], a[20:23], a[60:63], v[52:55]\n"
      "ds_read_b64_tr_b16 a[18:19], v245 offset:1536\n"
      "v_mfma_f32_16x16x32_bf16 v[84:87], a[20:23], a[64:67], v[84:87]\n"
      "s_add_u32 s68, s68, 131072\n"
      "v_mfma_f32_16x16x32_bf16 v[116:119], a[20:23], a[68:71], v[116:119]\n"
      "v_mfma_f32_16x16x32_bf16 v[148:151], a[20:23], a[72:75], v[148:151]\n"
      "ds_read_b128 a[52:55], v251 offset:47104\n"
      "v_mfma_f32_16x16x32_bf16 v[180:183], a[20:23], a[76:79], v[180:183]\n"
      "buffer_load_dwordx4 v247, s[40:43], s68 offen offset:0  lds\n"
      "v_mfma_f32_16x16x32_bf16 v[24:27], a[24:27], a[56:59], v[24:27]\n"
      "ds_read_b64_tr_b16 a[20:21], v246 offset:512\n"
      "v_mfma_f32_16x16x32_bf16 v[56:59], a[24:27], a[60:63], v[56:59]\n"
      "ds_read_b64_tr_b16 a[22:23], v246 offset:1536\n"
      "v_mfma_f32_16x16x32_bf16 v[88:91], a[24:27], a[64:67], v[88:91]\n"
      "buffer_load_dwordx4 v248, s[40:43], s68 offen offset:1024  lds\n"
      "v_mfma_f32_16x16x32_bf16 v[120:123], a[24:27], a[68:71], v[120:123]\n"
      "buffer_load_dwordx4 v249, s[40:43], s68 offen offset:2048  lds\n"
      "v_mfma_f32_16x16x32_bf16 v[152:155], a[24:27], a[72:75], v[152:155]\n"
      "v_mfma_f32_16x16x32_bf16 v[184:187], a[24:27], a[76:79], v[184:187]\n"
      "buffer_load_dwordx4 v250, s[40:43], s68 offen offset:3072  lds\n"
      "v_mfma_f32_16x16x32_bf16 v[28:31], a[28:31], a[56:59], v[28:31]\n"
      "ds_read_b64_tr_b16 a[24:25], v245 offset:768\n"
      "v_mfma_f32_16x16x32_bf16 v[60:63], a[28:31], a[60:63], v[60:63]\n"
      "ds_read_b64_tr_b16 a[26:27], v245 offset:1792\n"
      "v_mfma_f32_16x16x32_bf16 v[92:95], a[28:31], a[64:67], v[92:95]\n"
      "s_add_u32 s68, s68, 131072\n"
      "v_mfma_f32_16x16x32_bf16 v[124:127], a[28:31], a[68:71], v[124:127]\n"
      "v_mfma_f32_16x16x32_bf16 v[156:159], a[28:31], a[72:75], v[156:159]\n"
      "s_cmp_eq_u32 s68, 5242880\n"
      "s_cselect_b32 s68, 0, s68\n"
      "v_mfma_f32_16x16x32_bf16 v[188:191], a[28:31], a[76:79], v[188:191]\n"
      "s_waitcnt lgkmcnt(14)\n"
      "v_mfma_f32_16x16x32_bf16 v[0:3], a[0:3], a[32:35], v[0:3]\n"
      "ds_read_b64_tr_b16 a[28:29], v246 offset:768\n"
      "v_mfma_f32_16x16x32_bf16 v[32:35], a[0:3], a[36:39], v[32:35]\n"
      "s_waitcnt lgkmcnt(14)\n"
      "ds_read_b64_tr_b16 a[30:31], v246 offset:1792\n"
      "v_mfma_f32_16x16x32_bf16 v[64:67], a[0:3], a[40:43], v[64:67]\n"
      "s_waitcnt lgkmcnt(12)\n"
      "v_mfma_f32_16x16x32_bf16 v[96:99], a[0:3], a[44:47], v[96:99]\n"
      "s_waitcnt lgkmcnt(9)\n"
      "v_mfma_f32_16x16x32_bf16 v[128:131], a[0:3], a[48:51], v[128:131]\n"
      "s_waitcnt lgkmcnt(6)\n"
      "ds_read_b128 a[56:59], v251 offset:37376\n"
      "v_mfma_f32_16x16x32_bf16 v[160:163], a[0:3], a[52:55], v[160:163]\n"
      "v_mfma_f32_16x16x32_bf16 v[4:7], a[4:7], a[32:35], v[4:7]\n"
      "s_waitcnt vmcnt(15)\n"
      "ds_read_b64_tr_b16 a[0:1], v243 offset:0\n"
      "v_mfma_f32_16x16x32_bf16 v[36:39], a[4:7], a[36:39], v[36:39]\n"
      "ds_read_b64_tr_b16 a[2:3], v243 offset:1024\n"
      "v_mfma_f32_16x16x32_bf16 v[68:71], a[4:7], a[40:43], v[68:71]\n"
      "v_mfma_f32_16x16x32_bf16 v[100:103], a[4:7], a[44:47], v[100:103]\n"
      "s_add_u32 m0, s72, 65536\n"
      "v_mfma_f32_16x16x32_bf16 v[132:135], a[4:7], a[48:51], v[132:135]\n"
      "ds_read_b128 a[60:63], v251 offset:39424\n"
      "v_mfma_f32_16x16x32_bf16 v[164:167], a[4:7], a[52:55], v[164:167]\n"
      "s_waitcnt lgkmcnt(4)\n"
      "buffer_load_dwordx4 v247, s[40:43], s68 offen offset:0  lds\n"
      "v_mfma_f32_16x16x32_bf16 v[8:11], a[8:11], a[32:35], v[8:11]\n"
      "ds_read_b64_tr_b16 a[4:5], v244 offset:0\n"
      "v_mfma_f32_16x16x32_bf16 v[40:43], a[8:11], a[36:39], v[40:43]\n"
      "ds_read_b64_tr_b16 a[6:7], v244 offset:1024\n"
      "v_mfma_f32_16x16x32_bf16 v[72:75], a[8:11], a[40:43], v[72:75]\n"
      "v_mfma_f32_16x16x32_bf16 v[104:107], a[8:11], a[44:47], v[104:107]\n"
      "buffer_load_dwordx4 v248, s[40:43], s68 offen offset:1024  lds\n"
      "v_mfma_f32_16x16x32_bf16 v[136:139], a[8:11], a[48:51], v[136:139]\n"
      "ds_read_b128 a[64:67], v251 offset:41472\n"
      "v_mfma_f32_16x16x32_bf16 v[168:171], a[8:11], a[52:55], v[168:171]\n"
      "buffer_load_dwordx4 v249, s[40:43], s68 offen offset:2048  lds\n"
      "v_mfma_f32_16x16x32_bf16 v[12:15], a[12:15], a[32:35], v[12:15]\n"
      "ds_read_b64_tr_b16 a[8:9], v243 offset:256\n"
      "v_mfma_f32_16x16x32_bf16 v[44:47], a[12:15], a[36:39], v[44:47]\n"
      "ds_read_b64_tr_b16 a[10:11], v243 offset:1280\n"
      "v_mfma_f32_16x16x32_bf16 v[76:79], a[12:15], a[40:43], v[76:79]\n"
      "v_mfma_f32_16x16x32_bf16 v[108:111], a[12:15], a[44:47], v[108:111]\n"
      "buffer_load_dwordx4 v250, s[40:43], s68 offen offset:3072  lds\n"
      "v_mfma_f32_16x16x32_bf16 v[140:143], a[12:15], a[48:51], v[140:143]\n"
      "ds_read_b128 a[68:71], v251 offset:43520\n"
      "v_mfma_f32_16x16x32_bf16 v[172:175], a[12:15], a[52:55], v[172:175]\n"
      "v_mfma_f32_16x16x32_bf16 v[16:19], a[16:19], a[32:35], v[16:19]\n"
      "s_add_u32 m0, s72, 69632\n"
      "ds_read_b64_tr_b16 a[12:13], v244 offset:256\n"
      "v_mfma_f32_16x16x32_bf16 v[48:51], a[16:19], a[36:39], v[48:51]\n"
      "ds_read_b64_tr_b16 a[14:15], v244 offset:1280\n"
      "v_mfma_f32_16x16x32_bf16 v[80:83], a[16:19], a[40:43], v[80:83]\n"
      "v_mfma_f32_16x16x32_bf16 v[112:115], a[16:19], a[44:47], v[112:115]\n"
      "s_add_u32 s68, s68, 131072\n"
      "v_mfma_f32_16x16x32_bf16 v[144:147], a[16:19], a[48:51], v[144:147]\n"
      "ds_read_b128 a[72:75], v251 offset:45568\n"
      "v_mfma_f32_16x16x32_bf16 v[176:179], a[16:19], a[52:55], v[176:179]\n"
      "s_waitcnt vmcnt(12)\n"
      "ds_read_b128 a[96:99], v201 offset:0\n"
      "v_mfma_f32_16x16x32_bf16 v[20:23], a[20:23], a[32:35], v[20:23]\n"
      "ds_read_b64_tr_b16 a[16:17], v243 offset:512\n"
      "v_mfma_f32_16x16x32_bf16 v[52:55], a[20:23], a[36:39], v[52:55]\n"
      "ds_read_b64_tr_b16 a[18:19], v243 offset:1536\n"
      "v_mfma_f32_16x16x32_bf16 v[84:87], a[20:23], a[40:43], v[84:87]\n"
      "buffer_load_dwordx4 v247, s[40:43], s68 offen offset:0  lds\n"
      "v_mfma_f32_16x16x32_bf16 v[116:119], a[20:23], a[44:47], v[116:119]\n"
      "ds_read_b128 a[100:103], v201 offset:256\n"
      "v_mfma_f32_16x16x32_bf16 v[148:151], a[20:23], a[48:51], v[148:151]\n"
      "ds_read_b128 a[76:79], v251 offset:47616\n"
      "v_mfma_f32_16x16x32_bf16 v[180:183], a[20:23], a[52:55], v[180:183]\n"
      "buffer_load_dwordx4 v248, s[40:43], s68 offen offset:1024  lds\n"
      "v_mfma_f32_16x16x32_bf16 v[24:27], a[24:27], a[32:35], v[24:27]\n"
      "ds_read_b64_tr_b16 a[20:21], v244 offset:512\n"
      "v_mfma_f32_16x16x32_bf16 v[56:59], a[24:27], a[36:39], v[56:59]\n"
      "ds_read_b64_tr_b16 a[22:23], v244 offset:1536\n"
      "v_mfma_f32_16x16x32_bf16 v[88:91], a[24:27], a[40:43], v[88:91]\n"
      "ds_read_b128 a[104:107], v201 offset:512\n"
      "v_mfma_f32_16x16x32_bf16 v[120:123], a[24:27], a[44:47], v[120:123]\n"
      "buffer_load_dwordx4 v249, s[40:43], s68 offen offset:2048  lds\n"
      "v_mfma_f32_16x16x32_bf16 v[152:155], a[24:27], a[48:51], v[152:155]\n"
      "buffer_load_dwordx4 v250, s[40:43], s68 offen offset:3072  lds\n"
      "v_mfma_f32_16x16x32_bf16 v[184:187], a[24:27], a[52:55], v[184:187]\n"
      "ds_read_b128 a[108:111], v201 offset:768\n"
      "v_mfma_f32_16x16x32_bf16 v[28:31], a[28:31], a[32:35], v[28:31]\n"
      "ds_read_b64_tr_b16 a[24:25], v243 offset:768\n"
      "v_mfma_f32_16x16x32_bf16 v[60:63], a[28:31], a[36:39], v[60:63]\n"
      "s_add_u32 s68, s68, 131072\n"
      "ds_read_b64_tr_b16 a[26:27], v243 offset:1792\n"
      "v_mfma_f32_16x16x32_bf16 v[92:95], a[28:31], a[40:43], v[92:95]\n"
      "v_mfma_f32_16x16x32_bf16 v[124:127], a[28:31], a[44:47], v[124:127]\n"
      "v_mfma_f32_16x16x32_bf16 v[156:159], a[28:31], a[48:51], v[156:159]\n"
      "s_cmp_eq_u32 s68, 5242880\n"
      "s_cselect_b32 s68, 0, s68\n"
      "v_mfma_f32_16x16x32_bf16 v[188:191], a[28:31], a[52:55], v[188:191]\n"
      "s_waitcnt lgkmcnt(14)\n"
      "v_mfma_f32_16x16x32_bf16 v[0:3], a[0:3], a[56:59], v[0:3]\n"
      "s_barrier\n"
      "ds_read_b64_tr_b16 a[28:29], v244 offset:768\n"
      "v_mfma_f32_16x16x32_bf16 v[32:35], a[0:3], a[60:63], v[32:35]\n"
      "s_add_u32 m0, s81, 139264\n"
      "s_add_u32 s73, s69, 0\n"
      "ds_read_b64_tr_b16 a[30:31], v244 offset:1792\n"
      "v_mfma_f32_16x16x32_bf16 v[64:67], a[0:3], a[64:67], v[64:67]\n"
      "buffer_load_dwordx4 v252, s[36:39], s73 offen offset:0  lds\n"
      "v_mfma_f32_16x16x32_bf16 v[96:99], a[0:3], a[68:71], v[96:99]\n"
      "s_waitcnt lgkmcnt(13)\n"
      "s_add_u32 m0, s81, 143360\n"
      "v_mfma_f32_16x16x32_bf16 v[128:131], a[0:3], a[72:75], v[128:131]\n"
      "s_waitcnt lgkmcnt(8)\n"
      "s_add_u32 s73, s69, 40960\n"
      "ds_read_b128 a[32:35], v251 offset:0\n"
      "v_mfma_f32_16x16x32_bf16 v[160:163], a[0:3], a[76:79], v[160:163]\n"
      "buffer_load_dwordx4 v252, s[36:39], s73 offen offset:0  lds\n"
      "v_mfma_f32_16x16x32_bf16 v[4:7], a[4:7], a[56:59], v[4:7]\n"
      "s_waitcnt vmcnt(10)\n"
      "s_add_u32 m0, s81, 147456\n"
      "ds_read_b64_tr_b16 a[0:1], v243 offset:32768\n"
      "v_mfma_f32_16x16x32_bf16 v[36:39], a[4:7], a[60:63], v[36:39]\n"
      "s_add_u32 s73, s69, 81920\n"
      "ds_read_b64_tr_b16 a[2:3], v243 offset:33792\n"
      "v_mfma_f32_16x16x32_bf16 v[68:71], a[4:7], a[64:67], v[68:71]\n"
      "buffer_load_dwordx4 v252, s[36:39], s73 offen offset:0  lds\n"
      "v_mfma_f32_16x16x32_bf16 v[100:103], a[4:7], a[68:71], v[100:103]\n"
      "s_add_u32 s69, s69, 128\n"
      "s_cmp_eq_u32 s69, 1280\n"
      "s_cselect_b32 s69, 0, s69\n"
      "v_mfma_f32_16x16x32_bf16 v[132:135], a[4:7], a[72:75], v[132:135]\n"
      "s_mov_b32 m0, s78\n"
      "ds_read_b128 a[36:39], v251 offset:2048\n"
      "v_mfma_f32_16x16x32_bf16 v[164:167], a[4:7], a[76:79], v[164:167]\n"
      "s_waitcnt lgkmcnt(8)\n"
      "buffer_load_dwordx4 v234, s[44:47], s70 offen offset:0 nt lds\n"
      "v_mfma_f32_16x16x32_bf16 v[8:11], a[8:11], a[56:59], v[8:11]\n"
      "ds_read_b64_tr_b16 a[4:5], v244 offset:32768\n"
      "v_mfma_f32_16x16x32_bf16 v[40:43], a[8:11], a[60:63], v[40:43]\n"
      "ds_read_b64_tr_b16 a[6:7], v244 offset:33792\n"
      "v_mfma_f32_16x16x32_bf16 v[72:75], a[8:11], a[64:67], v[72:75]\n"
      "buffer_load_dwordx4 v234, s[44:47], s70 offen offset:1024 nt lds\n"
      "v_mfma_f32_16x16x32_bf16 v[104:107], a[8:11], a[68:71], v[104:107]\n"
      "buffer_load_dwordx4 v235, s[44:47], s70 offen offset:2048 nt lds\n"
      "v_mfma_f32_16x16x32_bf16 v[136:139], a[8:11], a[72:75], v[136:139]\n"
      "ds_read_b128 a[40:43], v251 offset:4096\n"
      "v_mfma_f32_16x16x32_bf16 v[168:171], a[8:11], a[76:79], v[168:171]\n"
      "buffer_load_dwordx4 v235, s[44:47], s70 offen offset:3072 nt lds\n"
      "s_add_u32 s70, s70, %16\n"
      "s_and_b32 s70, s70, 0x1ffff\n"
      "v_mfma_f32_16x16x32_bf16 v[12:15], a[12:15], a[56:59], v[12:15]\n"
      "ds_read_b64_tr_b16 a[8:9], v243 offset:33024\n"
      "v_mfma_f32_16x16x32_bf16 v[44:47], a[12:15], a[60:63], v[44:47]\n"
      "s_add_u32 m0, s72, 0\n"
      "ds_read_b64_tr_b16 a[10:11], v243 offset:34048\n"
      "v_mfma_f32_16x16x32_bf16 v[76:79], a[12:15], a[64:67], v[76:79]\n"
      "v_mfma_f32_16x16x32_bf16 v[108:111], a[12:15], a[68:71], v[108:111]\n"
      "s_waitcnt lgkmcnt(9)\n"
      "buffer_load_dwordx4 v247, s[40:43], s68 offen offset:0  lds\n"
      "v_mfma_f32_16x16x32_bf16 v[140:143], a[12:15], a[72:75], v[140:143]\n"
      "ds_read_b128 a[44:47], v251 offset:6144\n"
      "v_mfma_f32_16x16x32_bf16 v[172:175], a[12:15], a[76:79], v[172:175]\n"
      "buffer_load_dwordx4 v248, s[40:43], s68 offen offset:1024  lds\n"
      "v_mfma_f32_16x16x32_bf16 v[16:19], a[16:19], a[56:59], v[16:19]\n"
      "ds_read_b64_tr_b16 a[12:13], v244 offset:33024\n"
      "v_mfma_f32_16x16x32_bf16 v[48:51], a[16:19], a[60:63], v[48:51]\n"
      "ds_read_b64_tr_b16 a[14:15], v244 offset:34048\n"
      "v_mfma_f32_16x16x32_bf16 v[80:83], a[16:19], a[64:67], v[80:83]\n"
      "buffer_load_dwordx4 v249, s[40:43], s68 offen offset:2048  lds\n"
      "v_mfma_f32_16x16x32_bf16 v[112:115], a[16:19], a[68:71], v[112:115]\n"
      "buffer_load_dwordx4 v250, s[40:43], s68 offen offset:3072  lds\n"
      "v_mfma_f32_16x16x32_bf16 v[144:147], a[16:19], a[72:75], v[144:147]\n"
      "ds_read_b128 a[48:51], v251 offset:8192\n"
      "v_mfma_f32_16x16x32_bf16 v[176:179], a[16:19], a[76:79], v[176:179]\n"
      "v_mfma_f32_16x16x32_bf16 v[20:23], a[20:23], a[56:59], v[20:23]\n"
      "s_add_u32 m0, s72, 4096\n"
      "ds_read_b64_tr_b16 a[16:17], v243 offset:33280\n"
      "v_mfma_f32_16x16x32_bf16 v[52:55], a[20:23], a[60:63], v[52:55]\n"
      "ds_read_b64_tr_b16 a[18:19], v243 offset:34304\n"
      "v_mfma_f32_16x16x32_bf16 v[84:87], a[20:23], a[64:67], v[84:87]\n"
      "s_add_u32 s68, s68, 131072\n"
      "v_mfma_f32_16x16x32_bf16 v[116:119], a[20:23], a[68:71], v[116:119]\n"
      "v_mfma_f32_16x16x32_bf16 v[148:151], a[20:23], a[72:75], v[148:151]\n"
      "ds_read_b128 a[52:55], v251 offset:10240\n"
      "v_mfma_f32_16x16x32_bf16 v[180:183], a[20:23], a[76:79], v[180:183]\n"
      "buffer_load_dwordx4 v247, s[40:43], s68 offen offset:0  lds\n"
      "v_mfma_f32_16x16x32_bf16 v[24:27], a[24:27], a[56:59], v[24:27]\n"
      "ds_read_b64_tr_b16 a[20:21], v244 offset:33280\n"
      "v_mfma_f32_16x16x32_bf16 v[56:59], a[24:27], a[60:63], v[56:59]\n"
      "ds_read_b64_tr_b16 a[22:23], v244 offset:34304\n"
      "v_mfma_f32_16x16x32_bf16 v[88:91], a[24:27], a[64:67], v[88:91]\n"
      "buffer_load_dwordx4 v248, s[40:43], s68 offen offset:1024  lds\n"
      "v_mfma_f32_16x16x32_bf16 v[120:123], a[24:27], a[68:71], v[120:123]\n"
      "buffer_load_dwordx4 v249, s[40:43], s68 offen offset:2048  lds\n"
      "v_mfma_f32_16x16x32_bf16 v[152:155], a[24:27], a[72:75], v[152:155]\n"
      "v_mfma_f32_16x16x32_bf16 v[184:187], a[24:27], a[76:79], v[184:187]\n"
      "buffer_load_dwordx4 v250, s[40:43], s68 offen offset:3072  lds\n"
      "v_mfma_f32_16x16x32_bf16 v[28:31], a[28:31], a[56:59], v[28:31]\n"
      "ds_read_b64_tr_b16 a[24:25], v243 offset:33536\n"
      "v_mfma_f32_16x16x32_bf16 v[60:63], a[28:31], a[60:63], v[60:63]\n"
      "ds_read_b64_tr_b16 a[26:27], v243 offset:34560\n"
      "v_mfma_f32_16x16x32_bf16 v[92:95], a[28:31], a[64:67], v[92:95]\n"
      "s_add_u32 s68, s68, 131072\n"
      "v_mfma_f32_16x16x32_bf16 v[124:127], a[28:31], a[68:71], v[124:127]\n"
      "v_mfma_f32_16x16x32_bf16 v[156:159], a[28:31], a[72:75], v[156:159]\n"
      "s_cmp_eq_u32 s68, 5242880\n"
      "s_cselect_b32 s68, 0, s68\n"
      "v_mfma_f32_16x16x32_bf16 v[188:191], a[28:31], a[76:79], v[188:191]\n"
      "s_waitcnt lgkmcnt(14)\n"
      "v_mfma_f32_16x16x32_bf16 v[0:3], a[0:3], a[32:35], v[0:3]\n"
      "ds_read_b64_tr_b16 a[28:29], v244 offset:33536\n"
      "v_mfma_f32_16x16x32_bf16 v[32:35], a[0:3], a[36:39], v[32:35]\n"
      "s_waitcnt lgkmcnt(14)\n"
      "ds_read_b64_tr_b16 a[30:31], v244 offset:34560\n"
      "v_mfma_f32_16x16x32_bf16 v[64:67], a[0:3], a[40:43], v[64:67]\n"
      "s_waitcnt lgkmcnt(12)\n"
      "v_mfma_f32_16x16x32_bf16 v[96:99], a[0:3], a[44:47], v[96:99]\n"
      "s_waitcnt lgkmcnt(9)\n"
      "v_mfma_f32_16x16x32_bf16 v[128:131], a[0:3], a[48:51], v[128:131]\n"
      "s_waitcnt lgkmcnt(6)\n"
      "ds_read_b128 a[56:59], v251 offset:512\n"
      "v_mfma_f32_16x16x32_bf16 v[160:163], a[0:3], a[52:55], v[160:163]\n"
      "v_mfma_f32_16x16x32_bf16 v[4:7], a[4:7], a[32:35], v[4:7]\n"
      "s_waitcnt vmcnt(15)\n"
      "s_lshl_b32 s90, %13, 2\n"
      "ds_read_b64_tr_b16 a[0:1], v245 offset:0\n"
      "v_mfma_f32_16x16x32_bf16 v[36:39], a[4:7], a[36:39], v[36:39]\n"
      "ds_read_b64_tr_b16 a[2:3], v245 offset:1024\n"
      "v_mfma_f32_16x16x32_bf16 v[68:71], a[4:7], a[40:43], v[68:71]\n"
      "v_mfma_f32_16x16x32_bf16 v[100:103], a[4:7], a[44:47], v[100:103]\n"
      "s_add_u32 m0, s72, 32768\n"
      "v_mfma_f32_16x16x32_bf16 v[132:135], a[4:7], a[48:51], v[132:135]\n"
      "ds_read_b128 a[60:63], v251 offset:2560\n"
      "v_mfma_f32_16x16x32_bf16 v[164:167], a[4:7], a[52:55], v[164:167]\n"
      "s_waitcnt lgkmcnt(4)\n"
      "buffer_load_dwordx4 v247, s[40:43], s68 offen offset:0  lds\n"
      "v_mfma_f32_16x16x32_bf16 v[8:11], a[8:11], a[32:35], v[8:11]\n"
      "ds_read_b64_tr_b16 a[4:5], v246 offset:0\n"
      "v_mfma_f32_16x16x32_bf16 v[40:43], a[8:11], a[36:39], v[40:43]\n"
      "ds_read_b64_tr_b16 a[6:7], v246 offset:1024\n"
      "v_mfma_f32_16x16x32_bf16 v[72:75], a[8:11], a[40:43], v[72:75]\n"
      "v_mfma_f32_16x16x32_bf16 v[104:107], a[8:11], a[44:47], v[104:107]\n"
      "s_add_u32 s90, s90, 256\n"
      "buffer_load_dwordx4 v248, s[40:43], s68 offen offset:1024  lds\n"
      "v_mfma_f32_16x16x32_bf16 v[136:139], a[8:11], a[48:51], v[136:139]\n"
      "ds_read_b128 a[64:67], v251 offset:4608\n"
      "v_mfma_f32_16x16x32_bf16 v[168:171], a[8:11], a[52:55], v[168:171]\n"
      "buffer_load_dwordx4 v249, s[40:43], s68 offen offset:2048  lds\n"
      "v_mfma_f32_16x16x32_bf16 v[12:15], a[12:15], a[32:35], v[12:15]\n"
      "ds_read_b64_tr_b16 a[8:9], v245 offset:256\n"
      "v_mfma_f32_16x16x32_bf16 v[44:47], a[12:15], a[36:39], v[44:47]\n"
      "ds_read_b64_tr_b16 a[10:11], v245 offset:1280\n"
      "v_mfma_f32_16x16x32_bf16 v[76:79], a[12:15], a[40:43], v[76:79]\n"
      "v_mfma_f32_16x16x32_bf16 v[108:111], a[12:15], a[44:47], v[108:111]\n"
      "buffer_load_dwordx4 v250, s[40:43], s68 offen offset:3072  lds\n"
      "v_mfma_f32_16x16x32_bf16 v[140:143], a[12:15], a[48:51], v[140:143]\n"
      "ds_read_b128 a[68:71], v251 offset:6656\n"
      "v_mfma_f32_16x16x32_bf16 v[172:175], a[12:15], a[52:55], v[172:175]\n"
      "v_and_b32 v199, 7, v192\n"
      "v_mfma_f32_16x16x32_bf16 v[16:19], a[16:19], a[32:35], v[16:19]\n"
      "s_add_u32 m0, s72, 36864\n"
      "ds_read_b64_tr_b16 a[12:13], v246 offset:256\n"
      "v_mfma_f32_16x16x32_bf16 v[48:51], a[16:19], a[36:39], v[48:51]\n"
      "ds_read_b64_tr_b16 a[14:15], v246 offset:1280\n"
      "v_mfma_f32_16x16x32_bf16 v[80:83], a[16:19], a[40:43], v[80:83]\n"
      "v_mfma_f32_16x16x32_bf16 v[112:115], a[16:19], a[44:47], v[112:115]\n"
      "s_add_u32 s68, s68, 131072\n"
      "v_mfma_f32_16x16x32_bf16 v[144:147], a[16:19], a[48:51], v[144:147]\n"
      "ds_read_b128 a[72:75], v251 offset:8704\n"
      "v_mfma_f32_16x16x32_bf16 v[176:179], a[16:19], a[52:55], v[176:179]\n"
      "s_waitcnt vmcnt(12)\n"
      "ds_read_b128 a[112:115], v201 offset:0\n"
      "v_mfma_f32_16x16x32_bf16 v[20:23], a[20:23], a[32:35], v[20:23]\n"
      "ds_read_b64_tr_b16 a[16:17], v245 offset:512\n"
      "v_mfma_f32_16x16x32_bf16 v[52:55], a[20:23], a[36:39], v[52:55]\n"
      "ds_read_b64_tr_b16 a[18:19], v245 offset:1536\n"
      "v_mfma_f32_16x16x32_bf16 v[84:87], a[20:23], a[40:43], v[84:87]\n"
      "buffer_load_dwordx4 v247, s[40:43], s68 offen offset:0  lds\n"
      "v_lshl_add_u32 v199, v199, 6, s90\n"
      "v_mfma_f32_16x16x32_bf16 v[116:119], a[20:23], a[44:47], v[116:119]\n"
      "ds_read_b128 a[116:119], v201 offset:256\n"
      "v_mfma_f32_16x16x32_bf16 v[148:151], a[20:23], a[48:51], v[148:151]\n"
      "ds_read_b128 a[76:79], v251 offset:10752\n"
      "v_mfma_f32_16x16x32_bf16 v[180:183], a[20:23], a[52:55], v[180:183]\n"
      "buffer_load_dwordx4 v248, s[40:43], s68 offen offset:1024  lds\n"
      "v_mfma_f32_16x16x32_bf16 v[24:27], a[24:27], a[32:35], v[24:27]\n"
      "ds_read_b64_tr_b16 a[20:21], v246 offset:512\n"
      "v_mfma_f32_16x16x32_bf16 v[56:59], a[24:27], a[36:39], v[56:59]\n"
      "ds_read_b64_tr_b16 a[22:23], v246 offset:1536\n"
      "v_mfma_f32_16x16x32_bf16 v[88:91], a[24:27], a[40:43], v[88:91]\n"
      "ds_read_b128 a[120:123], v201 offset:512\n"
      "v_mfma_f32_16x16x32_bf16 v[120:123], a[24:27], a[44:47], v[120:123]\n"
      "buffer_load_dwordx4 v249, s[40:43], s68 offen offset:2048  lds\n"
      "v_mfma_f32_16x16x32_bf16 v[152:155], a[24:27], a[48:51], v[152:155]\n"
      "buffer_load_dwordx4 v250, s[40:43], s68 offen offset:3072  lds\n"
      "v_mfma_f32_16x16x32_bf16 v[184:187], a[24:27], a[52:55], v[184:187]\n"
      "ds_read_b128 a[124:127], v201 offset:768\n"
      "v_mfma_f32_16x16x32_bf16 v[28:31], a[28:31], a[32:35], v[28:31]\n"
      "ds_read_b64_tr_b16 a[24:25], v245 offset:768\n"
      "v_mfma_f32_16x16x32_bf16 v[60:63], a[28:31], a[36:39], v[60:63]\n"
      "s_add_u32 s68, s68, 131072\n"
      "ds_read_b64_tr_b16 a[26:27], v245 offset:1792\n"
      "v_mfma_f32_16x16x32_bf16 v[92:95], a[28:31], a[40:43], v[92:95]\n"
      "buffer_load_dword v198, v199, s[64:67], 0 offen offset:0 sc1\n"
      "v_mfma_f32_16x16x32_bf16 v[124:127], a[28:31], a[44:47], v[124:127]\n"
      "v_mfma_f32_16x16x32_bf16 v[156:159], a[28:31], a[48:51], v[156:159]\n"
      "s_cmp_eq_u32 s68, 5242880\n"
      "s_cselect_b32 s68, 0, s68\n"
      "v_mfma_f32_16x16x32_bf16 v[188:191], a[28:31], a[52:55], v[188:191]\n"
      "s_waitcnt lgkmcnt(14)\n"
      "v_mfma_f32_16x16x32_bf16 v[0:3], a[0:3], a[56:59], v[0:3]\n"
      "s_barrier\n"
      "ds_read_b64_tr_b16 a[28:29], v246 offset:768\n"
      "v_mfma_f32_16x16x32_bf16 v[32:35], a[0:3], a[60:63], v[32:35]\n"
      "s_add_u32 m0, s81, 151552\n"
      "s_add_u32 s73, s69, 0\n"
      "ds_read_b64_tr_b16 a[30:31], v246 offset:1792\n"
      "v_mfma_f32_16x16x32_bf16 v[64:67], a[0:3], a[64:67], v[64:67]\n"
      "buffer_load_dwordx4 v252, s[36:39], s73 offen offset:0  lds\n"
      "v_mfma_f32_16x16x32_bf16 v[96:99], a[0:3], a[68:71], v[96:99]\n"
      "s_waitcnt lgkmcnt(13)\n"
      "s_add_u32 m0, s81, 155648\n"
      "v_mfma_f32_16x16x32_bf16 v[128:131], a[0:3], a[72:75], v[128:131]\n"
      "s_waitcnt lgkmcnt(8)\n"
      "s_add_u32 s73, s69, 40960\n"
      "ds_read_b128 a[32:35], v251 offset:12288\n"
      "v_mfma_f32_16x16x32_bf16 v[160:163], a[0:3], a[76:79], v[160:163]\n"
      "buffer_load_dwordx4 v252, s[36:39], s73 offen offset:0  lds\n"
      "v_mfma_f32_16x16x32_bf16 v[4:7], a[4:7], a[56:59], v[4:7]\n"
      "s_waitcnt vmcnt(11)\n"
      "s_add_u32 m0, s81, 159744\n"
      "ds_read_b64_tr_b16 a[0:1], v243 offset:0\n"
      "v_mfma_f32_16x16x32_bf16 v[36:39], a[4:7], a[60:63], v[36:39]\n"
      "s_add_u32 s73, s69, 81920\n"
      "ds_read_b64_tr_b16 a[2:3], v243 offset:1024\n"
      "v_mfma_f32_16x16x32_bf16 v[68:71], a[4:7], a[64:67], v[68:71]\n"
      "buffer_load_dwordx4 v252, s[36:39], s73 offen offset:0  lds\n"
      "v_mfma_f32_16x16x32_bf16 v[100:103], a[4:7], a[68:71], v[100:103]\n"
      "s_add_u32 s69, s69, 128\n"
      "s_cmp_eq_u32 s69, 1280\n"
      "s_cselect_b32 s69, 0, s69\n"
      "v_mfma_f32_16x16x32_bf16 v[132:135], a[4:7], a[72:75], v[132:135]\n"
      "s_mov_b32 m0, s78\n"
      "ds_read_b128 a[36:39], v251 offset:14336\n"
      "v_mfma_f32_16x16x32_bf16 v[164:167], a[4:7], a[76:79], v[164:167]\n"
      "s_waitcnt lgkmcnt(8)\n"
      "buffer_load_dwordx4 v234, s[48:51], s71 offen offset:0 nt lds\n"
      "v_mfma_f32_16x16x32_bf16 v[8:11], a[8:11], a[56:59], v[8:11]\n"
      "ds_read_b64_tr_b16 a[4:5], v244 offset:0\n"
      "v_mfma_f32_16x16x32_bf16 v[40:43], a[8:11], a[60:63], v[40:43]\n"
      "ds_read_b64_tr_b16 a[6:7], v244 offset:1024\n"
      "v_mfma_f32_16x16x32_bf16 v[72:75], a[8:11], a[64:67], v[72:75]\n"
      "buffer_load_dwordx4 v236, s[48:51], s71 offen offset:1024 nt lds\n"
      "v_mfma_f32_16x16x32_bf16 v[104:107], a[8:11], a[68:71], v[104:107]\n"
      "buffer_load_dwordx4 v234, s[48:51], s71 offen offset:2048 nt lds\n"
      "v_mfma_f32_16x16x32_bf16 v[136:139], a[8:11], a[72:75], v[136:139]\n"
      "ds_read_b128 a[40:43], v251 offset:16384\n"
      "v_mfma_f32_16x16x32_bf16 v[168:171], a[8:11], a[76:79], v[168:171]\n"
      "buffer_load_dwordx4 v236, s[48:51], s71 offen offset:3072 nt lds\n"
      "s_add_u32 s71, s71, %16\n"
      "s_and_b32 s71, s71, 0x1ffff\n"
      "v_mfma_f32_16x16x32_bf16 v[12:15], a[12:15], a[56:59], v[12:15]\n"
      "ds_read_b64_tr_b16 a[8:9], v243 offset:256\n"
      "v_mfma_f32_16x16x32_bf16 v[44:47], a[12:15], a[60:63], v[44:47]\n"
      "s_add_u32 m0, s72, 65536\n"
      "ds_read_b64_tr_b16 a[10:11], v243 offset:1280\n"
      "v_mfma_f32_16x16x32_bf16 v[76:79], a[12:15], a[64:67], v[76:79]\n"
      "v_mfma_f32_16x16x32_bf16 v[108:111], a[12:15], a[68:71], v[108:111]\n"
      "s_waitcnt lgkmcnt(9)\n"
      "buffer_load_dwordx4 v247, s[40:43], s68 offen offset:0  lds\n"
      "v_mfma_f32_16x16x32_bf16 v[140:143], a[12:15], a[72:75], v[140:143]\n"
      "ds_read_b128 a[44:47], v251 offset:18432\n"
      "v_mfma_f32_16x16x32_bf16 v[172:175], a[12:15], a[76:79], v[172:175]\n"
      "buffer_load_dwordx4 v248, s[40:43], s68 offen offset:1024  lds\n"
      "v_mfma_f32_16x16x32_bf16 v[16:19], a[16:19], a[56:59], v[16:19]\n"
      "ds_read_b64_tr_b16 a[12:13], v244 offset:256\n"
      "v_mfma_f32_16x16x32_bf16 v[48:51], a[16:19], a[60:63], v[48:51]\n"
      "ds_read_b64_tr_b16 a[14:15], v244 offset:1280\n"
      "v_mfma_f32_16x16x32_bf16 v[80:83], a[16:19], a[64:67], v[80:83]\n"
      "buffer_load_dwordx4 v249, s[40:43], s68 offen offset:2048  lds\n"
      "v_mfma_f32_16x16x32_bf16 v[112:115], a[16:19], a[68:71], v[112:115]\n"
      "buffer_load_dwordx4 v250, s[40:43], s68 offen offset:3072  lds\n"
      "v_mfma_f32_16x16x32_bf16 v[144:147], a[16:19], a[72:75], v[144:147]\n"
      "ds_read_b128 a[48:51], v251 offset:20480\n"
      "v_mfma_f32_16x16x32_bf16 v[176:179], a[16:19], a[76:79], v[176:179]\n"
      "v_mfma_f32_16x16x32_bf16 v[20:23], a[20:23], a[56:59], v[20:23]\n"
      "s_add_u32 m0, s72, 69632\n"
      "ds_read_b64_tr_b16 a[16:17], v243 offset:512\n"
      "v_mfma_f32_16x16x32_bf16 v[52:55], a[20:23], a[60:63], v[52:55]\n"
      "ds_read_b64_tr_b16 a[18:19], v243 offset:1536\n"
      "v_mfma_f32_16x16x32_bf16 v[84:87], a[20:23], a[64:67], v[84:87]\n"
      "s_add_u32 s68, s68, 131072\n"
      "v_mfma_f32_16x16x32_bf16 v[116:119], a[20:23], a[68:71], v[116:119]\n"
      "v_mfma_f32_16x16x32_bf16 v[148:151], a[20:23], a[72:75], v[148:151]\n"
      "ds_read_b128 a[52:55], v251 offset:22528\n"
      "v_mfma_f32_16x16x32_bf16 v[180:183], a[20:23], a[76:79], v[180:183]\n"
      "buffer_load_dwordx4 v247, s[40:43], s68 offen offset:0  lds\n"
      "v_mfma_f32_16x16x32_bf16 v[24:27], a[24:27], a[56:59], v[24:27]\n"
      "ds_read_b64_tr_b16 a[20:21], v244 offset:512\n"
      "v_mfma_f32_16x16x32_bf16 v[56:59], a[24:27], a[60:63], v[56:59]\n"
      "ds_read_b64_tr_b16 a[22:23], v244 offset:1536\n"
      "v_mfma_f32_16x16x32_bf16 v[88:91], a[24:27], a[64:67], v[88:91]\n"
      "buffer_load_dwordx4 v248, s[40:43], s68 offen offset:1024  lds\n"
      "v_mfma_f32_16x16x32_bf16 v[120:123], a[24:27], a[68:71], v[120:123]\n"
      "buffer_load_dwordx4 v249, s[40:43], s68 offen offset:2048  lds\n"
      "v_mfma_f32_16x16x32_bf16 v[152:155], a[24:27], a[72:75], v[152:155]\n"
      "v_mfma_f32_16x16x32_bf16 v[184:187], a[24:27], a[76:79], v[184:187]\n"
      "buffer_load_dwordx4 v250, s[40:43], s68 offen offset:3072  lds\n"
      "v_mfma_f32_16x16x32_bf16 v[28:31], a[28:31], a[56:59], v[28:31]\n"
      "ds_read_b64_tr_b16 a[24:25], v243 offset:768\n"
      "v_mfma_f32_16x16x32_bf16 v[60:63], a[28:31], a[60:63], v[60:63]\n"
      "ds_read_b64_tr_b16 a[26:27], v243 offset:1792\n"
      "v_mfma_f32_16x16x32_bf16 v[92:95], a[28:31], a[64:67], v[92:95]\n"
      "s_add_u32 s68, s68, 131072\n"
      "v_mfma_f32_16x16x32_bf16 v[124:127], a[28:31], a[68:71], v[124:127]\n"
      "v_mfma_f32_16x16x32_bf16 v[156:159], a[28:31], a[72:75], v[156:159]\n"
      "s_cmp_eq_u32 s68, 5242880\n"
      "s_cselect_b32 s68, 0, s68\n"
      "v_mfma_f32_16x16x32_bf16 v[188:191], a[28:31], a[76:79], v[188:191]\n"
      "s_waitcnt lgkmcnt(14)\n"
      "v_mfma_f32_16x16x32_bf16 v[0:3], a[0:3], a[32:35], v[0:3]\n"
      "ds_read_b64_tr_b16 a[28:29], v244 offset:768\n"
      "v_mfma_f32_16x16x32_bf16 v[32:35], a[0:3], a[36:39], v[32:35]\n"
      "s_waitcnt lgkmcnt(14)\n"
      "ds_read_b64_tr_b16 a[30:31], v244 offset:1792\n"
      "v_mfma_f32_16x16x32_bf16 v[64:67], a[0:3], a[40:43], v[64:67]\n"
      "s_waitcnt lgkmcnt(12)\n"
      "v_mfma_f32_16x16x32_bf16 v[96:99], a[0:3], a[44:47], v[96:99]\n"
      "s_waitcnt lgkmcnt(9)\n"
      "v_mfma_f32_16x16x32_bf16 v[128:131], a[0:3], a[48:51], v[128:131]\n"
      "s_waitcnt lgkmcnt(6)\n"
      "ds_read_b128 a[56:59], v251 offset:12800\n"
      "v_mfma_f32_16x16x32_bf16 v[160:163], a[0:3], a[52:55], v[160:163]\n"
      "v_mfma_f32_16x16x32_bf16 v[4:7], a[4:7], a[32:35], v[4:7]\n"
      "s_waitcnt vmcnt(16)\n"
      "ds_read_b64_tr_b16 a[0:1], v243 offset:32768\n"
      "v_and_b32 v199, 7, v192\n"
      "v_mfma_f32_16x16x32_bf16 v[36:39], a[4:7], a[36:39], v[36:39]\n"
      "ds_read_b64_tr_b16 a[2:3], v243 offset:33792\n"
      "v_mfma_f32_16x16x32_bf16 v[68:71], a[4:7], a[40:43], v[68:71]\n"
      "v_mfma_f32_16x16x32_bf16 v[100:103], a[4:7], a[44:47], v[100:103]\n"
      "s_add_u32 m0, s72, 0\n"
      "v_mfma_f32_16x16x32_bf16 v[132:135], a[4:7], a[48:51], v[132:135]\n"
      "ds_read_b128 a[60:63], v251 offset:14848\n"
      "v_mfma_f32_16x16x32_bf16 v[164:167], a[4:7], a[52:55], v[164:167]\n"
      "s_waitcnt lgkmcnt(4)\n"
      "buffer_load_dwordx4 v247, s[40:43], s68 offen offset:0  lds\n"
      "v_mfma_f32_16x16x32_bf16 v[8:11], a[8:11], a[32:35], v[8:11]\n"
      "ds_read_b64_tr_b16 a[4:5], v244 offset:32768\n"
      "v_mfma_f32_16x16x32_bf16 v[40:43], a[8:11], a[36:39], v[40:43]\n"
      "ds_read_b64_tr_b16 a[6:7], v244 offset:33792\n"
      "v_mfma_f32_16x16x32_bf16 v[72:75], a[8:11], a[40:43], v[72:75]\n"
      "v_mfma_f32_16x16x32_bf16 v[104:107], a[8:11], a[44:47], v[104:107]\n"
      "buffer_load_dwordx4 v248, s[40:43], s68 offen offset:1024  lds\n"
      "v_lshlrev_b32 v199, 2, v199\n"
      "v_mfma_f32_16x16x32_bf16 v[136:139], a[8:11], a[48:51], v[136:139]\n"
      "ds_read_b128 a[64:67], v251 offset:16896\n"
      "v_mfma_f32_16x16x32_bf16 v[168:171], a[8:11], a[52:55], v[168:171]\n"
      "buffer_load_dwordx4 v249, s[40:43], s68 offen offset:2048  lds\n"
      "v_mfma_f32_16x16x32_bf16 v[12:15], a[12:15], a[32:35], v[12:15]\n"
      "ds_read_b64_tr_b16 a[8:9], v243 offset:33024\n"
      "v_mfma_f32_16x16x32_bf16 v[44:47], a[12:15], a[36:39], v[44:47]\n"
      "ds_read_b64_tr_b16 a[10:11], v243 offset:34048\n"
      "v_mfma_f32_16x16x32_bf16 v[76:79], a[12:15], a[40:43], v[76:79]\n"
      "v_mfma_f32_16x16x32_bf16 v[108:111], a[12:15], a[44:47], v[108:111]\n"
      "buffer_load_dwordx4 v250, s[40:43], s68 offen offset:3072  lds\n"
      "v_mfma_f32_16x16x32_bf16 v[140:143], a[12:15], a[48:51], v[140:143]\n"
      "s_waitcnt vmcnt(19)\n"
      "ds_read_b128 a[68:71], v251 offset:18944\n"
      "v_cmp_ne_u32 vcc, s89, v198\n"
      "v_mfma_f32_16x16x32_bf16 v[172:175], a[12:15], a[52:55], v[172:175]\n"
      "s_mov_b64 s[94:95], vcc\n"
      "buffer_load_dword v198, v199, s[64:67], 0 offen offset:0 sc1\n"
      "v_mfma_f32_16x16x32_bf16 v[16:19], a[16:19], a[32:35], v[16:19]\n"
      "s_add_u32 m0, s72, 4096\n"
      "ds_read_b64_tr_b16 a[12:13], v244 offset:33024\n"
      "v_mfma_f32_16x16x32_bf16 v[48:51], a[16:19], a[36:39], v[48:51]\n"
      "ds_read_b64_tr_b16 a[14:15], v244 offset:34048\n"
      "v_mfma_f32_16x16x32_bf16 v[80:83], a[16:19], a[40:43], v[80:83]\n"
      "s_add_u32 s68, s68, 131072\n"
      "buffer_load_dwordx4 v247, s[40:43], s68 offen offset:0  lds\n"
      "v_mfma_f32_16x16x32_bf16 v[112:115], a[16:19], a[44:47], v[112:115]\n"
      "buffer_load_dwordx4 v248, s[40:43], s68 offen offset:1024  lds\n"
      "v_mfma_f32_16x16x32_bf16 v[144:147], a[16:19], a[48:51], v[144:147]\n"
      "ds_read_b128 a[72:75], v251 offset:20992\n"
      "v_mfma_f32_16x16x32_bf16 v[176:179], a[16:19], a[52:55], v[176:179]\n"
      "s_waitcnt vmcnt(15)\n"
      "ds_read_b64_tr_b16 a[128:129], v202 offset:0\n"
      "v_mfma_f32_16x16x32_bf16 v[20:23], a[20:23], a[32:35], v[20:23]\n"
      "ds_read_b64_tr_b16 a[16:17], v243 offset:33280\n"
      "v_mfma_f32_16x16x32_bf16 v[52:55], a[20:23], a[36:39], v[52:55]\n"
      "ds_read_b64_tr_b16 a[18:19], v243 offset:34304\n"
      "v_mfma_f32_16x16x32_bf16 v[84:87], a[20:23], a[40:43], v[84:87]\n"
      "ds_read_b64_tr_b16 a[132:133], v203 offset:0\n"
      "v_mfma_f32_16x16x32_bf16 v[116:119], a[20:23], a[44:47], v[116:119]\n"
      "ds_read_b64_tr_b16 a[136:137], v202 offset:256\n"
      "v_mfma_f32_16x16x32_bf16 v[148:151], a[20:23], a[48:51], v[148:151]\n"
      "ds_read_b128 a[76:79], v251 offset:23040\n"
      "v_mfma_f32_16x16x32_bf16 v[180:183], a[20:23], a[52:55], v[180:183]\n"
      "ds_read_b64_tr_b16 a[140:141], v203 offset:256\n"
      "v_mfma_f32_16x16x32_bf16 v[24:27], a[24:27], a[32:35], v[24:27]\n"
      "ds_read_b64_tr_b16 a[20:21], v244 offset:33280\n"
      "v_mfma_f32_16x16x32_bf16 v[56:59], a[24:27], a[36:39], v[56:59]\n"
      "ds_read_b64_tr_b16 a[22:23], v244 offset:34304\n"
      "v_mfma_f32_16x16x32_bf16 v[88:91], a[24:27], a[40:43], v[88:91]\n"
      "ds_read_b64_tr_b16 a[144:145], v202 offset:512\n"
      "v_mfma_f32_16x16x32_bf16 v[120:123], a[24:27], a[44:47], v[120:123]\n"
      "ds_read_b64_tr_b16 a[148:149], v203 offset:512\n"
      "v_mfma_f32_16x16x32_bf16 v[152:155], a[24:27], a[48:51], v[152:155]\n"
      "ds_read_b64_tr_b16 a[152:153], v202 offset:768\n"
      "v_mfma_f32_16x16x32_bf16 v[184:187], a[24:27], a[52:55], v[184:187]\n"
      "ds_read_b64_tr_b16 a[156:157], v203 offset:768\n"
      "v_mfma_f32_16x16x32_bf16 v[28:31], a[28:31], a[32:35], v[28:31]\n"
      "ds_read_b64_tr_b16 a[24:25], v243 offset:33536\n"
      "v_mfma_f32_16x16x32_bf16 v[60:63], a[28:31], a[36:39], v[60:63]\n"
      "ds_read_b64_tr_b16 a[26:27], v243 offset:34560\n"
      "v_mfma_f32_16x16x32_bf16 v[92:95], a[28:31], a[40:43], v[92:95]\n"
      "buffer_load_dwordx4 v249, s[40:43], s68 offen offset:2048  lds\n"
      "v_mfma_f32_16x16x32_bf16 v[124:127], a[28:31], a[44:47], v[124:127]\n"
      "buffer_load_dwordx4 v250, s[40:43], s68 offen offset:3072  lds\n"
      "s_add_u32 s68, s68, 131072\n"
      "v_mfma_f32_16x16x32_bf16 v[156:159], a[28:31], a[48:51], v[156:159]\n"
      "s_cmp_eq_u32 s68, 5242880\n"
      "s_cselect_b32 s68, 0, s68\n"
      "v_mfma_f32_16x16x32_bf16 v[188:191], a[28:31], a[52:55], v[188:191]\n"
      "s_waitcnt lgkmcnt(14)\n"
      "v_mfma_f32_16x16x32_bf16 v[0:3], a[0:3], a[56:59], v[0:3]\n"
      "s_barrier\n"
      "ds_read_b64_tr_b16 a[28:29], v244 offset:33536\n"
      "v_mfma_f32_16x16x32_bf16 v[32:35], a[0:3], a[60:63], v[32:35]\n"
      "s_mov_b32 m0, s78\n"
      "ds_read_b64_tr_b16 a[30:31], v244 offset:34560\n"
      "v_mfma_f32_16x16x32_bf16 v[64:67], a[0:3], a[64:67], v[64:67]\n"
      "s_waitcnt lgkmcnt(4)\n"
      "buffer_load_dwordx4 v234, s[48:51], s71 offen offset:0 nt lds\n"
      "buffer_load_dwordx4 v236, s[48:51], s71 offen offset:1024 nt lds\n"
      "v_mfma_f32_16x16x32_bf16 v[96:99], a[0:3], a[68:71], v[96:99]\n"
      "buffer_load_dwordx4 v234, s[48:51], s71 offen offset:2048 nt lds\n"
      "v_mfma_f32_16x16x32_bf16 v[128:131], a[0:3], a[72:75], v[128:131]\n"
      "ds_read_b128 a[32:35], v251 offset:24576\n"
      "v_mfma_f32_16x16x32_bf16 v[160:163], a[0:3], a[76:79], v[160:163]\n"
      "buffer_load_dwordx4 v236, s[48:51], s71 offen offset:3072 nt lds\n"
      "s_add_u32 s71, s71, %16\n"
      "v_mfma_f32_16x16x32_bf16 v[4:7], a[4:7], a[56:59], v[4:7]\n"
      "s_waitcnt vmcnt(13)\n"
      "s_and_b32 s71, s71, 0x1ffff\n"
      "ds_read_b64_tr_b16 a[0:1], v245 offset:0\n"
      "v_mfma_f32_16x16x32_bf16 v[36:39], a[4:7], a[60:63], v[36:39]\n"
      "ds_read_b64_tr_b16 a[2:3], v245 offset:1024\n"
      "v_mfma_f32_16x16x32_bf16 v[68:71], a[4:7], a[64:67], v[68:71]\n"
      "v_mfma_f32_16x16x32_bf16 v[100:103], a[4:7], a[68:71], v[100:103]\n"
      "s_add_u32 m0, s72, 32768\n"
      "v_mfma_f32_16x16x32_bf16 v[132:135], a[4:7], a[72:75], v[132:135]\n"
      "ds_read_b128 a[36:39], v251 offset:26624\n"
      "v_mfma_f32_16x16x32_bf16 v[164:167], a[4:7], a[76:79], v[164:167]\n"
      "s_waitcnt lgkmcnt(4)\n"
      "buffer_load_dwordx4 v247, s[40:43], s68 offen offset:0  lds\n"
      "v_mfma_f32_16x16x32_bf16 v[8:11], a[8:11], a[56:59], v[8:11]\n"
      "ds_read_b64_tr_b16 a[4:5], v246 offset:0\n"
      "v_mfma_f32_16x16x32_bf16 v[40:43], a[8:11], a[60:63], v[40:43]\n"
      "ds_read_b64_tr_b16 a[6:7], v246 offset:1024\n"
      "v_mfma_f32_16x16x32_bf16 v[72:75], a[8:11], a[64:67], v[72:75]\n"
      "v_mfma_f32_16x16x32_bf16 v[104:107], a[8:11], a[68:71], v[104:107]\n"
      "buffer_load_dwordx4 v248, s[40:43], s68 offen offset:1024  lds\n"
      "v_mfma_f32_16x16x32_bf16 v[136:139], a[8:11], a[72:75], v[136:139]\n"
      "ds_read_b128 a[40:43], v251 offset:28672\n"
      "v_mfma_f32_16x16x32_bf16 v[168:171], a[8:11], a[76:79], v[168:171]\n"
      "buffer_load_dwordx4 v249, s[40:43], s68 offen offset:2048  lds\n"
      "v_mfma_f32_16x16x32_bf16 v[12:15], a[12:15], a[56:59], v[12:15]\n"
      "ds_read_b64_tr_b16 a[8:9], v245 offset:256\n"
      "v_mfma_f32_16x16x32_bf16 v[44:47], a[12:15], a[60:63], v[44:47]\n"
      "ds_read_b64_tr_b16 a[10:11], v245 offset:1280\n"
      "v_mfma_f32_16x16x32_bf16 v[76:79], a[12:15], a[64:67], v[76:79]\n"
      "v_mfma_f32_16x16x32_bf16 v[108:111], a[12:15], a[68:71], v[108:111]\n"
      "buffer_load_dwordx4 v250, s[40:43], s68 offen offset:3072  lds\n"
      "v_mfma_f32_16x16x32_bf16 v[140:143], a[12:15], a[72:75], v[140:143]\n"
      "ds_read_b128 a[44:47], v251 offset:30720\n"
      "v_mfma_f32_16x16x32_bf16 v[172:175], a[12:15], a[76:79], v[172:175]\n"
      "v_mfma_f32_16x16x32_bf16 v[16:19], a[16:19], a[56:59], v[16:19]\n"
      "s_add_u32 m0, s72, 36864\n"
      "ds_read_b64_tr_b16 a[12:13], v246 offset:256\n"
      "v_mfma_f32_16x16x32_bf16 v[48:51], a[16:19], a[60:63], v[48:51]\n"
      "ds_read_b64_tr_b16 a[14:15], v246 offset:1280\n"
      "v_mfma_f32_16x16x32_bf16 v[80:83], a[16:19], a[64:67], v[80:83]\n"
      "v_mfma_f32_16x16x32_bf16 v[112:115], a[16:19], a[68:71], v[112:115]\n"
      "s_add_u32 s68, s68, 131072\n"
      "v_mfma_f32_16x16x32_bf16 v[144:147], a[16:19], a[72:75], v[144:147]\n"
      "ds_read_b128 a[48:51], v251 offset:32768\n"
      "v_mfma_f32_16x16x32_bf16 v[176:179], a[16:19], a[76:79], v[176:179]\n"
      "v_mfma_f32_16x16x32_bf16 v[20:23], a[20:23], a[56:59], v[20:23]\n"
      "ds_read_b64_tr_b16 a[16:17], v245 offset:512\n"
      "v_mfma_f32_16x16x32_bf16 v[52:55], a[20:23], a[60:63], v[52:55]\n"
      "ds_read_b64_tr_b16 a[18:19], v245 offset:1536\n"
      "v_mfma_f32_16x16x32_bf16 v[84:87], a[20:23], a[64:67], v[84:87]\n"
      "buffer_load_dwordx4 v247, s[40:43], s68 offen offset:0  lds\n"
      "v_mfma_f32_16x16x32_bf16 v[116:119], a[20:23], a[68:71], v[116:119]\n"
      "buffer_load_dwordx4 v248, s[40:43], s68 offen offset:1024  lds\n"
      "v_mfma_f32_16x16x32_bf16 v[148:151], a[20:23], a[72:75], v[148:151]\n"
      "ds_read_b128 a[52:55], v251 offset:34816\n"
      "v_mfma_f32_16x16x32_bf16 v[180:183], a[20:23], a[76:79], v[180:183]\n"
      "v_mfma_f32_16x16x32_bf16 v[24:27], a[24:27], a[56:59], v[24:27]\n"
      "ds_read_b64_tr_b16 a[20:21], v246 offset:512\n"
      "v_mfma_f32_16x16x32_bf16 v[56:59], a[24:27], a[60:63], v[56:59]\n"
      "ds_read_b64_tr_b16 a[22:23], v246 offset:1536\n"
      "v_mfma_f32_16x16x32_bf16 v[88:91], a[24:27], a[64:67], v[88:91]\n"
      "buffer_load_dwordx4 v249, s[40:43], s68 offen offset:2048  lds\n"
      "v_mfma_f32_16x16x32_bf16 v[120:123], a[24:27], a[68:71], v[120:123]\n"
      "v_mfma_f32_16x16x32_bf16 v[152:155], a[24:27], a[72:75], v[152:155]\n"
      "buffer_load_dwordx4 v250, s[40:43], s68 offen offset:3072  lds\n"
      "v_mfma_f32_16x16x32_bf16 v[184:187], a[24:27], a[76:79], v[184:187]\n"
      "v_mfma_f32_16x16x32_bf16 v[28:31], a[28:31], a[56:59], v[28:31]\n"
      "ds_read_b64_tr_b16 a[24:25], v245 offset:768\n"
      "v_mfma_f32_16x16x32_bf16 v[60:63], a[28:31], a[60:63], v[60:63]\n"
      "s_add_u32 s68, s68, 131072\n"
      "ds_read_b64_tr_b16 a[26:27], v245 offset:1792\n"
      "v_mfma_f32_16x16x32_bf16 v[92:95], a[28:31], a[64:67], v[92:95]\n"
      "v_mfma_f32_16x16x32_bf16 v[124:127], a[28:31], a[68:71], v[124:127]\n"
      "v_mfma_f32_16x16x32_bf16 v[156:159], a[28:31], a[72:75], v[156:159]\n"
      "s_cmp_eq_u32 s68, 5242880\n"
      "s_cselect_b32 s68, 0, s68\n"
      "v_mfma_f32_16x16x32_bf16 v[188:191], a[28:31], a[76:79], v[188:191]\n"
      "s_waitcnt lgkmcnt(14)\n"
      "v_mfma_f32_16x16x32_bf16 v[0:3], a[0:3], a[32:35], v[0:3]\n"
      "ds_read_b64_tr_b16 a[28:29], v246 offset:768\n"
      "v_mfma_f32_16x16x32_bf16 v[32:35], a[0:3], a[36:39], v[32:35]\n"
      "s_waitcnt lgkmcnt(14)\n"
      "ds_read_b64_tr_b16 a[30:31], v246 offset:1792\n"
      "v_mfma_f32_16x16x32_bf16 v[64:67], a[0:3], a[40:43], v[64:67]\n"
      "s_waitcnt lgkmcnt(12)\n"
      "v_mfma_f32_16x16x32_bf16 v[96:99], a[0:3], a[44:47], v[96:99]\n"
      "s_waitcnt lgkmcnt(9)\n"
      "v_mfma_f32_16x16x32_bf16 v[128:131], a[0:3], a[48:51], v[128:131]\n"
      "s_waitcnt lgkmcnt(6)\n"
      "s_lshl_b32 s90, %13, 6\n"
      "ds_read_b128 a[56:59], v251 offset:25088\n"
      "v_mfma_f32_16x16x32_bf16 v[160:163], a[0:3], a[52:55], v[160:163]\n"
      "v_mfma_f32_16x16x32_bf16 v[4:7], a[4:7], a[32:35], v[4:7]\n"
      "s_waitcnt vmcnt(12)\n"
      "ds_read_b64_tr_b16 a[0:1], v243 offset:0\n"
      "v_mfma_f32_16x16x32_bf16 v[36:39], a[4:7], a[36:39], v[36:39]\n"
      "ds_read_b64_tr_b16 a[2:3], v243 offset:1024\n"
      "v_mfma_f32_16x16x32_bf16 v[68:71], a[4:7], a[40:43], v[68:71]\n"
      "v_mfma_f32_16x16x32_bf16 v[100:103], a[4:7], a[44:47], v[100:103]\n"
      "s_add_u32 m0, s72, 65536\n"
      "v_mfma_f32_16x16x32_bf16 v[132:135], a[4:7], a[48:51], v[132:135]\n"
      "s_add_u32 s90, s90, 256\n"
      "ds_read_b128 a[60:63], v251 offset:27136\n"
      "v_mfma_f32_16x16x32_bf16 v[164:167], a[4:7], a[52:55], v[164:167]\n"
      "s_waitcnt lgkmcnt(4)\n"
      "buffer_load_dwordx4 v247, s[40:43], s68 offen offset:0  lds\n"
      "v_mfma_f32_16x16x32_bf16 v[8:11], a[8:11], a[32:35], v[8:11]\n"
      "ds_read_b64_tr_b16 a[4:5], v244 offset:0\n"
      "v_mfma_f32_16x16x32_bf16 v[40:43], a[8:11], a[36:39], v[40:43]\n"
      "ds_read_b64_tr_b16 a[6:7], v244 offset:1024\n"
      "v_mfma_f32_16x16x32_bf16 v[72:75], a[8:11], a[40:43], v[72:75]\n"
      "v_mfma_f32_16x16x32_bf16 v[104:107], a[8:11], a[44:47], v[104:107]\n"
      "buffer_load_dwordx4 v248, s[40:43], s68 offen offset:1024  lds\n"
      "v_lshl_add_u32 v199, v192, 2, s90\n"
      "v_mfma_f32_16x16x32_bf16 v[136:139], a[8:11], a[48:51], v[136:139]\n"
      "ds_read_b128 a[64:67], v251 offset:29184\n"
      "v_mfma_f32_16x16x32_bf16 v[168:171], a[8:11], a[52:55], v[168:171]\n"
      "buffer_load_dwordx4 v249, s[40:43], s68 offen offset:2048  lds\n"
      "v_mfma_f32_16x16x32_bf16 v[12:15], a[12:15], a[32:35], v[12:15]\n"
      "ds_read_b64_tr_b16 a[8:9], v243 offset:256\n"
      "v_mfma_f32_16x16x32_bf16 v[44:47], a[12:15], a[36:39], v[44:47]\n"
      "ds_read_b64_tr_b16 a[10:11], v243 offset:1280\n"
      "v_mfma_f32_16x16x32_bf16 v[76:79], a[12:15], a[40:43], v[76:79]\n"
      "v_add_u32 v196, s72, v192\n"
      "v_cmp_gt_u32 vcc, 8, v196\n"
      "v_mfma_f32_16x16x32_bf16 v[108:111], a[12:15], a[44:47], v[108:111]\n"
      "buffer_load_dwordx4 v250, s[40:43], s68 offen offset:3072  lds\n"
      "v_mov_b32 v196, 0x40000000\n"
      "v_mfma_f32_16x16x32_bf16 v[140:143], a[12:15], a[48:51], v[140:143]\n"
      "ds_read_b128 a[68:71], v251 offset:31232\n"
      "v_cndmask_b32 v199, v196, v199, vcc\n"
      "v_mfma_f32_16x16x32_bf16 v[172:175], a[12:15], a[52:55], v[172:175]\n"
      "buffer_store_dword v198, v199, s[64:67], 0 offen offset:0 sc1\n"
      "v_mfma_f32_16x16x32_bf16 v[16:19], a[16:19], a[32:35], v[16:19]\n"
      "s_add_u32 m0, s72, 69632\n"
      "ds_read_b64_tr_b16 a[12:13], v244 offset:256\n"
      "v_mfma_f32_16x16x32_bf16 v[48:51], a[16:19], a[36:39], v[48:51]\n"
      "ds_read_b64_tr_b16 a[14:15], v244 offset:1280\n"
      "v_mfma_f32_16x16x32_bf16 v[80:83], a[16:19], a[40:43], v[80:83]\n"
      "s_add_u32 s68, s68, 131072\n"
      "buffer_load_dwordx4 v247, s[40:43], s68 offen offset:0  lds\n"
      "v_mfma_f32_16x16x32_bf16 v[112:115], a[16:19], a[44:47], v[112:115]\n"
      "buffer_load_dwordx4 v248, s[40:43], s68 offen offset:1024  lds\n"
      "v_mfma_f32_16x16x32_bf16 v[144:147], a[16:19], a[48:51], v[144:147]\n"
      "ds_read_b128 a[72:75], v251 offset:33280\n"
      "v_mfma_f32_16x16x32_bf16 v[176:179], a[16:19], a[52:55], v[176:179]\n"
      "s_waitcnt vmcnt(15)\n"
      "ds_read_b64_tr_b16 a[130:131], v202 offset:0\n"
      "v_mfma_f32_16x16x32_bf16 v[20:23], a[20:23], a[32:35], v[20:23]\n"
      "ds_read_b64_tr_b16 a[16:17], v243 offset:512\n"
      "v_mfma_f32_16x16x32_bf16 v[52:55], a[20:23], a[36:39], v[52:55]\n"
      "ds_read_b64_tr_b16 a[18:19], v243 offset:1536\n"
      "v_mfma_f32_16x16x32_bf16 v[84:87], a[20:23], a[40:43], v[84:87]\n"
      "ds_read_b64_tr_b16 a[134:135], v203 offset:0\n"
      "v_mfma_f32_16x16x32_bf16 v[116:119], a[20:23], a[44:47], v[116:119]\n"
      "ds_read_b64_tr_b16 a[138:139], v202 offset:256\n"
      "v_mfma_f32_16x16x32_bf16 v[148:151], a[20:23], a[48:51], v[148:151]\n"
      "ds_read_b128 a[76:79], v251 offset:35328\n"
      "v_mfma_f32_16x16x32_bf16 v[180:183], a[20:23], a[52:55], v[180:183]\n"
      "ds_read_b64_tr_b16 a[142:143], v203 offset:256\n"
      "v_mfma_f32_16x16x32_bf16 v[24:27], a[24:27], a[32:35], v[24:27]\n"
      "ds_read_b64_tr_b16 a[20:21], v244 offset:512\n"
      "v_mfma_f32_16x16x32_bf16 v[56:59], a[24:27], a[36:39], v[56:59]\n"
      "ds_read_b64_tr_b16 a[22:23], v244 offset:1536\n"
      "v_mfma_f32_16x16x32_bf16 v[88:91], a[24:27], a[40:43], v[88:91]\n"
      "ds_read_b64_tr_b16 a[146:147], v202 offset:512\n"
      "v_mfma_f32_16x16x32_bf16 v[120:123], a[24:27], a[44:47], v[120:123]\n"
      "ds_read_b64_tr_b16 a[150:151], v203 offset:512\n"
      "v_mfma_f32_16x16x32_bf16 v[152:155], a[24:27], a[48:51], v[152:155]\n"
      "ds_read_b64_tr_b16 a[154:155], v202 offset:768\n"
      "v_mfma_f32_16x16x32_bf16 v[184:187], a[24:27], a[52:55], v[184:187]\n"
      "ds_read_b64_tr_b16 a[158:159], v203 offset:768\n"
      "v_mfma_f32_16x16x32_bf16 v[28:31], a[28:31], a[32:35], v[28:31]\n"
      "ds_read_b64_tr_b16 a[24:25], v243 offset:768\n"
      "v_mfma_f32_16x16x32_bf16 v[60:63], a[28:31], a[36:39], v[60:63]\n"
      "ds_read_b64_tr_b16 a[26:27], v243 offset:1792\n"
      "v_mfma_f32_16x16x32_bf16 v[92:95], a[28:31], a[40:43], v[92:95]\n"
      "buffer_load_dwordx4 v249, s[40:43], s68 offen offset:2048  lds\n"
      "v_mfma_f32_16x16x32_bf16 v[124:127], a[28:31], a[44:47], v[124:127]\n"
      "buffer_load_dwordx4 v250, s[40:43], s68 offen offset:3072  lds\n"
      "s_add_u32 s68, s68, 131072\n"
      "v_mfma_f32_16x16x32_bf16 v[156:159], a[28:31], a[48:51], v[156:159]\n"
      "s_cmp_eq_u32 s68, 5242880\n"
      "s_cselect_b32 s68, 0, s68\n"
      "v_mfma_f32_16x16x32_bf16 v[188:191], a[28:31], a[52:55], v[188:191]\n"
      "s_waitcnt lgkmcnt(14)\n"
      "v_mfma_f32_16x16x32_bf16 v[0:3], a[0:3], a[56:59], v[0:3]\n"
      "s_barrier\n"
      "ds_read_b64_tr_b16 a[28:29], v244 offset:768\n"
      "v_mfma_f32_16x16x32_bf16 v[32:35], a[0:3], a[60:63], v[32:35]\n"
      "ds_read_b64_tr_b16 a[30:31], v244 offset:1792\n"
      "v_mfma_f32_16x16x32_bf16 v[64:67], a[0:3], a[64:67], v[64:67]\n"
      "v_mfma_f32_16x16x32_bf16 v[96:99], a[0:3], a[68:71], v[96:99]\n"
      "v_mfma_f32_16x16x32_bf16 v[128:131], a[0:3], a[72:75], v[128:131]\n"
      "s_waitcnt lgkmcnt(11)\n"
      "ds_read_b128 a[32:35], v251 offset:36864\n"
      "v_mfma_f32_16x16x32_bf16 v[160:163], a[0:3], a[76:79], v[160:163]\n"
      "v_mfma_f32_16x16x32_bf16 v[4:7], a[4:7], a[56:59], v[4:7]\n"
      "s_waitcnt vmcnt(9)\n"
      "ds_read_b64_tr_b16 a[0:1], v243 offset:32768\n"
      "v_mfma_f32_16x16x32_bf16 v[36:39], a[4:7], a[60:63], v[36:39]\n"
      "ds_read_b64_tr_b16 a[2:3], v243 offset:33792\n"
      "v_mfma_f32_16x16x32_bf16 v[68:71], a[4:7], a[64:67], v[68:71]\n"
      "v_mfma_f32_16x16x32_bf16 v[100:103], a[4:7], a[68:71], v[100:103]\n"
      "v_mfma_f32_16x16x32_bf16 v[132:135], a[4:7], a[72:75], v[132:135]\n"
      "s_add_u32 m0, s79, 0\n"
      "ds_read_b128 a[36:39], v251 offset:38912\n"
      "v_mfma_f32_16x16x32_bf16 v[164:167], a[4:7], a[76:79], v[164:167]\n"
      "v_mfma_f32_16x16x32_bf16 v[8:11], a[8:11], a[56:59], v[8:11]\n"
      "ds_read_b64_tr_b16 a[4:5], v244 offset:32768\n"
      "v_mfma_f32_16x16x32_bf16 v[40:43], a[8:11], a[60:63], v[40:43]\n"
      "ds_read_b64_tr_b16 a[6:7], v244 offset:33792\n"
      "v_mfma_f32_16x16x32_bf16 v[72:75], a[8:11], a[64:67], v[72:75]\n"
      "v_mfma_f32_16x16x32_bf16 v[104:107], a[8:11], a[68:71], v[104:107]\n"
      "v_mfma_f32_16x16x32_bf16 v[136:139], a[8:11], a[72:75], v[136:139]\n"
      "ds_read_b128 a[40:43], v251 offset:40960\n"
      "v_mfma_f32_16x16x32_bf16 v[168:171], a[8:11], a[76:79], v[168:171]\n"
      "s_waitcnt lgkmcnt(7)\n"
      "buffer_load_dwordx4 v234, s[44:47], s70 offen offset:0 nt lds\n"
      "v_mfma_f32_16x16x32_bf16 v[12:15], a[12:15], a[56:59], v[12:15]\n"
      "ds_read_b64_tr_b16 a[8:9], v243 offset:33024\n"
      "v_mfma_f32_16x16x32_bf16 v[44:47], a[12:15], a[60:63], v[44:47]\n"
      "ds_read_b64_tr_b16 a[10:11], v243 offset:34048\n"
      "v_mfma_f32_16x16x32_bf16 v[76:79], a[12:15], a[64:67], v[76:79]\n"
      "v_mfma_f32_16x16x32_bf16 v[108:111], a[12:15], a[68:71], v[108:111]\n"
      "buffer_load_dwordx4 v234, s[44:47], s70 offen offset:1024 nt lds\n"
      "v_mfma_f32_16x16x32_bf16 v[140:143], a[12:15], a[72:75], v[140:143]\n"
      "ds_read_b128 a[44:47], v251 offset:43008\n"
      "v_mfma_f32_16x16x32_bf16 v[172:175], a[12:15], a[76:79], v[172:175]\n"
      "v_mfma_f32_16x16x32_bf16 v[16:19], a[16:19], a[56:59], v[16:19]\n"
      "ds_read_b64_tr_b16 a[12:13], v244 offset:33024\n"
      "v_mfma_f32_16x16x32_bf16 v[48:51], a[16:19], a[60:63], v[48:51]\n"
      "ds_read_b64_tr_b16 a[14:15], v244 offset:34048\n"
      "v_mfma_f32_16x16x32_bf16 v[80:83], a[16:19], a[64:67], v[80:83]\n"
      "v_mfma_f32_16x16x32_bf16 v[112:115], a[16:19], a[68:71], v[112:115]\n"
      "buffer_load_dwordx4 v235, s[44:47], s70 offen offset:2048 nt lds\n"
      "v_mfma_f32_16x16x32_bf16 v[144:147], a[16:19], a[72:75], v[144:147]\n"
      "ds_read_b128 a[48:51], v251 offset:45056\n"
      "v_mfma_f32_16x16x32_bf16 v[176:179], a[16:19], a[76:79], v[176:179]\n"
      "v_mfma_f32_16x16x32_bf16 v[20:23], a[20:23], a[56:59], v[20:23]\n"
      "ds_read_b64_tr_b16 a[16:17], v243 offset:33280\n"
      "v_mfma_f32_16x16x32_bf16 v[52:55], a[20:23], a[60:63], v[52:55]\n"
      "ds_read_b64_tr_b16 a[18:19], v243 offset:34304\n"
      "v_mfma_f32_16x16x32_bf16 v[84:87], a[20:23], a[64:67], v[84:87]\n"
      "v_mfma_f32_16x16x32_bf16 v[116:119], a[20:23], a[68:71], v[116:119]\n"
      "buffer_load_dwordx4 v235, s[44:47], s70 offen offset:3072 nt lds\n"
      "v_mfma_f32_16x16x32_bf16 v[148:151], a[20:23], a[72:75], v[148:151]\n"
      "ds_read_b128 a[52:55], v251 offset:47104\n"
      "v_mfma_f32_16x16x32_bf16 v[180:183], a[20:23], a[76:79], v[180:183]\n"
      "v_mfma_f32_16x16x32_bf16 v[24:27], a[24:27], a[56:59], v[24:27]\n"
      "ds_read_b64_tr_b16 a[20:21], v244 offset:33280\n"
      "v_mfma_f32_16x16x32_bf16 v[56:59], a[24:27], a[60:63], v[56:59]\n"
      "ds_read_b64_tr_b16 a[22:23], v244 offset:34304\n"
      "v_mfma_f32_16x16x32_bf16 v[88:91], a[24:27], a[64:67], v[88:91]\n"
      "v_mfma_f32_16x16x32_bf16 v[120:123], a[24:27], a[68:71], v[120:123]\n"
      "s_add_u32 s70, s70, %16\n"
      "v_mfma_f32_16x16x32_bf16 v[152:155], a[24:27], a[72:75], v[152:155]\n"
      "v_mfma_f32_16x16x32_bf16 v[184:187], a[24:27], a[76:79], v[184:187]\n"
      "v_mfma_f32_16x16x32_bf16 v[28:31], a[28:31], a[56:59], v[28:31]\n"
      "ds_read_b64_tr_b16 a[24:25], v243 offset:33536\n"
      "v_mfma_f32_16x16x32_bf16 v[60:63], a[28:31], a[60:63], v[60:63]\n"
      "ds_read_b64_tr_b16 a[26:27], v243 offset:34560\n"
      "v_mfma_f32_16x16x32_bf16 v[92:95], a[28:31], a[64:67], v[92:95]\n"
      "v_mfma_f32_16x16x32_bf16 v[124:127], a[28:31], a[68:71], v[124:127]\n"
      "s_and_b32 s70, s70, 0x1ffff\n"
      "v_mfma_f32_16x16x32_bf16 v[156:159], a[28:31], a[72:75], v[156:159]\n"
      "v_mfma_f32_16x16x32_bf16 v[188:191], a[28:31], a[76:79], v[188:191]\n"
      "s_waitcnt lgkmcnt(14)\n"
      "v_mfma_f32_16x16x32_bf16 v[0:3], a[0:3], a[32:35], v[0:3]\n"
      "ds_read_b64_tr_b16 a[28:29], v244 offset:33536\n"
      "v_mfma_f32_16x16x32_bf16 v[32:35], a[0:3], a[36:39], v[32:35]\n"
      "s_waitcnt lgkmcnt(14)\n"
      "ds_read_b64_tr_b16 a[30:31], v244 offset:34560\n"
      "v_mfma_f32_16x16x32_bf16 v[64:67], a[0:3], a[40:43], v[64:67]\n"
      "s_waitcnt lgkmcnt(12)\n"
      "v_mfma_f32_16x16x32_bf16 v[96:99], a[0:3], a[44:47], v[96:99]\n"
      "s_waitcnt lgkmcnt(9)\n"
      "v_mfma_f32_16x16x32_bf16 v[128:131], a[0:3], a[48:51], v[128:131]\n"
      "s_waitcnt lgkmcnt(6)\n"
      "ds_read_b128 a[56:59], v251 offset:37376\n"
      "v_mfma_f32_16x16x32_bf16 v[160:163], a[0:3], a[52:55], v[160:163]\n"
      "v_mfma_f32_16x16x32_bf16 v[4:7], a[4:7], a[32:35], v[4:7]\n"
      "s_waitcnt vmcnt(4)\n"
      "ds_read_b64_tr_b16 a[0:1], v245 offset:0\n"
      "v_mfma_f32_16x16x32_bf16 v[36:39], a[4:7], a[36:39], v[36:39]\n"
      "ds_read_b64_tr_b16 a[2:3], v245 offset:1024\n"
      "v_mfma_f32_16x16x32_bf16 v[68:71], a[4:7], a[40:43], v[68:71]\n"
      "v_mfma_f32_16x16x32_bf16 v[100:103], a[4:7], a[44:47], v[100:103]\n"
      "v_mfma_f32_16x16x32_bf16 v[132:135], a[4:7], a[48:51], v[132:135]\n"
      "s_add_u32 m0, s79, 4096\n"
      "ds_read_b128 a[60:63], v251 offset:39424\n"
      "v_mfma_f32_16x16x32_bf16 v[164:167], a[4:7], a[52:55], v[164:167]\n"
      "v_mfma_f32_16x16x32_bf16 v[8:11], a[8:11], a[32:35], v[8:11]\n"
      "ds_read_b64_tr_b16 a[4:5], v246 offset:0\n"
      "v_mfma_f32_16x16x32_bf16 v[40:43], a[8:11], a[36:39], v[40:43]\n"
      "ds_read_b64_tr_b16 a[6:7], v246 offset:1024\n"
      "v_mfma_f32_16x16x32_bf16 v[72:75], a[8:11], a[40:43], v[72:75]\n"
      "v_mfma_f32_16x16x32_bf16 v[104:107], a[8:11], a[44:47], v[104:107]\n"
      "buffer_load_dwordx4 v234, s[44:47], s70 offen offset:0 nt lds\n"
      "v_mfma_f32_16x16x32_bf16 v[136:139], a[8:11], a[48:51], v[136:139]\n"
      "ds_read_b128 a[64:67], v251 offset:41472\n"
      "v_mfma_f32_16x16x32_bf16 v[168:171], a[8:11], a[52:55], v[168:171]\n"
      "v_mfma_f32_16x16x32_bf16 v[12:15], a[12:15], a[32:35], v[12:15]\n"
      "ds_read_b64_tr_b16 a[8:9], v245 offset:256\n"
      "v_mfma_f32_16x16x32_bf16 v[44:47], a[12:15], a[36:39], v[44:47]\n"
      "ds_read_b64_tr_b16 a[10:11], v245 offset:1280\n"
      "v_mfma_f32_16x16x32_bf16 v[76:79], a[12:15], a[40:43], v[76:79]\n"
      "buffer_load_dwordx4 v234, s[44:47], s70 offen offset:1024 nt lds\n"
      "v_mfma_f32_16x16x32_bf16 v[108:111], a[12:15], a[44:47], v[108:111]\n"
      "v_mfma_f32_16x16x32_bf16 v[140:143], a[12:15], a[48:51], v[140:143]\n"
      "ds_read_b128 a[68:71], v251 offset:43520\n"
      "v_mfma_f32_16x16x32_bf16 v[172:175], a[12:15], a[52:55], v[172:175]\n"
      "v_mfma_f32_16x16x32_bf16 v[16:19], a[16:19], a[32:35], v[16:19]\n"
      "ds_read_b64_tr_b16 a[12:13], v246 offset:256\n"
      "v_mfma_f32_16x16x32_bf16 v[48:51], a[16:19], a[36:39], v[48:51]\n"
      "ds_read_b64_tr_b16 a[14:15], v246 offset:1280\n"
      "v_mfma_f32_16x16x32_bf16 v[80:83], a[16:19], a[40:43], v[80:83]\n"
      "buffer_load_dwordx4 v235, s[44:47], s70 offen offset:2048 nt lds\n"
      "v_mfma_f32_16x16x32_bf16 v[112:115], a[16:19], a[44:47], v[112:115]\n"
      "v_mfma_f32_16x16x32_bf16 v[144:147], a[16:19], a[48:51], v[144:147]\n"
      "ds_read_b128 a[72:75], v251 offset:45568\n"
      "v_mfma_f32_16x16x32_bf16 v[176:179], a[16:19], a[52:55], v[176:179]\n"
      "s_waitcnt lgkmcnt(14)\n"
      "buffer_load_dwordx4 v235, s[44:47], s70 offen offset:3072 nt lds\n"
      "v_mfma_f32_16x16x32_bf16 v[20:23], a[20:23], a[32:35], v[20:23]\n"
      "ds_read_b64_tr_b16 a[16:17], v245 offset:512\n"
      "v_mfma_f32_16x16x32_bf16 v[52:55], a[20:23], a[36:39], v[52:55]\n"
      "ds_read_b64_tr_b16 a[18:19], v245 offset:1536\n"
      "v_mfma_f32_16x16x32_bf16 v[84:87], a[20:23], a[40:43], v[84:87]\n"
      "v_mfma_f32_16x16x32_bf16 v[116:119], a[20:23], a[44:47], v[116:119]\n"
      "v_mfma_f32_16x16x32_bf16 v[148:151], a[20:23], a[48:51], v[148:151]\n"
      "ds_read_b128 a[76:79], v251 offset:47616\n"
      "v_mfma_f32_16x16x32_bf16 v[180:183], a[20:23], a[52:55], v[180:183]\n"
      "s_add_u32 s70, s70, %16\n"
      "v_mfma_f32_16x16x32_bf16 v[24:27], a[24:27], a[32:35], v[24:27]\n"
      "ds_read_b64_tr_b16 a[20:21], v246 offset:512\n"
      "v_mfma_f32_16x16x32_bf16 v[56:59], a[24:27], a[36:39], v[56:59]\n"
      "ds_read_b64_tr_b16 a[22:23], v246 offset:1536\n"
      "v_mfma_f32_16x16x32_bf16 v[88:91], a[24:27], a[40:43], v[88:91]\n"
      "v_mfma_f32_16x16x32_bf16 v[120:123], a[24:27], a[44:47], v[120:123]\n"
      "v_mfma_f32_16x16x32_bf16 v[152:155], a[24:27], a[48:51], v[152:155]\n"
      "s_and_b32 s70, s70, 0x1ffff\n"
      "v_mfma_f32_16x16x32_bf16 v[184:187], a[24:27], a[52:55], v[184:187]\n"
      "s_waitcnt lgkmcnt(14)\n"
      "v_mfma_f32_16x16x32_bf16 v[28:31], a[28:31], a[32:35], v[28:31]\n"
      "ds_read_b64_tr_b16 a[24:25], v245 offset:768\n"
      "v_mfma_f32_16x16x32_bf16 v[60:63], a[28:31], a[36:39], v[60:63]\n"
      "ds_read_b64_tr_b16 a[26:27], v245 offset:1792\n"
      "v_mfma_f32_16x16x32_bf16 v[92:95], a[28:31], a[40:43], v[92:95]\n"
      "v_mfma_f32_16x16x32_bf16 v[124:127], a[28:31], a[44:47], v[124:127]\n"
      "s_xor_b32 s79, s79, 65536\n"
      "v_mfma_f32_16x16x32_bf16 v[156:159], a[28:31], a[48:51], v[156:159]\n"
      "v_mfma_f32_16x16x32_bf16 v[188:191], a[28:31], a[52:55], v[188:191]\n"
      "v_mfma_f32_16x16x32_bf16 v[0:3], a[0:3], a[56:59], v[0:3]\n"
      "s_waitcnt lgkmcnt(4)\n"
      "s_barrier\n"
      "ds_read_b64_tr_b16 a[28:29], v246 offset:768\n"
      "v_mfma_f32_16x16x32_bf16 v[32:35], a[0:3], a[60:63], v[32:35]\n"
      "ds_read_b64_tr_b16 a[30:31], v246 offset:1792\n"
      "v_mfma_f32_16x16x32_bf16 v[64:67], a[0:3], a[64:67], v[64:67]\n"
      "v_mfma_f32_16x16x32_bf16 v[96:99], a[0:3], a[68:71], v[96:99]\n"
      "v_mfma_f32_16x16x32_bf16 v[128:131], a[0:3], a[72:75], v[128:131]\n"
      "v_mfma_f32_16x16x32_bf16 v[160:163], a[0:3], a[76:79], v[160:163]\n"
      "v_mfma_f32_16x16x32_bf16 v[4:7], a[4:7], a[56:59], v[4:7]\n"
      "v_mfma_f32_16x16x32_bf16 v[36:39], a[4:7], a[60:63], v[36:39]\n"
      "v_mfma_f32_16x16x32_bf16 v[68:71], a[4:7], a[64:67], v[68:71]\n"
      "v_mfma_f32_16x16x32_bf16 v[100:103], a[4:7], a[68:71], v[100:103]\n"
      "v_mfma_f32_16x16x32_bf16 v[132:135], a[4:7], a[72:75], v[132:135]\n"
      "s_add_u32 m0, s80, 0\n"
      "v_mfma_f32_16x16x32_bf16 v[164:167], a[4:7], a[76:79], v[164:167]\n"
      "v_mfma_f32_16x16x32_bf16 v[8:11], a[8:11], a[56:59], v[8:11]\n"
      "v_mfma_f32_16x16x32_bf16 v[40:43], a[8:11], a[60:63], v[40:43]\n"
      "v_mfma_f32_16x16x32_bf16 v[72:75], a[8:11], a[64:67], v[72:75]\n"
      "v_mfma_f32_16x16x32_bf16 v[104:107], a[8:11], a[68:71], v[104:107]\n"
      "v_mfma_f32_16x16x32_bf16 v[136:139], a[8:11], a[72:75], v[136:139]\n"
      "buffer_load_dwordx4 v234, s[48:51], s71 offen offset:0 nt lds\n"
      "v_mfma_f32_16x16x32_bf16 v[168:171], a[8:11], a[76:79], v[168:171]\n"
      "v_mfma_f32_16x16x32_bf16 v[12:15], a[12:15], a[56:59], v[12:15]\n"
      "v_mfma_f32_16x16x32_bf16 v[44:47], a[12:15], a[60:63], v[44:47]\n"
      "v_mfma_f32_16x16x32_bf16 v[76:79], a[12:15], a[64:67], v[76:79]\n"
      "v_mfma_f32_16x16x32_bf16 v[108:111], a[12:15], a[68:71], v[108:111]\n"
      "v_mfma_f32_16x16x32_bf16 v[140:143], a[12:15], a[72:75], v[140:143]\n"
      "buffer_load_dwordx4 v236, s[48:51], s71 offen offset:1024 nt lds\n"
      "v_mfma_f32_16x16x32_bf16 v[172:175], a[12:15], a[76:79], v[172:175]\n"
      "v_mfma_f32_16x16x32_bf16 v[16:19], a[16:19], a[56:59], v[16:19]\n"
      "v_mfma_f32_16x16x32_bf16 v[48:51], a[16:19], a[60:63], v[48:51]\n"
      "v_mfma_f32_16x16x32_bf16 v[80:83], a[16:19], a[64:67], v[80:83]\n"
      "v_mfma_f32_16x16x32_bf16 v[112:115], a[16:19], a[68:71], v[112:115]\n"
      "buffer_load_dwordx4 v234, s[48:51], s71 offen offset:2048 nt lds\n"
      "v_mfma_f32_16x16x32_bf16 v[144:147], a[16:19], a[72:75], v[144:147]\n"
      "v_mfma_f32_16x16x32_bf16 v[176:179], a[16:19], a[76:79], v[176:179]\n"
      "s_waitcnt lgkmcnt(4)\n"
      "v_mfma_f32_16x16x32_bf16 v[20:23], a[20:23], a[56:59], v[20:23]\n"
      "v_mfma_f32_16x16x32_bf16 v[52:55], a[20:23], a[60:63], v[52:55]\n"
      "v_mfma_f32_16x16x32_bf16 v[84:87], a[20:23], a[64:67], v[84:87]\n"
      "v_mfma_f32_16x16x32_bf16 v[116:119], a[20:23], a[68:71], v[116:119]\n"
      "buffer_load_dwordx4 v236, s[48:51], s71 offen offset:3072 nt lds\n"
      "v_mfma_f32_16x16x32_bf16 v[148:151], a[20:23], a[72:75], v[148:151]\n"
      "v_mfma_f32_16x16x32_bf16 v[180:183], a[20:23], a[76:79], v[180:183]\n"
      "s_waitcnt lgkmcnt(2)\n"
      "v_mfma_f32_16x16x32_bf16 v[24:27], a[24:27], a[56:59], v[24:27]\n"
      "v_mfma_f32_16x16x32_bf16 v[56:59], a[24:27], a[60:63], v[56:59]\n"
      "v_mfma_f32_16x16x32_bf16 v[88:91], a[24:27], a[64:67], v[88:91]\n"
      "v_mfma_f32_16x16x32_bf16 v[120:123], a[24:27], a[68:71], v[120:123]\n"
      "s_add_u32 s71, s71, %16\n"
      "v_mfma_f32_16x16x32_bf16 v[152:155], a[24:27], a[72:75], v[152:155]\n"
      "v_mfma_f32_16x16x32_bf16 v[184:187], a[24:27], a[76:79], v[184:187]\n"
      "s_waitcnt lgkmcnt(0)\n"
      "v_mfma_f32_16x16x32_bf16 v[28:31], a[28:31], a[56:59], v[28:31]\n"
      "v_mfma_f32_16x16x32_bf16 v[60:63], a[28:31], a[60:63], v[60:63]\n"
      "v_mfma_f32_16x16x32_bf16 v[92:95], a[28:31], a[64:67], v[92:95]\n"
      "v_mfma_f32_16x16x32_bf16 v[124:127], a[28:31], a[68:71], v[124:127]\n"
      "s_and_b32 s71, s71, 0x1ffff\n"
      "v_mfma_f32_16x16x32_bf16 v[156:159], a[28:31], a[72:75], v[156:159]\n"
      "v_mfma_f32_16x16x32_bf16 v[188:191], a[28:31], a[76:79], v[188:191]\n"
      "s_add_u32 m0, s80, 4096\n"
      "v_cvt_pk_bf16_f32 v0, v0, v1\n"
      "v_cvt_pk_bf16_f32 v1, v2, v3\n"
      "v_cvt_pk_bf16_f32 v2, v4, v5\n"
      "v_cvt_pk_bf16_f32 v3, v6, v7\n"
      "v_cvt_pk_bf16_f32 v8, v8, v9\n"
      "v_cvt_pk_bf16_f32 v9, v10, v11\n"
      "v_cvt_pk_bf16_f32 v10, v12, v13\n"
      "v_cvt_pk_bf16_f32 v11, v14, v15\n"
      "v_cvt_pk_bf16_f32 v16, v16, v17\n"
      "v_cvt_pk_bf16_f32 v17, v18, v19\n"
      "v_cvt_pk_bf16_f32 v18, v20, v21\n"
      "v_cvt_pk_bf16_f32 v19, v22, v23\n"
      "v_cvt_pk_bf16_f32 v24, v24, v25\n"
      "v_cvt_pk_bf16_f32 v25, v26, v27\n"
      "v_cvt_pk_bf16_f32 v26, v28, v29\n"
      "v_cvt_pk_bf16_f32 v27, v30, v31\n"
      "v_permlane16_swap_b32 v0, v2\n"
      "v_permlane16_swap_b32 v1, v3\n"
      "v_permlane16_swap_b32 v8, v10\n"
      "v_permlane16_swap_b32 v9, v11\n"
      "v_permlane16_swap_b32 v16, v18\n"
      "v_permlane16_swap_b32 v17, v19\n"
      "v_permlane16_swap_b32 v24, v26\n"
      "v_permlane16_swap_b32 v25, v27\n"
      "v_accvgpr_write_b32 a0, v0\n"
      "v_accvgpr_write_b32 a1, v1\n"
      "v_accvgpr_write_b32 a2, v2\n"
      "v_accvgpr_write_b32 a3, v3\n"
      "v_accvgpr_write_b32 a4, v8\n"
      "v_accvgpr_write_b32 a5, v9\n"
      "v_accvgpr_write_b32 a6, v10\n"
      "v_accvgpr_write_b32 a7, v11\n"
      "v_accvgpr_write_b32 a8, v16\n"
      "v_accvgpr_write_b32 a9, v17\n"
      "v_accvgpr_write_b32 a10, v18\n"
      "v_accvgpr_write_b32 a11, v19\n"
      "v_accvgpr_write_b32 a12, v24\n"
      "v_accvgpr_write_b32 a13, v25\n"
      "v_accvgpr_write_b32 a14, v26\n"
      "v_accvgpr_write_b32 a15, v27\n"
      "buffer_load_dwordx4 v234, s[48:51], s71 offen offset:0 nt lds\n"
      "v_cvt_pk_bf16_f32 v32, v32, v33\n"
      "v_cvt_pk_bf16_f32 v33, v34, v35\n"
      "v_cvt_pk_bf16_f32 v34, v36, v37\n"
      "v_cvt_pk_bf16_f32 v35, v38, v39\n"
      "v_cvt_pk_bf16_f32 v40, v40, v41\n"
      "v_cvt_pk_bf16_f32 v41, v42, v43\n"
      "v_cvt_pk_bf16_f32 v42, v44, v45\n"
      "v_cvt_pk_bf16_f32 v43, v46, v47\n"
      "v_cvt_pk_bf16_f32 v48, v48, v49\n"
      "v_cvt_pk_bf16_f32 v49, v50, v51\n"
      "v_cvt_pk_bf16_f32 v50, v52, v53\n"
      "v_cvt_pk_bf16_f32 v51, v54, v55\n"
      "v_cvt_pk_bf16_f32 v56, v56, v57\n"
      "v_cvt_pk_bf16_f32 v57, v58, v59\n"
      "v_cvt_pk_bf16_f32 v58, v60, v61\n"
      "v_cvt_pk_bf16_f32 v59, v62, v63\n"
      "v_permlane16_swap_b32 v32, v34\n"
      "v_permlane16_swap_b32 v33, v35\n"
      "v_permlane16_swap_b32 v40, v42\n"
      "v_permlane16_swap_b32 v41, v43\n"
      "v_permlane16_swap_b32 v48, v50\n"
      "v_permlane16_swap_b32 v49, v51\n"
      "v_permlane16_swap_b32 v56, v58\n"
      "v_permlane16_swap_b32 v57, v59\n"
      "v_accvgpr_write_b32 a16, v32\n"
      "v_accvgpr_write_b32 a17, v33\n"
      "v_accvgpr_write_b32 a18, v34\n"
      "v_accvgpr_write_b32 a19, v35\n"
      "v_accvgpr_write_b32 a20, v40\n"
      "v_accvgpr_write_b32 a21, v41\n"
      "v_accvgpr_write_b32 a22, v42\n"
      "v_accvgpr_write_b32 a23, v43\n"
      "v_accvgpr_write_b32 a24, v48\n"
      "v_accvgpr_write_b32 a25, v49\n"
      "v_accvgpr_write_b32 a26, v50\n"
      "v_accvgpr_write_b32 a27, v51\n"
      "v_accvgpr_write_b32 a28, v56\n"
      "v_accvgpr_write_b32 a29, v57\n"
      "v_accvgpr_write_b32 a30, v58\n"
      "v_accvgpr_write_b32 a31, v59\n"
      "buffer_load_dwordx4 v236, s[48:51], s71 offen offset:1024 nt lds\n"
      "buffer_load_dwordx4 v234, s[48:51], s71 offen offset:2048 nt lds\n"
      "v_cvt_pk_bf16_f32 v64, v64, v65\n"
      "v_cvt_pk_bf16_f32 v65, v66, v67\n"
      "v_cvt_pk_bf16_f32 v66, v68, v69\n"
      "v_cvt_pk_bf16_f32 v67, v70, v71\n"
      "v_cvt_pk_bf16_f32 v72, v72, v73\n"
      "v_cvt_pk_bf16_f32 v73, v74, v75\n"
      "v_cvt_pk_bf16_f32 v74, v76, v77\n"
      "v_cvt_pk_bf16_f32 v75, v78, v79\n"
      "v_cvt_pk_bf16_f32 v80, v80, v81\n"
      "v_cvt_pk_bf16_f32 v81, v82, v83\n"
      "v_cvt_pk_bf16_f32 v82, v84, v85\n"
      "v_cvt_pk_bf16_f32 v83, v86, v87\n"
      "v_cvt_pk_bf16_f32 v88, v88, v89\n"
      "v_cvt_pk_bf16_f32 v89, v90, v91\n"
      "v_cvt_pk_bf16_f32 v90, v92, v93\n"
      "v_cvt_pk_bf16_f32 v91, v94, v95\n"
      "v_permlane16_swap_b32 v64, v66\n"
      "v_permlane16_swap_b32 v65, v67\n"
      "v_permlane16_swap_b32 v72, v74\n"
      "v_permlane16_swap_b32 v73, v75\n"
      "v_permlane16_swap_b32 v80, v82\n"
      "v_permlane16_swap_b32 v81, v83\n"
      "v_permlane16_swap_b32 v88, v90\n"
      "v_permlane16_swap_b32 v89, v91\n"
      "v_accvgpr_write_b32 a32, v64\n"
      "v_accvgpr_write_b32 a33, v65\n"
      "v_accvgpr_write_b32 a34, v66\n"
      "v_accvgpr_write_b32 a35, v67\n"
      "v_accvgpr_write_b32 a36, v72\n"
      "v_accvgpr_write_b32 a37, v73\n"
      "v_accvgpr_write_b32 a38, v74\n"
      "v_accvgpr_write_b32 a39, v75\n"
      "v_accvgpr_write_b32 a40, v80\n"
      "v_accvgpr_write_b32 a41, v81\n"
      "v_accvgpr_write_b32 a42, v82\n"
      "v_accvgpr_write_b32 a43, v83\n"
      "v_accvgpr_write_b32 a44, v88\n"
      "v_accvgpr_write_b32 a45, v89\n"
      "v_accvgpr_write_b32 a46, v90\n"
      "v_accvgpr_write_b32 a47, v91\n"
      "buffer_load_dwordx4 v236, s[48:51], s71 offen offset:3072 nt lds\n"
      "v_cvt_pk_bf16_f32 v96, v96, v97\n"
      "v_cvt_pk_bf16_f32 v97, v98, v99\n"
      "v_cvt_pk_bf16_f32 v98, v100, v101\n"
      "v_cvt_pk_bf16_f32 v99, v102, v103\n"
      "v_cvt_pk_bf16_f32 v104, v104, v105\n"
      "v_cvt_pk_bf16_f32 v105, v106, v107\n"
      "v_cvt_pk_bf16_f32 v106, v108, v109\n"
      "v_cvt_pk_bf16_f32 v107, v110, v111\n"
      "v_cvt_pk_bf16_f32 v112, v112, v113\n"
      "v_cvt_pk_bf16_f32 v113, v114, v115\n"
      "v_cvt_pk_bf16_f32 v114, v116, v117\n"
      "v_cvt_pk_bf16_f32 v115, v118, v119\n"
      "v_cvt_pk_bf16_f32 v120, v120, v121\n"
      "v_cvt_pk_bf16_f32 v121, v122, v123\n"
      "v_cvt_pk_bf16_f32 v122, v124, v125\n"
      "v_cvt_pk_bf16_f32 v123, v126, v127\n"
      "v_permlane16_swap_b32 v96, v98\n"
      "v_permlane16_swap_b32 v97, v99\n"
      "v_permlane16_swap_b32 v104, v106\n"
      "v_permlane16_swap_b32 v105, v107\n"
      "v_permlane16_swap_b32 v112, v114\n"
      "v_permlane16_swap_b32 v113, v115\n"
      "v_permlane16_swap_b32 v120, v122\n"
      "v_permlane16_swap_b32 v121, v123\n"
      "v_accvgpr_write_b32 a48, v96\n"
      "v_accvgpr_write_b32 a49, v97\n"
      "v_accvgpr_write_b32 a50, v98\n"
      "v_accvgpr_write_b32 a51, v99\n"
      "v_accvgpr_write_b32 a52, v104\n"
      "v_accvgpr_write_b32 a53, v105\n"
      "v_accvgpr_write_b32 a54, v106\n"
      "v_accvgpr_write_b32 a55, v107\n"
      "v_accvgpr_write_b32 a56, v112\n"
      "v_accvgpr_write_b32 a57, v113\n"
      "v_accvgpr_write_b32 a58, v114\n"
      "v_accvgpr_write_b32 a59, v115\n"
      "v_accvgpr_write_b32 a60, v120\n"
      "v_accvgpr_write_b32 a61, v121\n"
      "v_accvgpr_write_b32 a62, v122\n"
      "v_accvgpr_write_b32 a63, v123\n"
      "s_add_u32 s71, s71, %16\n"
      "v_cvt_pk_bf16_f32 v128, v128, v129\n"
      "v_cvt_pk_bf16_f32 v129, v130, v131\n"
      "v_cvt_pk_bf16_f32 v130, v132, v133\n"
      "v_cvt_pk_bf16_f32 v131, v134, v135\n"
      "v_cvt_pk_bf16_f32 v136, v136, v137\n"
      "v_cvt_pk_bf16_f32 v137, v138, v139\n"
      "v_cvt_pk_bf16_f32 v138, v140, v141\n"
      "v_cvt_pk_bf16_f32 v139, v142, v143\n"
      "v_cvt_pk_bf16_f32 v144, v144, v145\n"
      "v_cvt_pk_bf16_f32 v145, v146, v147\n"
      "v_cvt_pk_bf16_f32 v146, v148, v149\n"
      "v_cvt_pk_bf16_f32 v147, v150, v151\n"
      "v_cvt_pk_bf16_f32 v152, v152, v153\n"
      "v_cvt_pk_bf16_f32 v153, v154, v155\n"
      "v_cvt_pk_bf16_f32 v154, v156, v157\n"
      "v_cvt_pk_bf16_f32 v155, v158, v159\n"
      "v_permlane16_swap_b32 v128, v130\n"
      "v_permlane16_swap_b32 v129, v131\n"
      "v_permlane16_swap_b32 v136, v138\n"
      "v_permlane16_swap_b32 v137, v139\n"
      "v_permlane16_swap_b32 v144, v146\n"
      "v_permlane16_swap_b32 v145, v147\n"
      "v_permlane16_swap_b32 v152, v154\n"
      "v_permlane16_swap_b32 v153, v155\n"
      "v_accvgpr_write_b32 a64, v128\n"
      "v_accvgpr_write_b32 a65, v129\n"
      "v_accvgpr_write_b32 a66, v130\n"
      "v_accvgpr_write_b32 a67, v131\n"
      "v_accvgpr_write_b32 a68, v136\n"
      "v_accvgpr_write_b32 a69, v137\n"
      "v_accvgpr_write_b32 a70, v138\n"
      "v_accvgpr_write_b32 a71, v139\n"
      "v_accvgpr_write_b32 a72, v144\n"
      "v_accvgpr_write_b32 a73, v145\n"
      "v_accvgpr_write_b32 a74, v146\n"
      "v_accvgpr_write_b32 a75, v147\n"
      "v_accvgpr_write_b32 a76, v152\n"
      "v_accvgpr_write_b32 a77, v153\n"
      "v_accvgpr_write_b32 a78, v154\n"
      "v_accvgpr_write_b32 a79, v155\n"
      "s_and_b32 s71, s71, 0x1ffff\n"
      "s_xor_b32 s80, s80, 65536\n"
      "v_cvt_pk_bf16_f32 v160, v160, v161\n"
      "v_cvt_pk_bf16_f32 v161, v162, v163\n"
      "v_cvt_pk_bf16_f32 v162, v164, v165\n"
      "v_cvt_pk_bf16_f32 v163, v166, v167\n"
      "v_cvt_pk_bf16_f32 v168, v168, v169\n"
      "v_cvt_pk_bf16_f32 v169, v170, v171\n"
      "v_cvt_pk_bf16_f32 v170, v172, v173\n"
      "v_cvt_pk_bf16_f32 v171, v174, v175\n"
      "v_cvt_pk_bf16_f32 v176, v176, v177\n"
      "v_cvt_pk_bf16_f32 v177, v178, v179\n"
      "v_cvt_pk_bf16_f32 v178, v180, v181\n"
      "v_cvt_pk_bf16_f32 v179, v182, v183\n"
      "v_cvt_pk_bf16_f32 v184, v184, v185\n"
      "v_cvt_pk_bf16_f32 v185, v186, v187\n"
      "v_cvt_pk_bf16_f32 v186, v188, v189\n"
      "v_cvt_pk_bf16_f32 v187, v190, v191\n"
      "v_permlane16_swap_b32 v160, v162\n"
      "v_permlane16_swap_b32 v161, v163\n"
      "v_permlane16_swap_b32 v168, v170\n"
      "v_permlane16_swap_b32 v169, v171\n"
      "v_permlane16_swap_b32 v176, v178\n"
      "v_permlane16_swap_b32 v177, v179\n"
      "v_permlane16_swap_b32 v184, v186\n"
      "v_permlane16_swap_b32 v185, v187\n"
      "v_accvgpr_write_b32 a80, v160\n"
      "v_accvgpr_write_b32 a81, v161\n"
      "v_accvgpr_write_b32 a82, v162\n"
      "v_accvgpr_write_b32 a83, v163\n"
      "v_accvgpr_write_b32 a84, v168\n"
      "v_accvgpr_write_b32 a85, v169\n"
      "v_accvgpr_write_b32 a86, v170\n"
      "v_accvgpr_write_b32 a87, v171\n"
      "v_accvgpr_write_b32 a88, v176\n"
      "v_accvgpr_write_b32 a89, v177\n"
      "v_accvgpr_write_b32 a90, v178\n"
      "v_accvgpr_write_b32 a91, v179\n"
      "v_accvgpr_write_b32 a92, v184\n"
      "v_accvgpr_write_b32 a93, v185\n"
      "v_accvgpr_write_b32 a94, v186\n"
      "v_accvgpr_write_b32 a95, v187\n"
      "s_lshl_b32 s74, %8, 8\n"
      "v_and_b32 v196, 1, v194\n"
      "v_xor_b32 v196, 0, v196\n"
      "v_lshlrev_b32 v196, 5, v196\n"
      "v_lshl_add_u32 v196, v193, 6, v196\n"
      "v_lshl_add_u32 v196, v195, 3, v196\n"
      "v_mul_u32_u24 v197, 1024, v194\n"
      "v_add3_u32 v196, v196, v197, s74\n"
      "v_add_u32 v243, 0, v196\n"
      "v_add_u32 v245, 98304, v243\n"
      "v_and_b32 v196, 1, v194\n"
      "v_xor_b32 v196, 1, v196\n"
      "v_lshlrev_b32 v196, 5, v196\n"
      "v_lshl_add_u32 v196, v193, 6, v196\n"
      "v_lshl_add_u32 v196, v195, 3, v196\n"
      "v_mul_u32_u24 v197, 1024, v194\n"
      "v_add3_u32 v196, v196, v197, s74\n"
      "v_add_u32 v244, 0, v196\n"
      "v_add_u32 v246, 98304, v244\n"
      "v_xor_b32 v196, 0, v195\n"
      "v_lshlrev_b32 v196, 4, v196\n"
      "v_lshl_add_u32 v196, v194, 6, v196\n"
      "v_mul_u32_u24 v197, 1280, v193\n"
      "v_add_u32 v196, v196, v197\n"
      "v_add_u32 v247, 0, v196\n"
      "v_xor_b32 v196, 2, v195\n"
      "v_lshlrev_b32 v196, 4, v196\n"
      "v_lshl_add_u32 v196, v194, 6, v196\n"
      "v_mul_u32_u24 v197, 1280, v193\n"
      "v_add_u32 v196, v196, v197\n"
      "v_add_u32 v248, 4096, v196\n"
      "v_xor_b32 v196, 0, v195\n"
      "v_lshlrev_b32 v196, 4, v196\n"
      "v_lshl_add_u32 v196, v194, 6, v196\n"
      "v_mul_u32_u24 v197, 1280, v193\n"
      "v_add_u32 v196, v196, v197\n"
      "v_add_u32 v249, 8192, v196\n"
      "v_xor_b32 v196, 2, v195\n"
      "v_lshlrev_b32 v196, 4, v196\n"
      "v_lshl_add_u32 v196, v194, 6, v196\n"
      "v_mul_u32_u24 v197, 1280, v193\n"
      "v_add_u32 v196, v196, v197\n"
      "v_add_u32 v250, 12288, v196\n"
      "v_and_b32 v196, 15, v192\n"
      "v_mul_u32_u24 v196, 2560, v196\n"
      "v_bfe_u32 v197, v192, 4, 1\n"
      "v_lshl_add_u32 v196, v197, 5, v196\n"
      "v_lshrrev_b32 v197, 5, v192\n"
      "v_lshl_add_u32 v252, v197, 4, v196\n"
      "v_mov_b32 v196, 0x40000000\n"
      "v_and_b32 v197, 7, v192\n"
      "v_lshlrev_b32 v197, 2, v197\n"
      "v_add_u32 v253, 128, v197\n"
      "s_lshl_b32 s73, %13, 2\n"
      "s_add_u32 s73, s73, 128\n"
      "v_add_u32 v197, s72, v192\n"
      "v_cmp_eq_u32 vcc, 0, v197\n"
      "v_mov_b32 v197, s73\n"
      "s_nop 0\n"
      "v_cndmask_b32 v254, v196, v197, vcc\n"
      "v_mfma_f32_16x16x32_bf16 v[192:195], a[160:163], a[0:3], 0\n"
      "v_mfma_f32_16x16x32_bf16 v[196:199], a[176:179], a[0:3], 0\n"
      "v_mfma_f32_16x16x32_bf16 v[192:195], a[164:167], a[4:7], v[192:195]\n"
      "v_mfma_f32_16x16x32_bf16 v[196:199], a[180:183], a[4:7], v[196:199]\n"
      "v_mfma_f32_16x16x32_bf16 v[192:195], a[168:171], a[8:11], v[192:195]\n"
      "v_mfma_f32_16x16x32_bf16 v[196:199], a[184:187], a[8:11], v[196:199]\n"
      "v_mfma_f32_16x16x32_bf16 v[192:195], a[172:175], a[12:15], v[192:195]\n"
      "v_mfma_f32_16x16x32_bf16 v[196:199], a[188:191], a[12:15], v[196:199]\n"
      "s_mov_b32 s69, 48\n"
      "v_mfma_f32_16x16x32_bf16 v[200:203], a[160:163], a[16:19], 0\n"
      "s_nop 4\n"
      "v_max3_f32 v228, v192, v193, v194\n"
      "v_max3_f32 v228, v228, v195, v196\n"
      "v_max3_f32 v228, v228, v197, v198\n"
      "v_max_f32 v228, v228, v199\n"
      "v_mfma_f32_16x16x32_bf16 v[204:207], a[176:179], a[16:19], 0\n"
      "v_mov_b32 v229, v228\n"
      "s_nop 1\n"
      "v_permlane16_swap_b32 v228, v229\n"
      "v_max_f32 v228, v228, v229\n"
      "v_mov_b32 v229, v228\n"
      "v_mfma_f32_16x16x32_bf16 v[200:203], a[164:167], a[20:23], v[200:203]\n"
      "s_add_u32 m0, s79, 0\n"
      "v_permlane32_swap_b32 v228, v229\n"
      "v_max_f32 v228, v228, v229\n"
      "v_mul_f32 v216, s76, v228\n"
      "v_fma_f32 v192, v192, s76, -v216\n"
      "v_mfma_f32_16x16x32_bf16 v[204:207], a[180:183], a[20:23], v[204:207]\n"
      "v_fma_f32 v193, v193, s76, -v216\n"
      "v_fma_f32 v194, v194, s76, -v216\n"
      "v_fma_f32 v195, v195, s76, -v216\n"
      "v_mfma_f32_16x16x32_bf16 v[200:203], a[168:171], a[24:27], v[200:203]\n"
      "v_fma_f32 v196, v196, s76, -v216\n"
      "v_fma_f32 v197, v197, s76, -v216\n"
      "v_fma_f32 v198, v198, s76, -v216\n"
      "v_fma_f32 v199, v199, s76, -v216\n"
      "v_mfma_f32_16x16x32_bf16 v[204:207], a[184:187], a[24:27], v[204:207]\n"
      "v_exp_f32 v192, v192\n"
      "v_exp_f32 v193, v193\n"
      "v_exp_f32 v194, v194\n"
      "v_cvt_pk_bf16_f32 v208, v192, v193\n"
      "v_mfma_f32_16x16x32_bf16 v[200:203], a[172:175], a[28:31], v[200:203]\n"
      "buffer_load_dwordx4 v234, s[44:47], s70 offen offset:0 nt lds\n"
      "v_exp_f32 v195, v195\n"
      "v_exp_f32 v196, v196\n"
      "v_cvt_pk_bf16_f32 v209, v194, v195\n"
      "v_exp_f32 v197, v197\n"
      "v_mfma_f32_16x16x32_bf16 v[204:207], a[188:191], a[28:31], v[204:207]\n"
      "v_exp_f32 v198, v198\n"
      "v_cvt_pk_bf16_f32 v210, v196, v197\n"
      "v_exp_f32 v199, v199\n"
      "s_nop 0\n"
      "v_cvt_pk_bf16_f32 v211, v198, v199\n"
      "v_mfma_f32_16x16x32_bf16 v[192:195], a[160:163], a[32:35], 0\n"
      "v_max3_f32 v228, v200, v201, v202\n"
      "s_nop 0\n"
      "v_max3_f32 v228, v228, v203, v204\n"
      "v_mfma_f32_16x16x32_bf16 v[196:199], a[176:179], a[32:35], 0\n"
      "v_max3_f32 v228, v228, v205, v206\n"
      "v_max_f32 v228, v228, v207\n"
      "v_mfma_f32_16x16x32_bf16 v[192:195], a[164:167], a[36:39], v[192:195]\n"
      "v_mov_b32 v229, v228\n"
      "s_nop 1\n"
      "v_permlane16_swap_b32 v228, v229\n"
      "v_mfma_f32_16x16x32_bf16 v[196:199], a[180:183], a[36:39], v[196:199]\n"
      "buffer_load_dwordx4 v234, s[44:47], s70 offen offset:1024 nt lds\n"
      "v_max_f32 v228, v228, v229\n"
      "v_mov_b32 v229, v228\n"
      "v_mfma_f32_16x16x32_bf16 v[192:195], a[168:171], a[40:43], v[192:195]\n"
      "s_nop 0\n"
      "v_permlane32_swap_b32 v228, v229\n"
      "v_mfma_f32_16x16x32_bf16 v[196:199], a[184:187], a[40:43], v[196:199]\n"
      "v_max_f32 v228, v228, v229\n"
      "v_mul_f32 v217, s76, v228\n"
      "v_mfma_f32_16x16x32_bf16 v[192:195], a[172:175], a[44:47], v[192:195]\n"
      "v_fma_f32 v200, v200, s76, -v217\n"
      "v_fma_f32 v201, v201, s76, -v217\n"
      "v_mfma_f32_16x16x32_bf16 v[196:199], a[188:191], a[44:47], v[196:199]\n"
      "buffer_load_dwordx4 v235, s[44:47], s70 offen offset:2048 nt lds\n"
      "v_fma_f32 v202, v202, s76, -v217\n"
      "v_fma_f32 v203, v203, s76, -v217\n"
      "v_mfma_f32_16x16x32_bf16 v[0:3], a[192:195], v[208:211], 0\n"
      "v_fma_f32 v204, v204, s76, -v217\n"
      "v_fma_f32 v205, v205, s76, -v217\n"
      "v_mfma_f32_16x16x32_bf16 v[4:7], a[196:199], v[208:211], 0\n"
      "v_fma_f32 v206, v206, s76, -v217\n"
      "v_fma_f32 v207, v207, s76, -v217\n"
      "v_mfma_f32_16x16x32_bf16 v[8:11], a[200:203], v[208:211], 0\n"
      "v_exp_f32 v200, v200\n"
      "v_mfma_f32_16x16x32_bf16 v[12:15], a[204:207], v[208:211], 0\n"
      "buffer_load_dwordx4 v235, s[44:47], s70 offen offset:3072 nt lds\n"
      "v_exp_f32 v201, v201\n"
      "v_exp_f32 v202, v202\n"
      "v_mfma_f32_16x16x32_bf16 v[16:19], a[208:211], v[208:211], 0\n"
      "v_cvt_pk_bf16_f32 v212, v200, v201\n"
      "v_exp_f32 v203, v203\n"
      "v_mfma_f32_16x16x32_bf16 v[20:23], a[212:215], v[208:211], 0\n"
      "v_exp_f32 v204, v204\n"
      "v_cvt_pk_bf16_f32 v213, v202, v203\n"
      "v_mfma_f32_16x16x32_bf16 v[24:27], a[216:219], v[208:211], 0\n"
      "v_exp_f32 v205, v205\n"
      "v_mfma_f32_16x16x32_bf16 v[28:31], a[220:223], v[208:211], 0\n"
      "v_exp_f32 v206, v206\n"
      "v_cvt_pk_bf16_f32 v214, v204, v205\n"
      "v_mfma_f32_16x16x32_bf16 a[224:227], a[248:251], v[208:211], 0\n"
      "s_add_u32 s70, s70, %16\n"
      "v_exp_f32 v207, v207\n"
      "s_nop 0\n"
      "v_cvt_pk_bf16_f32 v215, v206, v207\n"
      "v_mfma_f32_16x16x32_bf16 v[200:203], a[160:163], a[48:51], 0\n"
      "v_max3_f32 v228, v192, v193, v194\n"
      "v_max3_f32 v228, v228, v195, v196\n"
      "v_mfma_f32_16x16x32_bf16 v[204:207], a[176:179], a[48:51], 0\n"
      "v_max3_f32 v228, v228, v197, v198\n"
      "v_max_f32 v228, v228, v199\n"
      "v_mfma_f32_16x16x32_bf16 v[200:203], a[164:167], a[52:55], v[200:203]\n"
      "v_mov_b32 v229, v228\n"
      "s_nop 1\n"
      "v_permlane16_swap_b32 v228, v229\n"
      "v_mfma_f32_16x16x32_bf16 v[204:207], a[180:183], a[52:55], v[204:207]\n"
      "s_and_b32 s70, s70, 0x1ffff\n"
      "v_max_f32 v228, v228, v229\n"
      "v_mov_b32 v229, v228\n"
      "v_mfma_f32_16x16x32_bf16 v[200:203], a[168:171], a[56:59], v[200:203]\n"
      "s_nop 0\n"
      "v_permlane32_swap_b32 v228, v229\n"
      "v_mfma_f32_16x16x32_bf16 v[204:207], a[184:187], a[56:59], v[204:207]\n"
      "v_max_f32 v228, v228, v229\n"
      "v_mul_f32 v218, s76, v228\n"
      "v_mfma_f32_16x16x32_bf16 v[200:203], a[172:175], a[60:63], v[200:203]\n"
      "v_fma_f32 v192, v192, s76, -v218\n"
      "v_fma_f32 v193, v193, s76, -v218\n"
      "v_mfma_f32_16x16x32_bf16 v[204:207], a[188:191], a[60:63], v[204:207]\n"
      "s_add_u32 m0, s79, 4096\n"
      "v_fma_f32 v194, v194, s76, -v218\n"
      "v_fma_f32 v195, v195, s76, -v218\n"
      "v_mfma_f32_16x16x32_bf16 v[32:35], a[192:195], v[212:215], 0\n"
      "v_fma_f32 v196, v196, s76, -v218\n"
      "v_fma_f32 v197, v197, s76, -v218\n"
      "v_mfma_f32_16x16x32_bf16 v[36:39], a[196:199], v[212:215], 0\n"
      "v_fma_f32 v198, v198, s76, -v218\n"
      "v_fma_f32 v199, v199, s76, -v218\n"
      "v_mfma_f32_16x16x32_bf16 v[40:43], a[200:203], v[212:215], 0\n"
      "v_exp_f32 v192, v192\n"
      "v_mfma_f32_16x16x32_bf16 v[44:47], a[204:207], v[212:215], 0\n"
      "buffer_load_dwordx4 v234, s[44:47], s70 offen offset:0 nt lds\n"
      "v_exp_f32 v193, v193\n"
      "v_exp_f32 v194, v194\n"
      "v_mfma_f32_16x16x32_bf16 v[48:51], a[208:211], v[212:215], 0\n"
      "v_cvt_pk_bf16_f32 v208, v192, v193\n"
      "v_exp_f32 v195, v195\n"
      "v_mfma_f32_16x16x32_bf16 v[52:55], a[212:215], v[212:215], 0\n"
      "v_exp_f32 v196, v196\n"
      "v_cvt_pk_bf16_f32 v209, v194, v195\n"
      "v_mfma_f32_16x16x32_bf16 v[56:59], a[216:219], v[212:215], 0\n"
      "v_exp_f32 v197, v197\n"
      "v_mfma_f32_16x16x32_bf16 v[60:63], a[220:223], v[212:215], 0\n"
      "buffer_load_dwordx4 v234, s[44:47], s70 offen offset:1024 nt lds\n"
      "v_exp_f32 v198, v198\n"
      "v_cvt_pk_bf16_f32 v210, v196, v197\n"
      "v_mfma_f32_16x16x32_bf16 a[228:231], a[248:251], v[212:215], 0\n"
      "v_exp_f32 v199, v199\n"
      "s_nop 0\n"
      "v_cvt_pk_bf16_f32 v211, v198, v199\n"
      "v_mfma_f32_16x16x32_bf16 v[192:195], a[160:163], a[64:67], 0\n"
      "v_max3_f32 v228, v200, v201, v202\n"
      "v_max3_f32 v228, v228, v203, v204\n"
      "v_mfma_f32_16x16x32_bf16 v[196:199], a[176:179], a[64:67], 0\n"
      "v_max3_f32 v228, v228, v205, v206\n"
      "v_max_f32 v228, v228, v207\n"
      "v_mfma_f32_16x16x32_bf16 v[192:195], a[164:167], a[68:71], v[192:195]\n"
      "buffer_load_dwordx4 v235, s[44:47], s70 offen offset:2048 nt lds\n"
      "v_mov_b32 v229, v228\n"
      "s_nop 1\n"
      "v_permlane16_swap_b32 v228, v229\n"
      "v_mfma_f32_16x16x32_bf16 v[196:199], a[180:183], a[68:71], v[196:199]\n"
      "v_max_f32 v228, v228, v229\n"
      "v_mov_b32 v229, v228\n"
      "v_mfma_f32_16x16x32_bf16 v[192:195], a[168:171], a[72:75], v[192:195]\n"
      "s_nop 0\n"
      "v_permlane32_swap_b32 v228, v229\n"
      "v_mfma_f32_16x16x32_bf16 v[196:199], a[184:187], a[72:75], v[196:199]\n"
      "v_max_f32 v228, v228, v229\n"
      "v_mul_f32 v219, s76, v228\n"
      "v_mfma_f32_16x16x32_bf16 v[192:195], a[172:175], a[76:79], v[192:195]\n"
      "buffer_load_dwordx4 v235, s[44:47], s70 offen offset:3072 nt lds\n"
      "v_fma_f32 v200, v200, s76, -v219\n"
      "v_fma_f32 v201, v201, s76, -v219\n"
      "v_mfma_f32_16x16x32_bf16 v[196:199], a[188:191], a[76:79], v[196:199]\n"
      "v_fma_f32 v202, v202, s76, -v219\n"
      "v_fma_f32 v203, v203, s76, -v219\n"
      "v_mfma_f32_16x16x32_bf16 v[64:67], a[192:195], v[208:211], 0\n"
      "v_fma_f32 v204, v204, s76, -v219\n"
      "v_fma_f32 v205, v205, s76, -v219\n"
      "v_mfma_f32_16x16x32_bf16 v[68:71], a[196:199], v[208:211], 0\n"
      "v_fma_f32 v206, v206, s76, -v219\n"
      "v_fma_f32 v207, v207, s76, -v219\n"
      "v_mfma_f32_16x16x32_bf16 v[72:75], a[200:203], v[208:211], 0\n"
      "s_add_u32 s70, s70, %16\n"
      "v_exp_f32 v200, v200\n"
      "v_mfma_f32_16x16x32_bf16 v[76:79], a[204:207], v[208:211], 0\n"
      "v_exp_f32 v201, v201\n"
      "v_exp_f32 v202, v202\n"
      "v_mfma_f32_16x16x32_bf16 v[80:83], a[208:211], v[208:211], 0\n"
      "v_cvt_pk_bf16_f32 v212, v200, v201\n"
      "v_exp_f32 v203, v203\n"
      "v_mfma_f32_16x16x32_bf16 v[84:87], a[212:215], v[208:211], 0\n"
      "v_exp_f32 v204, v204\n"
      "v_cvt_pk_bf16_f32 v213, v202, v203\n"
      "v_mfma_f32_16x16x32_bf16 v[88:91], a[216:219], v[208:211], 0\n"
      "s_and_b32 s70, s70, 0x1ffff\n"
      "v_exp_f32 v205, v205\n"
      "v_mfma_f32_16x16x32_bf16 v[92:95], a[220:223], v[208:211], 0\n"
      "v_exp_f32 v206, v206\n"
      "v_cvt_pk_bf16_f32 v214, v204, v205\n"
      "v_mfma_f32_16x16x32_bf16 a[232:235], a[248:251], v[208:211], 0\n"
      "v_exp_f32 v207, v207\n"
      "s_nop 0\n"
      "v_cvt_pk_bf16_f32 v215, v206, v207\n"
      "v_mfma_f32_16x16x32_bf16 v[200:203], a[160:163], a[80:83], 0\n"
      "s_waitcnt vmcnt(16)\n"
      "ds_read_b128 a[160:163], v231 offset:0\n"
      "v_max3_f32 v228, v192, v193, v194\n"
      "v_max3_f32 v228, v228, v195, v196\n"
      "v_mfma_f32_16x16x32_bf16 v[204:207], a[176:179], a[80:83], 0\n"
      "s_xor_b32 s79, s79, 65536\n"
      "ds_read_b128 a[176:179], v231 offset:4096\n"
      "v_max3_f32 v228, v228, v197, v198\n"
      "v_max_f32 v228, v228, v199\n"
      "v_mfma_f32_16x16x32_bf16 v[200:203], a[164:167], a[84:87], v[200:203]\n"
      "ds_read_b128 a[164:167], v231 offset:256\n"
      "v_mov_b32 v229, v228\n"
      "s_nop 1\n"
      "v_permlane16_swap_b32 v228, v229\n"
      "v_mfma_f32_16x16x32_bf16 v[204:207], a[180:183], a[84:87], v[204:207]\n"
      "ds_read_b128 a[180:183], v231 offset:4352\n"
      "v_max_f32 v228, v228, v229\n"
      "v_mov_b32 v229, v228\n"
      "v_mfma_f32_16x16x32_bf16 v[200:203], a[168:171], a[88:91], v[200:203]\n"
      "ds_read_b128 a[168:171], v231 offset:512\n"
      "v_permlane32_swap_b32 v228, v229\n"
      "v_mfma_f32_16x16x32_bf16 v[204:207], a[184:187], a[88:91], v[204:207]\n"
      "s_add_u32 m0, s80, 0\n"
      "ds_read_b128 a[184:187], v231 offset:4608\n"
      "v_max_f32 v228, v228, v229\n"
      "v_mul_f32 v220, s76, v228\n"
      "v_mfma_f32_16x16x32_bf16 v[200:203], a[172:175], a[92:95], v[200:203]\n"
      "ds_read_b128 a[172:175], v231 offset:768\n"
      "v_fma_f32 v192, v192, s76, -v220\n"
      "v_fma_f32 v193, v193, s76, -v220\n"
      "v_mfma_f32_16x16x32_bf16 v[204:207], a[188:191], a[92:95], v[204:207]\n"
      "ds_read_b128 a[188:191], v231 offset:4864\n"
      "v_fma_f32 v194, v194, s76, -v220\n"
      "v_fma_f32 v195, v195, s76, -v220\n"
      "v_mfma_f32_16x16x32_bf16 v[96:99], a[192:195], v[212:215], 0\n"
      "v_fma_f32 v196, v196, s76, -v220\n"
      "v_fma_f32 v197, v197, s76, -v220\n"
      "v_mfma_f32_16x16x32_bf16 v[100:103], a[196:199], v[212:215], 0\n"
      "buffer_load_dwordx4 v234, s[48:51], s71 offen offset:0 nt lds\n"
      "v_fma_f32 v198, v198, s76, -v220\n"
      "v_fma_f32 v199, v199, s76, -v220\n"
      "v_mfma_f32_16x16x32_bf16 v[104:107], a[200:203], v[212:215], 0\n"
      "v_exp_f32 v192, v192\n"
      "v_mfma_f32_16x16x32_bf16 v[108:111], a[204:207], v[212:215], 0\n"
      "v_exp_f32 v193, v193\n"
      "v_exp_f32 v194, v194\n"
      "v_mfma_f32_16x16x32_bf16 v[112:115], a[208:211], v[212:215], 0\n"
      "v_cvt_pk_bf16_f32 v208, v192, v193\n"
      "v_exp_f32 v195, v195\n"
      "v_mfma_f32_16x16x32_bf16 v[116:119], a[212:215], v[212:215], 0\n"
      "v_exp_f32 v196, v196\n"
      "v_cvt_pk_bf16_f32 v209, v194, v195\n"
      "v_mfma_f32_16x16x32_bf16 v[120:123], a[216:219], v[212:215], 0\n"
      "buffer_load_dwordx4 v236, s[48:51], s71 offen offset:1024 nt lds\n"
      "v_exp_f32 v197, v197\n"
      "v_mfma_f32_16x16x32_bf16 v[124:127], a[220:223], v[212:215], 0\n"
      "v_exp_f32 v198, v198\n"
      "v_cvt_pk_bf16_f32 v210, v196, v197\n"
      "v_mfma_f32_16x16x32_bf16 a[236:239], a[248:251], v[212:215], 0\n"
      "v_exp_f32 v199, v199\n"
      "s_nop 0\n"
      "v_cvt_pk_bf16_f32 v211, v198, v199\n"
      "v_mfma_f32_16x16x32_bf16 v[192:195], a[96:99], a[0:3], 0\n"
      "v_max3_f32 v228, v200, v201, v202\n"
      "v_max3_f32 v228, v228, v203, v204\n"
      "v_mfma_f32_16x16x32_bf16 v[196:199], a[112:115], a[0:3], 0\n"
      "buffer_load_dwordx4 v234, s[48:51], s71 offen offset:2048 nt lds\n"
      "v_max3_f32 v228, v228, v205, v206\n"
      "v_max_f32 v228, v228, v207\n"
      "v_mfma_f32_16x16x32_bf16 v[192:195], a[100:103], a[4:7], v[192:195]\n"
      "v_mov_b32 v229, v228\n"
      "s_nop 1\n"
      "v_permlane16_swap_b32 v228, v229\n"
      "v_mfma_f32_16x16x32_bf16 v[196:199], a[116:119], a[4:7], v[196:199]\n"
      "v_max_f32 v228, v228, v229\n"
      "v_mov_b32 v229, v228\n"
      "v_mfma_f32_16x16x32_bf16 v[192:195], a[104:107], a[8:11], v[192:195]\n"
      "s_nop 0\n"
      "v_permlane32_swap_b32 v228, v229\n"
      "v_mfma_f32_16x16x32_bf16 v[196:199], a[120:123], a[8:11], v[196:199]\n"
      "buffer_load_dwordx4 v236, s[48:51], s71 offen offset:3072 nt lds\n"
      "v_max_f32 v228, v228, v229\n"
      "v_mul_f32 v221, s76, v228\n"
      "v_mfma_f32_16x16x32_bf16 v[192:195], a[108:111], a[12:15], v[192:195]\n"
      "v_fma_f32 v200, v200, s76, -v221\n"
      "v_fma_f32 v201, v201, s76, -v221\n"
      "v_mfma_f32_16x16x32_bf16 v[196:199], a[124:127], a[12:15], v[196:199]\n"
      "v_fma_f32 v202, v202, s76, -v221\n"
      "v_fma_f32 v203, v203, s76, -v221\n"
      "v_mfma_f32_16x16x32_bf16 v[128:131], a[192:195], v[208:211], 0\n"
      "v_fma_f32 v204, v204, s76, -v221\n"
      "v_fma_f32 v205, v205, s76, -v221\n"
      "v_xor_b32 v231, 65536, v231\n"
      "v_mfma_f32_16x16x32_bf16 v[132:135], a[196:199], v[208:211], 0\n"
      "v_fma_f32 v206, v206, s76, -v221\n"
      "v_fma_f32 v207, v207, s76, -v221\n"
      "v_mfma_f32_16x16x32_bf16 v[136:139], a[200:203], v[208:211], 0\n"
      "s_add_u32 s71, s71, %16\n"
      "v_exp_f32 v200, v200\n"
      "v_mfma_f32_16x16x32_bf16 v[140:143], a[204:207], v[208:211], 0\n"
      "v_exp_f32 v201, v201\n"
      "v_exp_f32 v202, v202\n"
      "v_mfma_f32_16x16x32_bf16 v[144:147], a[208:211], v[208:211], 0\n"
      "v_cvt_pk_bf16_f32 v212, v200, v201\n"
      "v_exp_f32 v203, v203\n"
      "v_mfma_f32_16x16x32_bf16 v[148:151], a[212:215], v[208:211], 0\n"
      "v_exp_f32 v204, v204\n"
      "v_cvt_pk_bf16_f32 v213, v202, v203\n"
      "v_mfma_f32_16x16x32_bf16 v[152:155], a[216:219], v[208:211], 0\n"
      "s_and_b32 s71, s71, 0x1ffff\n"
      "v_exp_f32 v205, v205\n"
      "v_mfma_f32_16x16x32_bf16 v[156:159], a[220:223], v[208:211], 0\n"
      "v_exp_f32 v206, v206\n"
      "v_cvt_pk_bf16_f32 v214, v204, v205\n"
      "v_mfma_f32_16x16x32_bf16 a[240:243], a[248:251], v[208:211], 0\n"
      "v_exp_f32 v207, v207\n"
      "s_nop 0\n"
      "v_cvt_pk_bf16_f32 v215, v206, v207\n"
      "v_mfma_f32_16x16x32_bf16 v[200:203], a[96:99], a[16:19], 0\n"
      "v_fma_f32 v192, v192, s76, -v216\n"
      "v_fma_f32 v193, v193, s76, -v216\n"
      "v_mfma_f32_16x16x32_bf16 v[204:207], a[112:115], a[16:19], 0\n"
      "v_fma_f32 v194, v194, s76, -v216\n"
      "v_max3_f32 v228, v192, v193, v194\n"
      "v_mfma_f32_16x16x32_bf16 v[200:203], a[100:103], a[20:23], v[200:203]\n"
      "s_add_u32 m0, s80, 4096\n"
      "v_fma_f32 v195, v195, s76, -v216\n"
      "v_fma_f32 v196, v196, s76, -v216\n"
      "v_mfma_f32_16x16x32_bf16 v[204:207], a[116:119], a[20:23], v[204:207]\n"
      "buffer_load_dwordx4 v234, s[48:51], s71 offen offset:0 nt lds\n"
      "v_max3_f32 v228, v228, v195, v196\n"
      "v_mfma_f32_16x16x32_bf16 v[200:203], a[104:107], a[24:27], v[200:203]\n"
      "v_fma_f32 v197, v197, s76, -v216\n"
      "v_fma_f32 v198, v198, s76, -v216\n"
      "v_mfma_f32_16x16x32_bf16 v[204:207], a[120:123], a[24:27], v[204:207]\n"
      "v_max3_f32 v228, v228, v197, v198\n"
      "v_fma_f32 v199, v199, s76, -v216\n"
      "v_mfma_f32_16x16x32_bf16 v[200:203], a[108:111], a[28:31], v[200:203]\n"
      "v_max_f32 v228, v228, v199\n"
      "v_cmp_nge_f32 vcc, s77, v228\n"
      "v_mfma_f32_16x16x32_bf16 v[204:207], a[124:127], a[28:31], v[204:207]\n"
      "buffer_load_dwordx4 v236, s[48:51], s71 offen offset:1024 nt lds\n"
      "buffer_load_dwordx4 v234, s[48:51], s71 offen offset:2048 nt lds\n"
      "v_mfma_f32_16x16x32_bf16 v[160:163], a[192:195], v[212:215], 0\n"
      "s_waitcnt vmcnt(15)\n"
      "s_cbranch_vccnz CRx_0_%=\n"
      "BRx_0_%=:\n"
      "ds_read_b64_tr_b16 a[192:193], v232 offset:0\n"
      "v_exp_f32 v192, v192\n"
      "v_mfma_f32_16x16x32_bf16 v[164:167], a[196:199], v[212:215], 0\n"
      "ds_read_b64_tr_b16 a[194:195], v232 offset:4096\n"
      "v_exp_f32 v193, v193\n"
      "v_mfma_f32_16x16x32_bf16 v[168:171], a[200:203], v[212:215], 0\n"
      "ds_read_b64_tr_b16 a[196:197], v233 offset:0\n"
      "v_exp_f32 v194, v194\n"
      "v_cvt_pk_bf16_f32 v208, v192, v193\n"
      "v_mfma_f32_16x16x32_bf16 v[172:175], a[204:207], v[212:215], 0\n"
      "ds_read_b64_tr_b16 a[198:199], v233 offset:4096\n"
      "v_exp_f32 v195, v195\n"
      "v_mfma_f32_16x16x32_bf16 v[176:179], a[208:211], v[212:215], 0\n"
      "ds_read_b64_tr_b16 a[200:201], v232 offset:256\n"
      "v_exp_f32 v196, v196\n"
      "v_cvt_pk_bf16_f32 v209, v194, v195\n"
      "v_mfma_f32_16x16x32_bf16 v[180:183], a[212:215], v[212:215], 0\n"
      "ds_read_b64_tr_b16 a[202:203], v232 offset:4352\n"
      "v_exp_f32 v197, v197\n"
      "v_mfma_f32_16x16x32_bf16 v[184:187], a[216:219], v[212:215], 0\n"
      "ds_read_b64_tr_b16 a[204:205], v233 offset:256\n"
      "v_exp_f32 v198, v198\n"
      "v_cvt_pk_bf16_f32 v210, v196, v197\n"
      "v_mfma_f32_16x16x32_bf16 v[188:191], a[220:223], v[212:215], 0\n"
      "ds_read_b64_tr_b16 a[206:207], v233 offset:4352\n"
      "v_exp_f32 v199, v199\n"
      "v_mfma_f32_16x16x32_bf16 a[244:247], a[248:251], v[212:215], 0\n"
      "ds_read_b64_tr_b16 a[208:209], v232 offset:512\n"
      "v_cvt_pk_bf16_f32 v211, v198, v199\n"
      "v_mfma_f32_16x16x32_bf16 v[192:195], a[96:99], a[32:35], 0\n"
      "ds_read_b64_tr_b16 a[210:211], v232 offset:4608\n"
      "v_fma_f32 v200, v200, s76, -v217\n"
      "v_fma_f32 v201, v201, s76, -v217\n"
      "v_mfma_f32_16x16x32_bf16 v[196:199], a[112:115], a[32:35], 0\n"
      "ds_read_b64_tr_b16 a[212:213], v233 offset:512\n"
      "v_fma_f32 v202, v202, s76, -v217\n"
      "v_max3_f32 v228, v200, v201, v202\n"
      "v_mfma_f32_16x16x32_bf16 v[192:195], a[100:103], a[36:39], v[192:195]\n"
      "ds_read_b64_tr_b16 a[214:215], v233 offset:4608\n"
      "v_fma_f32 v203, v203, s76, -v217\n"
      "v_fma_f32 v204, v204, s76, -v217\n"
      "v_mfma_f32_16x16x32_bf16 v[196:199], a[116:119], a[36:39], v[196:199]\n"
      "ds_read_b64_tr_b16 a[216:217], v232 offset:768\n"
      "v_max3_f32 v228, v228, v203, v204\n"
      "v_mfma_f32_16x16x32_bf16 v[192:195], a[104:107], a[40:43], v[192:195]\n"
      "ds_read_b64_tr_b16 a[218:219], v232 offset:4864\n"
      "v_fma_f32 v205, v205, s76, -v217\n"
      "v_fma_f32 v206, v206, s76, -v217\n"
      "v_mfma_f32_16x16x32_bf16 v[196:199], a[120:123], a[40:43], v[196:199]\n"
      "ds_read_b64_tr_b16 a[220:221], v233 offset:768\n"
      "v_max3_f32 v228, v228, v205, v206\n"
      "v_fma_f32 v207, v207, s76, -v217\n"
      "v_mfma_f32_16x16x32_bf16 v[192:195], a[108:111], a[44:47], v[192:195]\n"
      "buffer_load_dwordx4 v236, s[48:51], s71 offen offset:3072 nt lds\n"
      "s_add_u32 s71, s71, %16\n"
      "v_max_f32 v228, v228, v207\n"
      "v_cmp_nge_f32 vcc, s77, v228\n"
      "v_mfma_f32_16x16x32_bf16 v[196:199], a[124:127], a[44:47], v[196:199]\n"
      "ds_read_b64_tr_b16 a[222:223], v233 offset:4864\n"
      "v_mfma_f32_16x16x32_bf16 v[0:3], a[128:131], v[208:211], v[0:3]\n"
      "s_cbranch_vccnz CRx_1_%=\n"
      "BRx_1_%=:\n"
      "v_exp_f32 v200, v200\n"
      "v_mfma_f32_16x16x32_bf16 v[4:7], a[132:135], v[208:211], v[4:7]\n"
      "v_exp_f32 v201, v201\n"
      "v_mfma_f32_16x16x32_bf16 v[8:11], a[136:139], v[208:211], v[8:11]\n"
      "s_and_b32 s71, s71, 0x1ffff\n"
      "v_exp_f32 v202, v202\n"
      "v_cvt_pk_bf16_f32 v212, v200, v201\n"
      "v_mfma_f32_16x16x32_bf16 v[12:15], a[140:143], v[208:211], v[12:15]\n"
      "v_exp_f32 v203, v203\n"
      "v_mfma_f32_16x16x32_bf16 v[16:19], a[144:147], v[208:211], v[16:19]\n"
      "v_exp_f32 v204, v204\n"
      "v_cvt_pk_bf16_f32 v213, v202, v203\n"
      "v_mfma_f32_16x16x32_bf16 v[20:23], a[148:151], v[208:211], v[20:23]\n"
      "v_exp_f32 v205, v205\n"
      "v_mfma_f32_16x16x32_bf16 v[24:27], a[152:155], v[208:211], v[24:27]\n"
      "s_xor_b32 s80, s80, 65536\n"
      "v_exp_f32 v206, v206\n"
      "v_cvt_pk_bf16_f32 v214, v204, v205\n"
      "v_mfma_f32_16x16x32_bf16 v[28:31], a[156:159], v[208:211], v[28:31]\n"
      "v_exp_f32 v207, v207\n"
      "v_mfma_f32_16x16x32_bf16 a[224:227], a[248:251], v[208:211], a[224:227]\n"
      "v_cvt_pk_bf16_f32 v215, v206, v207\n"
      "v_mfma_f32_16x16x32_bf16 v[200:203], a[96:99], a[48:51], 0\n"
      "v_fma_f32 v192, v192, s76, -v218\n"
      "v_fma_f32 v193, v193, s76, -v218\n"
      "v_mfma_f32_16x16x32_bf16 v[204:207], a[112:115], a[48:51], 0\n"
      "v_fma_f32 v194, v194, s76, -v218\n"
      "v_max3_f32 v228, v192, v193, v194\n"
      "v_mfma_f32_16x16x32_bf16 v[200:203], a[100:103], a[52:55], v[200:203]\n"
      "s_add_u32 m0, s79, 0\n"
      "v_fma_f32 v195, v195, s76, -v218\n"
      "v_fma_f32 v196, v196, s76, -v218\n"
      "v_mfma_f32_16x16x32_bf16 v[204:207], a[116:119], a[52:55], v[204:207]\n"
      "s_waitcnt lgkmcnt(14)\n"
      "buffer_load_dwordx4 v234, s[44:47], s70 offen offset:0 nt lds\n"
      "v_max3_f32 v228, v228, v195, v196\n"
      "v_mfma_f32_16x16x32_bf16 v[200:203], a[104:107], a[56:59], v[200:203]\n"
      "v_fma_f32 v197, v197, s76, -v218\n"
      "v_fma_f32 v198, v198, s76, -v218\n"
      "v_mfma_f32_16x16x32_bf16 v[204:207], a[120:123], a[56:59], v[204:207]\n"
      "v_max3_f32 v228, v228, v197, v198\n"
      "v_fma_f32 v199, v199, s76, -v218\n"
      "v_mfma_f32_16x16x32_bf16 v[200:203], a[108:111], a[60:63], v[200:203]\n"
      "v_max_f32 v228, v228, v199\n"
      "v_cmp_nge_f32 vcc, s77, v228\n"
      "v_mfma_f32_16x16x32_bf16 v[204:207], a[124:127], a[60:63], v[204:207]\n"
      "buffer_load_dwordx4 v234, s[44:47], s70 offen offset:1024 nt lds\n"
      "v_xor_b32 v232, 65536, v232\n"
      "v_mfma_f32_16x16x32_bf16 v[32:35], a[128:131], v[212:215], v[32:35]\n"
      "s_cbranch_vccnz CRx_2_%=\n"
      "BRx_2_%=:\n"
      "v_exp_f32 v192, v192\n"
      "v_mfma_f32_16x16x32_bf16 v[36:39], a[132:135], v[212:215], v[36:39]\n"
      "v_exp_f32 v193, v193\n"
      "v_mfma_f32_16x16x32_bf16 v[40:43], a[136:139], v[212:215], v[40:43]\n"
      "v_exp_f32 v194, v194\n"
      "v_cvt_pk_bf16_f32 v208, v192, v193\n"
      "v_mfma_f32_16x16x32_bf16 v[44:47], a[140:143], v[212:215], v[44:47]\n"
      "v_exp_f32 v195, v195\n"
      "v_mfma_f32_16x16x32_bf16 v[48:51], a[144:147], v[212:215], v[48:51]\n"
      "v_exp_f32 v196, v196\n"
      "v_cvt_pk_bf16_f32 v209, v194, v195\n"
      "v_mfma_f32_16x16x32_bf16 v[52:55], a[148:151], v[212:215], v[52:55]\n"
      "v_exp_f32 v197, v197\n"
      "v_mfma_f32_16x16x32_bf16 v[56:59], a[152:155], v[212:215], v[56:59]\n"
      "v_exp_f32 v198, v198\n"
      "v_cvt_pk_bf16_f32 v210, v196, v197\n"
      "v_mfma_f32_16x16x32_bf16 v[60:63], a[156:159], v[212:215], v[60:63]\n"
      "v_exp_f32 v199, v199\n"
      "v_mfma_f32_16x16x32_bf16 a[228:231], a[248:251], v[212:215], a[228:231]\n"
      "buffer_load_dwordx4 v235, s[44:47], s70 offen offset:2048 nt lds\n"
      "v_cvt_pk_bf16_f32 v211, v198, v199\n"
      "v_mfma_f32_16x16x32_bf16 v[192:195], a[96:99], a[64:67], 0\n"
      "v_fma_f32 v200, v200, s76, -v219\n"
      "v_fma_f32 v201, v201, s76, -v219\n"
      "v_mfma_f32_16x16x32_bf16 v[196:199], a[112:115], a[64:67], 0\n"
      "v_fma_f32 v202, v202, s76, -v219\n"
      "v_max3_f32 v228, v200, v201, v202\n"
      "v_mfma_f32_16x16x32_bf16 v[192:195], a[100:103], a[68:71], v[192:195]\n"
      "v_fma_f32 v203, v203, s76, -v219\n"
      "v_fma_f32 v204, v204, s76, -v219\n"
      "v_mfma_f32_16x16x32_bf16 v[196:199], a[116:119], a[68:71], v[196:199]\n"
      "buffer_load_dwordx4 v235, s[44:47], s70 offen offset:3072 nt lds\n"
      "v_max3_f32 v228, v228, v203, v204\n"
      "v_mfma_f32_16x16x32_bf16 v[192:195], a[104:107], a[72:75], v[192:195]\n"
      "v_fma_f32 v205, v205, s76, -v219\n"
      "v_fma_f32 v206, v206, s76, -v219\n"
      "v_mfma_f32_16x16x32_bf16 v[196:199], a[120:123], a[72:75], v[196:199]\n"
      "v_max3_f32 v228, v228, v205, v206\n"
      "v_fma_f32 v207, v207, s76, -v219\n"
      "v_mfma_f32_16x16x32_bf16 v[192:195], a[108:111], a[76:79], v[192:195]\n"
      "v_max_f32 v228, v228, v207\n"
      "v_cmp_nge_f32 vcc, s77, v228\n"
      "v_mfma_f32_16x16x32_bf16 v[196:199], a[124:127], a[76:79], v[196:199]\n"
      "v_xor_b32 v233, 65536, v233\n"
      "v_mfma_f32_16x16x32_bf16 v[64:67], a[128:131], v[208:211], v[64:67]\n"
      "s_cbranch_vccnz CRx_3_%=\n"
      "BRx_3_%=:\n"
      "v_exp_f32 v200, v200\n"
      "v_mfma_f32_16x16x32_bf16 v[68:71], a[132:135], v[208:211], v[68:71]\n"
      "s_add_u32 s70, s70, %16\n"
      "v_exp_f32 v201, v201\n"
      "v_mfma_f32_16x16x32_bf16 v[72:75], a[136:139], v[208:211], v[72:75]\n"
      "v_exp_f32 v202, v202\n"
      "v_cvt_pk_bf16_f32 v212, v200, v201\n"
      "v_mfma_f32_16x16x32_bf16 v[76:79], a[140:143], v[208:211], v[76:79]\n"
      "v_exp_f32 v203, v203\n"
      "v_mfma_f32_16x16x32_bf16 v[80:83], a[144:147], v[208:211], v[80:83]\n"
      "v_exp_f32 v204, v204\n"
      "v_cvt_pk_bf16_f32 v213, v202, v203\n"
      "v_mfma_f32_16x16x32_bf16 v[84:87], a[148:151], v[208:211], v[84:87]\n"
      "v_exp_f32 v205, v205\n"
      "v_mfma_f32_16x16x32_bf16 v[88:91], a[152:155], v[208:211], v[88:91]\n"
      "s_and_b32 s70, s70, 0x1ffff\n"
      "v_exp_f32 v206, v206\n"
      "v_cvt_pk_bf16_f32 v214, v204, v205\n"
      "v_mfma_f32_16x16x32_bf16 v[92:95], a[156:159], v[208:211], v[92:95]\n"
      "v_exp_f32 v207, v207\n"
      "v_mfma_f32_16x16x32_bf16 a[232:235], a[248:251], v[208:211], a[232:235]\n"
      "v_cvt_pk_bf16_f32 v215, v206, v207\n"
      "v_mfma_f32_16x16x32_bf16 v[200:203], a[96:99], a[80:83], 0\n"
      "s_waitcnt vmcnt(12)\n"
      "ds_read_b128 a[96:99], v231 offset:0\n"
      "v_fma_f32 v192, v192, s76, -v220\n"
      "v_fma_f32 v193, v193, s76, -v220\n"
      "v_mfma_f32_16x16x32_bf16 v[204:207], a[112:115], a[80:83], 0\n"
      "ds_read_b128 a[112:115], v231 offset:4096\n"
      "v_fma_f32 v194, v194, s76, -v220\n"
      "v_max3_f32 v228, v192, v193, v194\n"
      "v_mfma_f32_16x16x32_bf16 v[200:203], a[100:103], a[84:87], v[200:203]\n"
      "s_add_u32 m0, s79, 4096\n"
      "ds_read_b128 a[100:103], v231 offset:256\n"
      "v_fma_f32 v195, v195, s76, -v220\n"
      "v_fma_f32 v196, v196, s76, -v220\n"
      "v_mfma_f32_16x16x32_bf16 v[204:207], a[116:119], a[84:87], v[204:207]\n"
      "ds_read_b128 a[116:119], v231 offset:4352\n"
      "v_max3_f32 v228, v228, v195, v196\n"
      "v_mfma_f32_16x16x32_bf16 v[200:203], a[104:107], a[88:91], v[200:203]\n"
      "ds_read_b128 a[104:107], v231 offset:512\n"
      "v_fma_f32 v197, v197, s76, -v220\n"
      "v_fma_f32 v198, v198, s76, -v220\n"
      "v_mfma_f32_16x16x32_bf16 v[204:207], a[120:123], a[88:91], v[204:207]\n"
      "ds_read_b128 a[120:123], v231 offset:4608\n"
      "v_max3_f32 v228, v228, v197, v198\n"
      "v_fma_f32 v199, v199, s76, -v220\n"
      "v_mfma_f32_16x16x32_bf16 v[200:203], a[108:111], a[92:95], v[200:203]\n"
      "ds_read_b128 a[108:111], v231 offset:768\n"
      "v_max_f32 v228, v228, v199\n"
      "v_cmp_nge_f32 vcc, s77, v228\n"
      "v_mfma_f32_16x16x32_bf16 v[204:207], a[124:127], a[92:95], v[204:207]\n"
      "ds_read_b128 a[124:127], v231 offset:4864\n"
      "buffer_load_dwordx4 v234, s[44:47], s70 offen offset:0 nt lds\n"
      "v_mfma_f32_16x16x32_bf16 v[96:99], a[128:131], v[212:215], v[96:99]\n"
      "s_cbranch_vccnz CRx_4_%=\n"
      "BRx_4_%=:\n"
      "v_exp_f32 v192, v192\n"
      "v_mfma_f32_16x16x32_bf16 v[100:103], a[132:135], v[212:215], v[100:103]\n"
      "v_exp_f32 v193, v193\n"
      "v_mfma_f32_16x16x32_bf16 v[104:107], a[136:139], v[212:215], v[104:107]\n"
      "v_exp_f32 v194, v194\n"
      "v_cvt_pk_bf16_f32 v208, v192, v193\n"
      "v_mfma_f32_16x16x32_bf16 v[108:111], a[140:143], v[212:215], v[108:111]\n"
      "v_exp_f32 v195, v195\n"
      "v_mfma_f32_16x16x32_bf16 v[112:115], a[144:147], v[212:215], v[112:115]\n"
      "v_exp_f32 v196, v196\n"
      "v_cvt_pk_bf16_f32 v209, v194, v195\n"
      "v_mfma_f32_16x16x32_bf16 v[116:119], a[148:151], v[212:215], v[116:119]\n"
      "v_exp_f32 v197, v197\n"
      "v_mfma_f32_16x16x32_bf16 v[120:123], a[152:155], v[212:215], v[120:123]\n"
      "v_exp_f32 v198, v198\n"
      "v_cvt_pk_bf16_f32 v210, v196, v197\n"
      "v_mfma_f32_16x16x32_bf16 v[124:127], a[156:159], v[212:215], v[124:127]\n"
      "v_exp_f32 v199, v199\n"
      "v_mfma_f32_16x16x32_bf16 a[236:239], a[248:251], v[212:215], a[236:239]\n"
      "buffer_load_dwordx4 v234, s[44:47], s70 offen offset:1024 nt lds\n"
      "v_cvt_pk_bf16_f32 v211, v198, v199\n"
      "v_mfma_f32_16x16x32_bf16 v[192:195], a[160:163], a[0:3], 0\n"
      "v_fma_f32 v200, v200, s76, -v221\n"
      "v_fma_f32 v201, v201, s76, -v221\n"
      "v_mfma_f32_16x16x32_bf16 v[196:199], a[176:179], a[0:3], 0\n"
      "v_fma_f32 v202, v202, s76, -v221\n"
      "v_max3_f32 v228, v200, v201, v202\n"
      "v_mfma_f32_16x16x32_bf16 v[192:195], a[164:167], a[4:7], v[192:195]\n"
      "v_fma_f32 v203, v203, s76, -v221\n"
      "v_fma_f32 v204, v204, s76, -v221\n"
      "v_mfma_f32_16x16x32_bf16 v[196:199], a[180:183], a[4:7], v[196:199]\n"
      "buffer_load_dwordx4 v235, s[44:47], s70 offen offset:2048 nt lds\n"
      "v_max3_f32 v228, v228, v203, v204\n"
      "v_mfma_f32_16x16x32_bf16 v[192:195], a[168:171], a[8:11], v[192:195]\n"
      "v_fma_f32 v205, v205, s76, -v221\n"
      "v_fma_f32 v206, v206, s76, -v221\n"
      "v_mfma_f32_16x16x32_bf16 v[196:199], a[184:187], a[8:11], v[196:199]\n"
      "v_max3_f32 v228, v228, v205, v206\n"
      "v_fma_f32 v207, v207, s76, -v221\n"
      "v_mfma_f32_16x16x32_bf16 v[192:195], a[172:175], a[12:15], v[192:195]\n"
      "v_max_f32 v228, v228, v207\n"
      "v_cmp_nge_f32 vcc, s77, v228\n"
      "v_mfma_f32_16x16x32_bf16 v[196:199], a[188:191], a[12:15], v[196:199]\n"
      "buffer_load_dwordx4 v235, s[44:47], s70 offen offset:3072 nt lds\n"
      "s_add_u32 s70, s70, %16\n"
      "v_xor_b32 v231, 65536, v231\n"
      "v_mfma_f32_16x16x32_bf16 v[128:131], a[128:131], v[208:211], v[128:131]\n"
      "s_cbranch_vccnz CRx_5_%=\n"
      "BRx_5_%=:\n"
      "v_exp_f32 v200, v200\n"
      "v_mfma_f32_16x16x32_bf16 v[132:135], a[132:135], v[208:211], v[132:135]\n"
      "v_exp_f32 v201, v201\n"
      "v_mfma_f32_16x16x32_bf16 v[136:139], a[136:139], v[208:211], v[136:139]\n"
      "s_and_b32 s70, s70, 0x1ffff\n"
      "v_exp_f32 v202, v202\n"
      "v_cvt_pk_bf16_f32 v212, v200, v201\n"
      "v_mfma_f32_16x16x32_bf16 v[140:143], a[140:143], v[208:211], v[140:143]\n"
      "v_exp_f32 v203, v203\n"
      "v_mfma_f32_16x16x32_bf16 v[144:147], a[144:147], v[208:211], v[144:147]\n"
      "v_exp_f32 v204, v204\n"
      "v_cvt_pk_bf16_f32 v213, v202, v203\n"
      "v_mfma_f32_16x16x32_bf16 v[148:151], a[148:151], v[208:211], v[148:151]\n"
      "v_exp_f32 v205, v205\n"
      "v_mfma_f32_16x16x32_bf16 v[152:155], a[152:155], v[208:211], v[152:155]\n"
      "s_xor_b32 s79, s79, 65536\n"
      "v_exp_f32 v206, v206\n"
      "v_cvt_pk_bf16_f32 v214, v204, v205\n"
      "v_mfma_f32_16x16x32_bf16 v[156:159], a[156:159], v[208:211], v[156:159]\n"
      "v_exp_f32 v207, v207\n"
      "v_mfma_f32_16x16x32_bf16 a[240:243], a[248:251], v[208:211], a[240:243]\n"
      "v_cvt_pk_bf16_f32 v215, v206, v207\n"
      "v_mfma_f32_16x16x32_bf16 v[200:203], a[160:163], a[16:19], 0\n"
      "v_fma_f32 v192, v192, s76, -v216\n"
      "v_fma_f32 v193, v193, s76, -v216\n"
      "v_mfma_f32_16x16x32_bf16 v[204:207], a[176:179], a[16:19], 0\n"
      "v_fma_f32 v194, v194, s76, -v216\n"
      "v_max3_f32 v228, v192, v193, v194\n"
      "v_mfma_f32_16x16x32_bf16 v[200:203], a[164:167], a[20:23], v[200:203]\n"
      "s_add_u32 m0, s80, 0\n"
      "v_fma_f32 v195, v195, s76, -v216\n"
      "v_fma_f32 v196, v196, s76, -v216\n"
      "v_mfma_f32_16x16x32_bf16 v[204:207], a[180:183], a[20:23], v[204:207]\n"
      "s_waitcnt lgkmcnt(8)\n"
      "buffer_load_dwordx4 v234, s[48:51], s71 offen offset:0 nt lds\n"
      "v_max3_f32 v228, v228, v195, v196\n"
      "v_mfma_f32_16x16x32_bf16 v[200:203], a[168:171], a[24:27], v[200:203]\n"
      "v_fma_f32 v197, v197, s76, -v216\n"
      "v_fma_f32 v198, v198, s76, -v216\n"
      "v_mfma_f32_16x16x32_bf16 v[204:207], a[184:187], a[24:27], v[204:207]\n"
      "v_max3_f32 v228, v228, v197, v198\n"
      "v_fma_f32 v199, v199, s76, -v216\n"
      "v_mfma_f32_16x16x32_bf16 v[200:203], a[172:175], a[28:31], v[200:203]\n"
      "v_max_f32 v228, v228, v199\n"
      "v_cmp_nge_f32 vcc, s77, v228\n"
      "v_mfma_f32_16x16x32_bf16 v[204:207], a[188:191], a[28:31], v[204:207]\n"
      "buffer_load_dwordx4 v236, s[48:51], s71 offen offset:1024 nt lds\n"
      "v_mfma_f32_16x16x32_bf16 v[160:163], a[128:131], v[212:215], v[160:163]\n"
      "s_waitcnt vmcnt(10)\n"
      "s_cbranch_vccnz CRy_0_%=\n"
      "BRy_0_%=:\n"
      "ds_read_b64_tr_b16 a[128:129], v232 offset:0\n"
      "v_exp_f32 v192, v192\n"
      "v_mfma_f32_16x16x32_bf16 v[164:167], a[132:135], v[212:215], v[164:167]\n"
      "ds_read_b64_tr_b16 a[130:131], v232 offset:4096\n"
      "v_exp_f32 v193, v193\n"
      "v_mfma_f32_16x16x32_bf16 v[168:171], a[136:139], v[212:215], v[168:171]\n"
      "ds_read_b64_tr_b16 a[132:133], v233 offset:0\n"
      "v_exp_f32 v194, v194\n"
      "v_cvt_pk_bf16_f32 v208, v192, v193\n"
      "v_mfma_f32_16x16x32_bf16 v[172:175], a[140:143], v[212:215], v[172:175]\n"
      "ds_read_b64_tr_b16 a[134:135], v233 offset:4096\n"
      "v_exp_f32 v195, v195\n"
      "v_mfma_f32_16x16x32_bf16 v[176:179], a[144:147], v[212:215], v[176:179]\n"
      "ds_read_b64_tr_b16 a[136:137], v232 offset:256\n"
      "v_exp_f32 v196, v196\n"
      "v_cvt_pk_bf16_f32 v209, v194, v195\n"
      "v_mfma_f32_16x16x32_bf16 v[180:183], a[148:151], v[212:215], v[180:183]\n"
      "ds_read_b64_tr_b16 a[138:139], v232 offset:4352\n"
      "v_exp_f32 v197, v197\n"
      "v_mfma_f32_16x16x32_bf16 v[184:187], a[152:155], v[212:215], v[184:187]\n"
      "ds_read_b64_tr_b16 a[140:141], v233 offset:256\n"
      "v_exp_f32 v198, v198\n"
      "v_cvt_pk_bf16_f32 v210, v196, v197\n"
      "v_mfma_f32_16x16x32_bf16 v[188:191], a[156:159], v[212:215], v[188:191]\n"
      "ds_read_b64_tr_b16 a[142:143], v233 offset:4352\n"
      "v_exp_f32 v199, v199\n"
      "v_mfma_f32_16x16x32_bf16 a[244:247], a[248:251], v[212:215], a[244:247]\n"
      "ds_read_b64_tr_b16 a[144:145], v232 offset:512\n"
      "v_cvt_pk_bf16_f32 v211, v198, v199\n"
      "v_mfma_f32_16x16x32_bf16 v[192:195], a[160:163], a[32:35], 0\n"
      "ds_read_b64_tr_b16 a[146:147], v232 offset:4608\n"
      "v_fma_f32 v200, v200, s76, -v217\n"
      "v_fma_f32 v201, v201, s76, -v217\n"
      "v_mfma_f32_16x16x32_bf16 v[196:199], a[176:179], a[32:35], 0\n"
      "ds_read_b64_tr_b16 a[148:149], v233 offset:512\n"
      "v_fma_f32 v202, v202, s76, -v217\n"
      "v_max3_f32 v228, v200, v201, v202\n"
      "v_mfma_f32_16x16x32_bf16 v[192:195], a[164:167], a[36:39], v[192:195]\n"
      "ds_read_b64_tr_b16 a[150:151], v233 offset:4608\n"
      "v_fma_f32 v203, v203, s76, -v217\n"
      "v_fma_f32 v204, v204, s76, -v217\n"
      "v_mfma_f32_16x16x32_bf16 v[196:199], a[180:183], a[36:39], v[196:199]\n"
      "ds_read_b64_tr_b16 a[152:153], v232 offset:768\n"
      "v_max3_f32 v228, v228, v203, v204\n"
      "v_mfma_f32_16x16x32_bf16 v[192:195], a[168:171], a[40:43], v[192:195]\n"
      "ds_read_b64_tr_b16 a[154:155], v232 offset:4864\n"
      "v_fma_f32 v205, v205, s76, -v217\n"
      "v_fma_f32 v206, v206, s76, -v217\n"
      "v_mfma_f32_16x16x32_bf16 v[196:199], a[184:187], a[40:43], v[196:199]\n"
      "ds_read_b64_tr_b16 a[156:157], v233 offset:768\n"
      "v_max3_f32 v228, v228, v205, v206\n"
      "v_fma_f32 v207, v207, s76, -v217\n"
      "v_mfma_f32_16x16x32_bf16 v[192:195], a[172:175], a[44:47], v[192:195]\n"
      "buffer_load_dwordx4 v234, s[48:51], s71 offen offset:2048 nt lds\n"
      "v_max_f32 v228, v228, v207\n"
      "v_cmp_nge_f32 vcc, s77, v228\n"
      "v_mfma_f32_16x16x32_bf16 v[196:199], a[188:191], a[44:47], v[196:199]\n"
      "ds_read_b64_tr_b16 a[158:159], v233 offset:4864\n"
      "buffer_load_dwordx4 v236, s[48:51], s71 offen offset:3072 nt lds\n"
      "v_mfma_f32_16x16x32_bf16 v[0:3], a[192:195], v[208:211], v[0:3]\n"
      "s_cbranch_vccnz CRy_1_%=\n"
      "BRy_1_%=:\n"
      "v_exp_f32 v200, v200\n"
      "v_mfma_f32_16x16x32_bf16 v[4:7], a[196:199], v[208:211], v[4:7]\n"
      "s_add_u32 s71, s71, %16\n"
      "v_exp_f32 v201, v201\n"
      "v_mfma_f32_16x16x32_bf16 v[8:11], a[200:203], v[208:211], v[8:11]\n"
      "v_exp_f32 v202, v202\n"
      "v_cvt_pk_bf16_f32 v212, v200, v201\n"
      "v_mfma_f32_16x16x32_bf16 v[12:15], a[204:207], v[208:211], v[12:15]\n"
      "v_exp_f32 v203, v203\n"
      "v_mfma_f32_16x16x32_bf16 v[16:19], a[208:211], v[208:211], v[16:19]\n"
      "v_exp_f32 v204, v204\n"
      "v_cvt_pk_bf16_f32 v213, v202, v203\n"
      "v_mfma_f32_16x16x32_bf16 v[20:23], a[212:215], v[208:211], v[20:23]\n"
      "v_exp_f32 v205, v205\n"
      "v_mfma_f32_16x16x32_bf16 v[24:27], a[216:219], v[208:211], v[24:27]\n"
      "s_and_b32 s71, s71, 0x1ffff\n"
      "v_exp_f32 v206, v206\n"
      "v_cvt_pk_bf16_f32 v214, v204, v205\n"
      "v_mfma_f32_16x16x32_bf16 v[28:31], a[220:223], v[208:211], v[28:31]\n"
      "v_exp_f32 v207, v207\n"
      "v_mfma_f32_16x16x32_bf16 a[224:227], a[248:251], v[208:211], a[224:227]\n"
      "v_cvt_pk_bf16_f32 v215, v206, v207\n"
      "v_mfma_f32_16x16x32_bf16 v[200:203], a[160:163], a[48:51], 0\n"
      "v_fma_f32 v192, v192, s76, -v218\n"
      "v_fma_f32 v193, v193, s76, -v218\n"
      "v_mfma_f32_16x16x32_bf16 v[204:207], a[176:179], a[48:51], 0\n"
      "v_fma_f32 v194, v194, s76, -v218\n"
      "v_max3_f32 v228, v192, v193, v194\n"
      "v_mfma_f32_16x16x32_bf16 v[200:203], a[164:167], a[52:55], v[200:203]\n"
      "s_add_u32 m0, s80, 4096\n"
      "v_fma_f32 v195, v195, s76, -v218\n"
      "v_fma_f32 v196, v196, s76, -v218\n"
      "v_mfma_f32_16x16x32_bf16 v[204:207], a[180:183], a[52:55], v[204:207]\n"
      "buffer_load_dwordx4 v234, s[48:51], s71 offen offset:0 nt lds\n"
      "v_max3_f32 v228, v228, v195, v196\n"
      "v_mfma_f32_16x16x32_bf16 v[200:203], a[168:171], a[56:59], v[200:203]\n"
      "v_fma_f32 v197, v197, s76, -v218\n"
      "v_fma_f32 v198, v198, s76, -v218\n"
      "v_mfma_f32_16x16x32_bf16 v[204:207], a[184:187], a[56:59], v[204:207]\n"
      "v_max3_f32 v228, v228, v197, v198\n"
      "v_fma_f32 v199, v199, s76, -v218\n"
      "v_mfma_f32_16x16x32_bf16 v[200:203], a[172:175], a[60:63], v[200:203]\n"
      "v_max_f32 v228, v228, v199\n"
      "v_cmp_nge_f32 vcc, s77, v228\n"
      "v_mfma_f32_16x16x32_bf16 v[204:207], a[188:191], a[60:63], v[204:207]\n"
      "buffer_load_dwordx4 v236, s[48:51], s71 offen offset:1024 nt lds\n"
      "v_xor_b32 v232, 65536, v232\n"
      "v_mfma_f32_16x16x32_bf16 v[32:35], a[192:195], v[212:215], v[32:35]\n"
      "s_cbranch_vccnz CRy_2_%=\n"
      "BRy_2_%=:\n"
      "v_exp_f32 v192, v192\n"
      "v_mfma_f32_16x16x32_bf16 v[36:39], a[196:199], v[212:215], v[36:39]\n"
      "v_exp_f32 v193, v193\n"
      "v_mfma_f32_16x16x32_bf16 v[40:43], a[200:203], v[212:215], v[40:43]\n"
      "v_exp_f32 v194, v194\n"
      "v_cvt_pk_bf16_f32 v208, v192, v193\n"
      "v_mfma_f32_16x16x32_bf16 v[44:47], a[204:207], v[212:215], v[44:47]\n"
      "v_exp_f32 v195, v195\n"
      "v_mfma_f32_16x16x32_bf16 v[48:51], a[208:211], v[212:215], v[48:51]\n"
      "v_exp_f32 v196, v196\n"
      "v_cvt_pk_bf16_f32 v209, v194, v195\n"
      "v_mfma_f32_16x16x32_bf16 v[52:55], a[212:215], v[212:215], v[52:55]\n"
      "v_exp_f32 v197, v197\n"
      "v_mfma_f32_16x16x32_bf16 v[56:59], a[216:219], v[212:215], v[56:59]\n"
      "v_exp_f32 v198, v198\n"
      "v_cvt_pk_bf16_f32 v210, v196, v197\n"
      "v_mfma_f32_16x16x32_bf16 v[60:63], a[220:223], v[212:215], v[60:63]\n"
      "v_exp_f32 v199, v199\n"
      "v_mfma_f32_16x16x32_bf16 a[228:231], a[248:251], v[212:215], a[228:231]\n"
      "buffer_load_dwordx4 v234, s[48:51], s71 offen offset:2048 nt lds\n"
      "v_cvt_pk_bf16_f32 v211, v198, v199\n"
      "v_mfma_f32_16x16x32_bf16 v[192:195], a[160:163], a[64:67], 0\n"
      "v_fma_f32 v200, v200, s76, -v219\n"
      "v_fma_f32 v201, v201, s76, -v219\n"
      "v_mfma_f32_16x16x32_bf16 v[196:199], a[176:179], a[64:67], 0\n"
      "v_fma_f32 v202, v202, s76, -v219\n"
      "v_max3_f32 v228, v200, v201, v202\n"
      "v_mfma_f32_16x16x32_bf16 v[192:195], a[164:167], a[68:71], v[192:195]\n"
      "v_fma_f32 v203, v203, s76, -v219\n"
      "v_fma_f32 v204, v204, s76, -v219\n"
      "v_mfma_f32_16x16x32_bf16 v[196:199], a[180:183], a[68:71], v[196:199]\n"
      "buffer_load_dwordx4 v236, s[48:51], s71 offen offset:3072 nt lds\n"
      "v_max3_f32 v228, v228, v203, v204\n"
      "v_mfma_f32_16x16x32_bf16 v[192:195], a[168:171], a[72:75], v[192:195]\n"
      "v_fma_f32 v205, v205, s76, -v219\n"
      "v_fma_f32 v206, v206, s76, -v219\n"
      "v_mfma_f32_16x16x32_bf16 v[196:199], a[184:187], a[72:75], v[196:199]\n"
      "v_max3_f32 v228, v228, v205, v206\n"
      "v_fma_f32 v207, v207, s76, -v219\n"
      "v_mfma_f32_16x16x32_bf16 v[192:195], a[172:175], a[76:79], v[192:195]\n"
      "s_add_u32 s71, s71, %16\n"
      "v_max_f32 v228, v228, v207\n"
      "v_cmp_nge_f32 vcc, s77, v228\n"
      "v_mfma_f32_16x16x32_bf16 v[196:199], a[188:191], a[76:79], v[196:199]\n"
      "v_xor_b32 v233, 65536, v233\n"
      "v_mfma_f32_16x16x32_bf16 v[64:67], a[192:195], v[208:211], v[64:67]\n"
      "s_cbranch_vccnz CRy_3_%=\n"
      "BRy_3_%=:\n"
      "v_exp_f32 v200, v200\n"
      "v_mfma_f32_16x16x32_bf16 v[68:71], a[196:199], v[208:211], v[68:71]\n"
      "v_exp_f32 v201, v201\n"
      "v_mfma_f32_16x16x32_bf16 v[72:75], a[200:203], v[208:211], v[72:75]\n"
      "s_and_b32 s71, s71, 0x1ffff\n"
      "v_exp_f32 v202, v202\n"
      "v_cvt_pk_bf16_f32 v212, v200, v201\n"
      "v_mfma_f32_16x16x32_bf16 v[76:79], a[204:207], v[208:211], v[76:79]\n"
      "v_exp_f32 v203, v203\n"
      "v_mfma_f32_16x16x32_bf16 v[80:83], a[208:211], v[208:211], v[80:83]\n"
      "v_exp_f32 v204, v204\n"
      "v_cvt_pk_bf16_f32 v213, v202, v203\n"
      "v_mfma_f32_16x16x32_bf16 v[84:87], a[212:215], v[208:211], v[84:87]\n"
      "v_exp_f32 v205, v205\n"
      "v_mfma_f32_16x16x32_bf16 v[88:91], a[216:219], v[208:211], v[88:91]\n"
      "s_xor_b32 s80, s80, 65536\n"
      "v_exp_f32 v206, v206\n"
      "v_cvt_pk_bf16_f32 v214, v204, v205\n"
      "v_mfma_f32_16x16x32_bf16 v[92:95], a[220:223], v[208:211], v[92:95]\n"
      "v_exp_f32 v207, v207\n"
      "v_mfma_f32_16x16x32_bf16 a[232:235], a[248:251], v[208:211], a[232:235]\n"
      "v_cvt_pk_bf16_f32 v215, v206, v207\n"
      "v_mfma_f32_16x16x32_bf16 v[200:203], a[160:163], a[80:83], 0\n"
      "v_fma_f32 v192, v192, s76, -v220\n"
      "v_fma_f32 v193, v193, s76, -v220\n"
      "v_mfma_f32_16x16x32_bf16 v[204:207], a[176:179], a[80:83], 0\n"
      "v_fma_f32 v194, v194, s76, -v220\n"
      "v_max3_f32 v228, v192, v193, v194\n"
      "v_mfma_f32_16x16x32_bf16 v[200:203], a[164:167], a[84:87], v[200:203]\n"
      "s_add_u32 m0, s79, 0\n"
      "v_fma_f32 v195, v195, s76, -v220\n"
      "v_fma_f32 v196, v196, s76, -v220\n"
      "v_mfma_f32_16x16x32_bf16 v[204:207], a[180:183], a[84:87], v[204:207]\n"
      "s_waitcnt lgkmcnt(14)\n"
      "buffer_load_dwordx4 v234, s[44:47], s70 offen offset:0 nt lds\n"
      "v_max3_f32 v228, v228, v195, v196\n"
      "v_mfma_f32_16x16x32_bf16 v[200:203], a[168:171], a[88:91], v[200:203]\n"
      "v_fma_f32 v197, v197, s76, -v220\n"
      "v_fma_f32 v198, v198, s76, -v220\n"
      "v_mfma_f32_16x16x32_bf16 v[204:207], a[184:187], a[88:91], v[204:207]\n"
      "v_max3_f32 v228, v228, v197, v198\n"
      "v_fma_f32 v199, v199, s76, -v220\n"
      "v_mfma_f32_16x16x32_bf16 v[200:203], a[172:175], a[92:95], v[200:203]\n"
      "v_max_f32 v228, v228, v199\n"
      "v_cmp_nge_f32 vcc, s77, v228\n"
      "v_mfma_f32_16x16x32_bf16 v[204:207], a[188:191], a[92:95], v[204:207]\n"
      "buffer_load_dwordx4 v234, s[44:47], s70 offen offset:1024 nt lds\n"
      "v_mfma_f32_16x16x32_bf16 v[96:99], a[192:195], v[212:215], v[96:99]\n"
      "s_cbranch_vccnz CRy_4_%=\n"
      "BRy_4_%=:\n"
      "v_exp_f32 v192, v192\n"
      "v_mfma_f32_16x16x32_bf16 v[100:103], a[196:199], v[212:215], v[100:103]\n"
      "v_exp_f32 v193, v193\n"
      "v_mfma_f32_16x16x32_bf16 v[104:107], a[200:203], v[212:215], v[104:107]\n"
      "v_exp_f32 v194, v194\n"
      "v_cvt_pk_bf16_f32 v208, v192, v193\n"
      "v_mfma_f32_16x16x32_bf16 v[108:111], a[204:207], v[212:215], v[108:111]\n"
      "v_exp_f32 v195, v195\n"
      "v_mfma_f32_16x16x32_bf16 v[112:115], a[208:211], v[212:215], v[112:115]\n"
      "v_exp_f32 v196, v196\n"
      "v_cvt_pk_bf16_f32 v209, v194, v195\n"
      "v_mfma_f32_16x16x32_bf16 v[116:119], a[212:215], v[212:215], v[116:119]\n"
      "v_exp_f32 v197, v197\n"
      "v_mfma_f32_16x16x32_bf16 v[120:123], a[216:219], v[212:215], v[120:123]\n"
      "v_exp_f32 v198, v198\n"
      "v_cvt_pk_bf16_f32 v210, v196, v197\n"
      "v_mfma_f32_16x16x32_bf16 v[124:127], a[220:223], v[212:215], v[124:127]\n"
      "v_exp_f32 v199, v199\n"
      "v_mfma_f32_16x16x32_bf16 a[236:239], a[248:251], v[212:215], a[236:239]\n"
      "buffer_load_dwordx4 v235, s[44:47], s70 offen offset:2048 nt lds\n"
      "v_cvt_pk_bf16_f32 v211, v198, v199\n"
      "v_mfma_f32_16x16x32_bf16 v[192:195], a[96:99], a[0:3], 0\n"
      "v_fma_f32 v200, v200, s76, -v221\n"
      "v_fma_f32 v201, v201, s76, -v221\n"
      "v_mfma_f32_16x16x32_bf16 v[196:199], a[112:115], a[0:3], 0\n"
      "v_fma_f32 v202, v202, s76, -v221\n"
      "v_max3_f32 v228, v200, v201, v202\n"
      "v_mfma_f32_16x16x32_bf16 v[192:195], a[100:103], a[4:7], v[192:195]\n"
      "v_fma_f32 v203, v203, s76, -v221\n"
      "v_fma_f32 v204, v204, s76, -v221\n"
      "v_mfma_f32_16x16x32_bf16 v[196:199], a[116:119], a[4:7], v[196:199]\n"
      "buffer_load_dwordx4 v235, s[44:47], s70 offen offset:3072 nt lds\n"
      "v_max3_f32 v228, v228, v203, v204\n"
      "v_mfma_f32_16x16x32_bf16 v[192:195], a[104:107], a[8:11], v[192:195]\n"
      "v_fma_f32 v205, v205, s76, -v221\n"
      "v_fma_f32 v206, v206, s76, -v221\n"
      "v_mfma_f32_16x16x32_bf16 v[196:199], a[120:123], a[8:11], v[196:199]\n"
      "v_max3_f32 v228, v228, v205, v206\n"
      "v_fma_f32 v207, v207, s76, -v221\n"
      "v_mfma_f32_16x16x32_bf16 v[192:195], a[108:111], a[12:15], v[192:195]\n"
      "v_max_f32 v228, v228, v207\n"
      "v_cmp_nge_f32 vcc, s77, v228\n"
      "v_mfma_f32_16x16x32_bf16 v[196:199], a[124:127], a[12:15], v[196:199]\n"
      "v_mfma_f32_16x16x32_bf16 v[128:131], a[192:195], v[208:211], v[128:131]\n"
      "s_cbranch_vccnz CRy_5_%=\n"
      "BRy_5_%=:\n"
      "v_exp_f32 v200, v200\n"
      "v_mfma_f32_16x16x32_bf16 v[132:135], a[196:199], v[208:211], v[132:135]\n"
      "s_add_u32 s70, s70, %16\n"
      "v_exp_f32 v201, v201\n"
      "v_mfma_f32_16x16x32_bf16 v[136:139], a[200:203], v[208:211], v[136:139]\n"
      "v_exp_f32 v202, v202\n"
      "v_cvt_pk_bf16_f32 v212, v200, v201\n"
      "v_mfma_f32_16x16x32_bf16 v[140:143], a[204:207], v[208:211], v[140:143]\n"
      "v_exp_f32 v203, v203\n"
      "v_mfma_f32_16x16x32_bf16 v[144:147], a[208:211], v[208:211], v[144:147]\n"
      "v_exp_f32 v204, v204\n"
      "v_cvt_pk_bf16_f32 v213, v202, v203\n"
      "v_mfma_f32_16x16x32_bf16 v[148:151], a[212:215], v[208:211], v[148:151]\n"
      "v_exp_f32 v205, v205\n"
      "v_mfma_f32_16x16x32_bf16 v[152:155], a[216:219], v[208:211], v[152:155]\n"
      "s_and_b32 s70, s70, 0x1ffff\n"
      "v_exp_f32 v206, v206\n"
      "v_cvt_pk_bf16_f32 v214, v204, v205\n"
      "v_mfma_f32_16x16x32_bf16 v[156:159], a[220:223], v[208:211], v[156:159]\n"
      "v_exp_f32 v207, v207\n"
      "v_mfma_f32_16x16x32_bf16 a[240:243], a[248:251], v[208:211], a[240:243]\n"
      "v_cvt_pk_bf16_f32 v215, v206, v207\n"
      "v_mfma_f32_16x16x32_bf16 v[200:203], a[96:99], a[16:19], 0\n"
      "v_fma_f32 v192, v192, s76, -v216\n"
      "v_fma_f32 v193, v193, s76, -v216\n"
      "v_mfma_f32_16x16x32_bf16 v[204:207], a[112:115], a[16:19], 0\n"
      "v_fma_f32 v194, v194, s76, -v216\n"
      "v_max3_f32 v228, v192, v193, v194\n"
      "v_mfma_f32_16x16x32_bf16 v[200:203], a[100:103], a[20:23], v[200:203]\n"
      "s_add_u32 m0, s79, 4096\n"
      "v_fma_f32 v195, v195, s76, -v216\n"
      "v_fma_f32 v196, v196, s76, -v216\n"
      "v_mfma_f32_16x16x32_bf16 v[204:207], a[116:119], a[20:23], v[204:207]\n"
      "buffer_load_dwordx4 v234, s[44:47], s70 offen offset:0 nt lds\n"
      "v_max3_f32 v228, v228, v195, v196\n"
      "v_mfma_f32_16x16x32_bf16 v[200:203], a[104:107], a[24:27], v[200:203]\n"
      "v_fma_f32 v197, v197, s76, -v216\n"
      "v_fma_f32 v198, v198, s76, -v216\n"
      "v_mfma_f32_16x16x32_bf16 v[204:207], a[120:123], a[24:27], v[204:207]\n"
      "v_max3_f32 v228, v228, v197, v198\n"
      "v_fma_f32 v199, v199, s76, -v216\n"
      "v_mfma_f32_16x16x32_bf16 v[200:203], a[108:111], a[28:31], v[200:203]\n"
      "v_max_f32 v228, v228, v199\n"
      "v_cmp_nge_f32 vcc, s77, v228\n"
      "v_mfma_f32_16x16x32_bf16 v[204:207], a[124:127], a[28:31], v[204:207]\n"
      "buffer_load_dwordx4 v234, s[44:47], s70 offen offset:1024 nt lds\n"
      "v_mfma_f32_16x16x32_bf16 v[160:163], a[192:195], v[212:215], v[160:163]\n"
      "s_cbranch_vccnz CRz_0_%=\n"
      "BRz_0_%=:\n"
      "v_exp_f32 v192, v192\n"
      "v_mfma_f32_16x16x32_bf16 v[164:167], a[196:199], v[212:215], v[164:167]\n"
      "v_exp_f32 v193, v193\n"
      "v_mfma_f32_16x16x32_bf16 v[168:171], a[200:203], v[212:215], v[168:171]\n"
      "v_exp_f32 v194, v194\n"
      "v_cvt_pk_bf16_f32 v208, v192, v193\n"
      "v_mfma_f32_16x16x32_bf16 v[172:175], a[204:207], v[212:215], v[172:175]\n"
      "v_exp_f32 v195, v195\n"
      "v_mfma_f32_16x16x32_bf16 v[176:179], a[208:211], v[212:215], v[176:179]\n"
      "v_exp_f32 v196, v196\n"
      "v_cvt_pk_bf16_f32 v209, v194, v195\n"
      "v_mfma_f32_16x16x32_bf16 v[180:183], a[212:215], v[212:215], v[180:183]\n"
      "v_exp_f32 v197, v197\n"
      "v_mfma_f32_16x16x32_bf16 v[184:187], a[216:219], v[212:215], v[184:187]\n"
      "v_exp_f32 v198, v198\n"
      "v_cvt_pk_bf16_f32 v210, v196, v197\n"
      "v_mfma_f32_16x16x32_bf16 v[188:191], a[220:223], v[212:215], v[188:191]\n"
      "v_exp_f32 v199, v199\n"
      "v_mfma_f32_16x16x32_bf16 a[244:247], a[248:251], v[212:215], a[244:247]\n"
      "buffer_load_dwordx4 v235, s[44:47], s70 offen offset:2048 nt lds\n"
      "v_cvt_pk_bf16_f32 v211, v198, v199\n"
      "v_mfma_f32_16x16x32_bf16 v[192:195], a[96:99], a[32:35], 0\n"
      "v_fma_f32 v200, v200, s76, -v217\n"
      "v_fma_f32 v201, v201, s76, -v217\n"
      "v_mfma_f32_16x16x32_bf16 v[196:199], a[112:115], a[32:35], 0\n"
      "v_fma_f32 v202, v202, s76, -v217\n"
      "v_max3_f32 v228, v200, v201, v202\n"
      "v_mfma_f32_16x16x32_bf16 v[192:195], a[100:103], a[36:39], v[192:195]\n"
      "v_fma_f32 v203, v203, s76, -v217\n"
      "v_fma_f32 v204, v204, s76, -v217\n"
      "v_mfma_f32_16x16x32_bf16 v[196:199], a[116:119], a[36:39], v[196:199]\n"
      "buffer_load_dwordx4 v235, s[44:47], s70 offen offset:3072 nt lds\n"
      "v_max3_f32 v228, v228, v203, v204\n"
      "v_mfma_f32_16x16x32_bf16 v[192:195], a[104:107], a[40:43], v[192:195]\n"
      "v_fma_f32 v205, v205, s76, -v217\n"
      "v_fma_f32 v206, v206, s76, -v217\n"
      "v_mfma_f32_16x16x32_bf16 v[196:199], a[120:123], a[40:43], v[196:199]\n"
      "v_max3_f32 v228, v228, v205, v206\n"
      "v_fma_f32 v207, v207, s76, -v217\n"
      "v_mfma_f32_16x16x32_bf16 v[192:195], a[108:111], a[44:47], v[192:195]\n"
      "s_add_u32 s70, s70, %16\n"
      "v_max_f32 v228, v228, v207\n"
      "v_cmp_nge_f32 vcc, s77, v228\n"
      "v_mfma_f32_16x16x32_bf16 v[196:199], a[124:127], a[44:47], v[196:199]\n"
      "v_mfma_f32_16x16x32_bf16 v[0:3], a[128:131], v[208:211], v[0:3]\n"
      "s_waitcnt lgkmcnt(12)\n"
      "s_cbranch_vccnz CRz_1_%=\n"
      "BRz_1_%=:\n"
      "v_exp_f32 v200, v200\n"
      "v_mfma_f32_16x16x32_bf16 v[4:7], a[132:135], v[208:211], v[4:7]\n"
      "s_waitcnt lgkmcnt(10)\n"
      "v_exp_f32 v201, v201\n"
      "v_mfma_f32_16x16x32_bf16 v[8:11], a[136:139], v[208:211], v[8:11]\n"
      "s_waitcnt lgkmcnt(8)\n"
      "s_and_b32 s70, s70, 0x1ffff\n"
      "v_exp_f32 v202, v202\n"
      "v_cvt_pk_bf16_f32 v212, v200, v201\n"
      "v_mfma_f32_16x16x32_bf16 v[12:15], a[140:143], v[208:211], v[12:15]\n"
      "s_waitcnt lgkmcnt(6)\n"
      "v_exp_f32 v203, v203\n"
      "v_mfma_f32_16x16x32_bf16 v[16:19], a[144:147], v[208:211], v[16:19]\n"
      "s_waitcnt lgkmcnt(4)\n"
      "v_exp_f32 v204, v204\n"
      "v_cvt_pk_bf16_f32 v213, v202, v203\n"
      "v_mfma_f32_16x16x32_bf16 v[20:23], a[148:151], v[208:211], v[20:23]\n"
      "s_waitcnt lgkmcnt(2)\n"
      "v_exp_f32 v205, v205\n"
      "v_mfma_f32_16x16x32_bf16 v[24:27], a[152:155], v[208:211], v[24:27]\n"
      "s_waitcnt lgkmcnt(0)\n"
      "s_xor_b32 s79, s79, 65536\n"
      "v_exp_f32 v206, v206\n"
      "v_cvt_pk_bf16_f32 v214, v204, v205\n"
      "v_mfma_f32_16x16x32_bf16 v[28:31], a[156:159], v[208:211], v[28:31]\n"
      "v_exp_f32 v207, v207\n"
      "v_mfma_f32_16x16x32_bf16 a[224:227], a[248:251], v[208:211], a[224:227]\n"
      "v_cvt_pk_bf16_f32 v215, v206, v207\n"
      "v_mfma_f32_16x16x32_bf16 v[200:203], a[96:99], a[48:51], 0\n"
      "v_fma_f32 v192, v192, s76, -v218\n"
      "v_fma_f32 v193, v193, s76, -v218\n"
      "v_mfma_f32_16x16x32_bf16 v[204:207], a[112:115], a[48:51], 0\n"
      "v_fma_f32 v194, v194, s76, -v218\n"
      "v_max3_f32 v228, v192, v193, v194\n"
      "v_mfma_f32_16x16x32_bf16 v[200:203], a[100:103], a[52:55], v[200:203]\n"
      "s_add_u32 m0, s80, 0\n"
      "v_fma_f32 v195, v195, s76, -v218\n"
      "v_fma_f32 v196, v196, s76, -v218\n"
      "v_mfma_f32_16x16x32_bf16 v[204:207], a[116:119], a[52:55], v[204:207]\n"
      "buffer_load_dwordx4 v234, s[48:51], s71 offen offset:0 nt lds\n"
      "v_max3_f32 v228, v228, v195, v196\n"
      "v_mfma_f32_16x16x32_bf16 v[200:203], a[104:107], a[56:59], v[200:203]\n"
      "v_fma_f32 v197, v197, s76, -v218\n"
      "v_fma_f32 v198, v198, s76, -v218\n"
      "v_mfma_f32_16x16x32_bf16 v[204:207], a[120:123], a[56:59], v[204:207]\n"
      "v_max3_f32 v228, v228, v197, v198\n"
      "v_fma_f32 v199, v199, s76, -v218\n"
      "v_mfma_f32_16x16x32_bf16 v[200:203], a[108:111], a[60:63], v[200:203]\n"
      "v_max_f32 v228, v228, v199\n"
      "v_cmp_nge_f32 vcc, s77, v228\n"
      "v_mfma_f32_16x16x32_bf16 v[204:207], a[124:127], a[60:63], v[204:207]\n"
      "buffer_load_dwordx4 v236, s[48:51], s71 offen offset:1024 nt lds\n"
      "v_mfma_f32_16x16x32_bf16 v[32:35], a[128:131], v[212:215], v[32:35]\n"
      "s_cbranch_vccnz CRz_2_%=\n"
      "BRz_2_%=:\n"
      "v_exp_f32 v192, v192\n"
      "v_mfma_f32_16x16x32_bf16 v[36:39], a[132:135], v[212:215], v[36:39]\n"
      "v_exp_f32 v193, v193\n"
      "v_mfma_f32_16x16x32_bf16 v[40:43], a[136:139], v[212:215], v[40:43]\n"
      "v_exp_f32 v194, v194\n"
      "v_cvt_pk_bf16_f32 v208, v192, v193\n"
      "v_mfma_f32_16x16x32_bf16 v[44:47], a[140:143], v[212:215], v[44:47]\n"
      "v_exp_f32 v195, v195\n"
      "v_mfma_f32_16x16x32_bf16 v[48:51], a[144:147], v[212:215], v[48:51]\n"
      "v_exp_f32 v196, v196\n"
      "v_cvt_pk_bf16_f32 v209, v194, v195\n"
      "v_mfma_f32_16x16x32_bf16 v[52:55], a[148:151], v[212:215], v[52:55]\n"
      "v_exp_f32 v197, v197\n"
      "v_mfma_f32_16x16x32_bf16 v[56:59], a[152:155], v[212:215], v[56:59]\n"
      "v_exp_f32 v198, v198\n"
      "v_cvt_pk_bf16_f32 v210, v196, v197\n"
      "v_mfma_f32_16x16x32_bf16 v[60:63], a[156:159], v[212:215], v[60:63]\n"
      "v_exp_f32 v199, v199\n"
      "v_mfma_f32_16x16x32_bf16 a[228:231], a[248:251], v[212:215], a[228:231]\n"
      "buffer_load_dwordx4 v234, s[48:51], s71 offen offset:2048 nt lds\n"
      "v_cvt_pk_bf16_f32 v211, v198, v199\n"
      "v_mfma_f32_16x16x32_bf16 v[192:195], a[96:99], a[64:67], 0\n"
      "v_fma_f32 v200, v200, s76, -v219\n"
      "v_fma_f32 v201, v201, s76, -v219\n"
      "v_mfma_f32_16x16x32_bf16 v[196:199], a[112:115], a[64:67], 0\n"
      "v_fma_f32 v202, v202, s76, -v219\n"
      "v_max3_f32 v228, v200, v201, v202\n"
      "v_mfma_f32_16x16x32_bf16 v[192:195], a[100:103], a[68:71], v[192:195]\n"
      "v_fma_f32 v203, v203, s76, -v219\n"
      "v_fma_f32 v204, v204, s76, -v219\n"
      "v_mfma_f32_16x16x32_bf16 v[196:199], a[116:119], a[68:71], v[196:199]\n"
      "buffer_load_dwordx4 v236, s[48:51], s71 offen offset:3072 nt lds\n"
      "v_max3_f32 v228, v228, v203, v204\n"
      "v_mfma_f32_16x16x32_bf16 v[192:195], a[104:107], a[72:75], v[192:195]\n"
      "v_fma_f32 v205, v205, s76, -v219\n"
      "v_fma_f32 v206, v206, s76, -v219\n"
      "v_mfma_f32_16x16x32_bf16 v[196:199], a[120:123], a[72:75], v[196:199]\n"
      "v_max3_f32 v228, v228, v205, v206\n"
      "v_fma_f32 v207, v207, s76, -v219\n"
      "v_mfma_f32_16x16x32_bf16 v[192:195], a[108:111], a[76:79], v[192:195]\n"
      "v_max_f32 v228, v228, v207\n"
      "v_cmp_nge_f32 vcc, s77, v228\n"
      "v_mfma_f32_16x16x32_bf16 v[196:199], a[124:127], a[76:79], v[196:199]\n"
      "v_mfma_f32_16x16x32_bf16 v[64:67], a[128:131], v[208:211], v[64:67]\n"
      "s_cbranch_vccnz CRz_3_%=\n"
      "BRz_3_%=:\n"
      "v_exp_f32 v200, v200\n"
      "v_mfma_f32_16x16x32_bf16 v[68:71], a[132:135], v[208:211], v[68:71]\n"
      "s_add_u32 s71, s71, %16\n"
      "v_exp_f32 v201, v201\n"
      "v_mfma_f32_16x16x32_bf16 v[72:75], a[136:139], v[208:211], v[72:75]\n"
      "v_exp_f32 v202, v202\n"
      "v_cvt_pk_bf16_f32 v212, v200, v201\n"
      "v_mfma_f32_16x16x32_bf16 v[76:79], a[140:143], v[208:211], v[76:79]\n"
      "v_exp_f32 v203, v203\n"
      "v_mfma_f32_16x16x32_bf16 v[80:83], a[144:147], v[208:211], v[80:83]\n"
      "v_exp_f32 v204, v204\n"
      "v_cvt_pk_bf16_f32 v213, v202, v203\n"
      "v_mfma_f32_16x16x32_bf16 v[84:87], a[148:151], v[208:211], v[84:87]\n"
      "v_exp_f32 v205, v205\n"
      "v_mfma_f32_16x16x32_bf16 v[88:91], a[152:155], v[208:211], v[88:91]\n"
      "s_and_b32 s71, s71, 0x1ffff\n"
      "v_exp_f32 v206, v206\n"
      "v_cvt_pk_bf16_f32 v214, v204, v205\n"
      "v_mfma_f32_16x16x32_bf16 v[92:95], a[156:159], v[208:211], v[92:95]\n"
      "v_exp_f32 v207, v207\n"
      "v_mfma_f32_16x16x32_bf16 a[232:235], a[248:251], v[208:211], a[232:235]\n"
      "v_cvt_pk_bf16_f32 v215, v206, v207\n"
      "v_mfma_f32_16x16x32_bf16 v[200:203], a[96:99], a[80:83], 0\n"
      "s_waitcnt vmcnt(20)\n"
      "ds_read_b128 a[96:99], v231 offset:0\n"
      "v_fma_f32 v192, v192, s76, -v220\n"
      "v_fma_f32 v193, v193, s76, -v220\n"
      "v_mfma_f32_16x16x32_bf16 v[204:207], a[112:115], a[80:83], 0\n"
      "ds_read_b128 a[112:115], v231 offset:4096\n"
      "v_fma_f32 v194, v194, s76, -v220\n"
      "v_max3_f32 v228, v192, v193, v194\n"
      "v_mfma_f32_16x16x32_bf16 v[200:203], a[100:103], a[84:87], v[200:203]\n"
      "s_add_u32 m0, s80, 4096\n"
      "ds_read_b128 a[100:103], v231 offset:256\n"
      "v_fma_f32 v195, v195, s76, -v220\n"
      "v_fma_f32 v196, v196, s76, -v220\n"
      "v_mfma_f32_16x16x32_bf16 v[204:207], a[116:119], a[84:87], v[204:207]\n"
      "ds_read_b128 a[116:119], v231 offset:4352\n"
      "v_max3_f32 v228, v228, v195, v196\n"
      "v_mfma_f32_16x16x32_bf16 v[200:203], a[104:107], a[88:91], v[200:203]\n"
      "ds_read_b128 a[104:107], v231 offset:512\n"
      "v_fma_f32 v197, v197, s76, -v220\n"
      "v_fma_f32 v198, v198, s76, -v220\n"
      "v_mfma_f32_16x16x32_bf16 v[204:207], a[120:123], a[88:91], v[204:207]\n"
      "ds_read_b128 a[120:123], v231 offset:4608\n"
      "v_max3_f32 v228, v228, v197, v198\n"
      "v_fma_f32 v199, v199, s76, -v220\n"
      "v_mfma_f32_16x16x32_bf16 v[200:203], a[108:111], a[92:95], v[200:203]\n"
      "ds_read_b128 a[108:111], v231 offset:768\n"
      "v_max_f32 v228, v228, v199\n"
      "v_cmp_nge_f32 vcc, s77, v228\n"
      "v_mfma_f32_16x16x32_bf16 v[204:207], a[124:127], a[92:95], v[204:207]\n"
      "ds_read_b128 a[124:127], v231 offset:4864\n"
      "buffer_load_dwordx4 v234, s[48:51], s71 offen offset:0 nt lds\n"
      "v_mfma_f32_16x16x32_bf16 v[96:99], a[128:131], v[212:215], v[96:99]\n"
      "s_cbranch_vccnz CRz_4_%=\n"
      "BRz_4_%=:\n"
      "v_exp_f32 v192, v192\n"
      "v_mfma_f32_16x16x32_bf16 v[100:103], a[132:135], v[212:215], v[100:103]\n"
      "v_exp_f32 v193, v193\n"
      "v_mfma_f32_16x16x32_bf16 v[104:107], a[136:139], v[212:215], v[104:107]\n"
      "v_exp_f32 v194, v194\n"
      "v_cvt_pk_bf16_f32 v208, v192, v193\n"
      "v_mfma_f32_16x16x32_bf16 v[108:111], a[140:143], v[212:215], v[108:111]\n"
      "v_exp_f32 v195, v195\n"
      "v_mfma_f32_16x16x32_bf16 v[112:115], a[144:147], v[212:215], v[112:115]\n"
      "v_exp_f32 v196, v196\n"
      "v_cvt_pk_bf16_f32 v209, v194, v195\n"
      "v_mfma_f32_16x16x32_bf16 v[116:119], a[148:151], v[212:215], v[116:119]\n"
      "v_exp_f32 v197, v197\n"
      "v_mfma_f32_16x16x32_bf16 v[120:123], a[152:155], v[212:215], v[120:123]\n"
      "v_exp_f32 v198, v198\n"
      "v_cvt_pk_bf16_f32 v210, v196, v197\n"
      "v_mfma_f32_16x16x32_bf16 v[124:127], a[156:159], v[212:215], v[124:127]\n"
      "v_exp_f32 v199, v199\n"
      "v_mfma_f32_16x16x32_bf16 a[236:239], a[248:251], v[212:215], a[236:239]\n"
      "s_waitcnt lgkmcnt(7)\n"
      "buffer_load_dwordx4 v236, s[48:51], s71 offen offset:1024 nt lds\n"
      "v_cvt_pk_bf16_f32 v211, v198, v199\n"
      "v_mfma_f32_16x16x32_bf16 v[192:195], a[96:99], a[0:3], 0\n"
      "s_waitcnt lgkmcnt(6)\n"
      "v_fma_f32 v200, v200, s76, -v221\n"
      "v_fma_f32 v201, v201, s76, -v221\n"
      "v_mfma_f32_16x16x32_bf16 v[196:199], a[112:115], a[0:3], 0\n"
      "s_waitcnt lgkmcnt(5)\n"
      "v_fma_f32 v202, v202, s76, -v221\n"
      "v_max3_f32 v228, v200, v201, v202\n"
      "v_mfma_f32_16x16x32_bf16 v[192:195], a[100:103], a[4:7], v[192:195]\n"
      "s_waitcnt lgkmcnt(4)\n"
      "v_fma_f32 v203, v203, s76, -v221\n"
      "v_fma_f32 v204, v204, s76, -v221\n"
      "v_mfma_f32_16x16x32_bf16 v[196:199], a[116:119], a[4:7], v[196:199]\n"
      "s_waitcnt lgkmcnt(3)\n"
      "buffer_load_dwordx4 v234, s[48:51], s71 offen offset:2048 nt lds\n"
      "v_max3_f32 v228, v228, v203, v204\n"
      "v_mfma_f32_16x16x32_bf16 v[192:195], a[104:107], a[8:11], v[192:195]\n"
      "s_waitcnt lgkmcnt(2)\n"
      "v_fma_f32 v205, v205, s76, -v221\n"
      "v_fma_f32 v206, v206, s76, -v221\n"
      "v_mfma_f32_16x16x32_bf16 v[196:199], a[120:123], a[8:11], v[196:199]\n"
      "s_waitcnt lgkmcnt(1)\n"
      "v_max3_f32 v228, v228, v205, v206\n"
      "v_fma_f32 v207, v207, s76, -v221\n"
      "v_mfma_f32_16x16x32_bf16 v[192:195], a[108:111], a[12:15], v[192:195]\n"
      "s_waitcnt lgkmcnt(0)\n"
      "v_max_f32 v228, v228, v207\n"
      "v_cmp_nge_f32 vcc, s77, v228\n"
      "v_mfma_f32_16x16x32_bf16 v[196:199], a[124:127], a[12:15], v[196:199]\n"
      "buffer_load_dwordx4 v236, s[48:51], s71 offen offset:3072 nt lds\n"
      "s_add_u32 s71, s71, %16\n"
      "v_xor_b32 v231, 65536, v231\n"
      "v_mfma_f32_16x16x32_bf16 v[128:131], a[128:131], v[208:211], v[128:131]\n"
      "s_cbranch_vccnz CRz_5_%=\n"
      "BRz_5_%=:\n"
      "v_exp_f32 v200, v200\n"
      "v_mfma_f32_16x16x32_bf16 v[132:135], a[132:135], v[208:211], v[132:135]\n"
      "v_exp_f32 v201, v201\n"
      "v_mfma_f32_16x16x32_bf16 v[136:139], a[136:139], v[208:211], v[136:139]\n"
      "s_and_b32 s71, s71, 0x1ffff\n"
      "v_exp_f32 v202, v202\n"
      "v_cvt_pk_bf16_f32 v212, v200, v201\n"
      "v_mfma_f32_16x16x32_bf16 v[140:143], a[140:143], v[208:211], v[140:143]\n"
      "v_exp_f32 v203, v203\n"
      "v_mfma_f32_16x16x32_bf16 v[144:147], a[144:147], v[208:211], v[144:147]\n"
      "v_exp_f32 v204, v204\n"
      "v_cvt_pk_bf16_f32 v213, v202, v203\n"
      "v_mfma_f32_16x16x32_bf16 v[148:151], a[148:151], v[208:211], v[148:151]\n"
      "v_exp_f32 v205, v205\n"
      "v_mfma_f32_16x16x32_bf16 v[152:155], a[152:155], v[208:211], v[152:155]\n"
      "s_xor_b32 s80, s80, 65536\n"
      "v_exp_f32 v206, v206\n"
      "v_cvt_pk_bf16_f32 v214, v204, v205\n"
      "v_mfma_f32_16x16x32_bf16 v[156:159], a[156:159], v[208:211], v[156:159]\n"
      "v_exp_f32 v207, v207\n"
      "v_mfma_f32_16x16x32_bf16 a[240:243], a[248:251], v[208:211], a[240:243]\n"
      "v_cvt_pk_bf16_f32 v215, v206, v207\n"
      "s_mov_b32 s75, 10\n"
      "LT_%=:\n"
      "v_mfma_f32_16x16x32_bf16 v[200:203], a[96:99], a[16:19], 0\n"
      "v_fma_f32 v192, v192, s76, -v216\n"
      "v_fma_f32 v193, v193, s76, -v216\n"
      "v_mfma_f32_16x16x32_bf16 v[204:207], a[112:115], a[16:19], 0\n"
      "s_add_u32 m0, s79, 0\n"
      "v_fma_f32 v194, v194, s76, -v216\n"
      "v_max3_f32 v228, v192, v193, v194\n"
      "v_mfma_f32_16x16x32_bf16 v[200:203], a[100:103], a[20:23], v[200:203]\n"
      "v_fma_f32 v195, v195, s76, -v216\n"
      "v_fma_f32 v196, v196, s76, -v216\n"
      "v_mfma_f32_16x16x32_bf16 v[204:207], a[116:119], a[20:23], v[204:207]\n"
      "buffer_load_dwordx4 v234, s[44:47], s70 offen offset:0 nt lds\n"
      "v_max3_f32 v228, v228, v195, v196\n"
      "v_mfma_f32_16x16x32_bf16 v[200:203], a[104:107], a[24:27], v[200:203]\n"
      "v_fma_f32 v197, v197, s76, -v216\n"
      "v_fma_f32 v198, v198, s76, -v216\n"
      "v_mfma_f32_16x16x32_bf16 v[204:207], a[120:123], a[24:27], v[204:207]\n"
      "v_max3_f32 v228, v228, v197, v198\n"
      "v_fma_f32 v199, v199, s76, -v216\n"
      "v_mfma_f32_16x16x32_bf16 v[200:203], a[108:111], a[28:31], v[200:203]\n"
      "buffer_load_dwordx4 v234, s[44:47], s70 offen offset:1024 nt lds\n"
      "v_max_f32 v228, v228, v199\n"
      "v_cmp_nge_f32 vcc, s77, v228\n"
      "v_mfma_f32_16x16x32_bf16 v[204:207], a[124:127], a[28:31], v[204:207]\n"
      "buffer_load_dwordx4 v235, s[44:47], s70 offen offset:2048 nt lds\n"
      "buffer_load_dwordx4 v235, s[44:47], s70 offen offset:3072 nt lds\n"
      "v_mfma_f32_16x16x32_bf16 v[160:163], a[128:131], v[212:215], v[160:163]\n"
      "s_waitcnt vmcnt(20)\n"
      "s_cbranch_vccnz CRa_0_%=\n"
      "BRa_0_%=:\n"
      "ds_read_b64_tr_b16 a[128:129], v232 offset:0\n"
      "v_exp_f32 v192, v192\n"
      "v_mfma_f32_16x16x32_bf16 v[164:167], a[132:135], v[212:215], v[164:167]\n"
      "ds_read_b64_tr_b16 a[130:131], v232 offset:4096\n"
      "v_exp_f32 v193, v193\n"
      "v_mfma_f32_16x16x32_bf16 v[168:171], a[136:139], v[212:215], v[168:171]\n"
      "ds_read_b64_tr_b16 a[132:133], v233 offset:0\n"
      "v_exp_f32 v194, v194\n"
      "v_cvt_pk_bf16_f32 v208, v192, v193\n"
      "v_mfma_f32_16x16x32_bf16 v[172:175], a[140:143], v[212:215], v[172:175]\n"
      "ds_read_b64_tr_b16 a[134:135], v233 offset:4096\n"
      "v_exp_f32 v195, v195\n"
      "v_mfma_f32_16x16x32_bf16 v[176:179], a[144:147], v[212:215], v[176:179]\n"
      "ds_read_b64_tr_b16 a[136:137], v232 offset:256\n"
      "v_exp_f32 v196, v196\n"
      "v_cvt_pk_bf16_f32 v209, v194, v195\n"
      "v_mfma_f32_16x16x32_bf16 v[180:183], a[148:151], v[212:215], v[180:183]\n"
      "ds_read_b64_tr_b16 a[138:139], v232 offset:4352\n"
      "v_exp_f32 v197, v197\n"
      "v_mfma_f32_16x16x32_bf16 v[184:187], a[152:155], v[212:215], v[184:187]\n"
      "ds_read_b64_tr_b16 a[140:141], v233 offset:256\n"
      "v_exp_f32 v198, v198\n"
      "v_cvt_pk_bf16_f32 v210, v196, v197\n"
      "v_mfma_f32_16x16x32_bf16 v[188:191], a[156:159], v[212:215], v[188:191]\n"
      "ds_read_b64_tr_b16 a[142:143], v233 offset:4352\n"
      "v_exp_f32 v199, v199\n"
      "v_mfma_f32_16x16x32_bf16 a[244:247], a[248:251], v[212:215], a[244:247]\n"
      "ds_read_b64_tr_b16 a[144:145], v232 offset:512\n"
      "v_cvt_pk_bf16_f32 v211, v198, v199\n"
      "v_mfma_f32_16x16x32_bf16 v[192:195], a[96:99], a[32:35], 0\n"
      "ds_read_b64_tr_b16 a[146:147], v232 offset:4608\n"
      "v_fma_f32 v200, v200, s76, -v217\n"
      "v_fma_f32 v201, v201, s76, -v217\n"
      "v_mfma_f32_16x16x32_bf16 v[196:199], a[112:115], a[32:35], 0\n"
      "ds_read_b64_tr_b16 a[148:149], v233 offset:512\n"
      "v_fma_f32 v202, v202, s76, -v217\n"
      "v_max3_f32 v228, v200, v201, v202\n"
      "v_mfma_f32_16x16x32_bf16 v[192:195], a[100:103], a[36:39], v[192:195]\n"
      "s_add_u32 s70, s70, %16\n"
      "ds_read_b64_tr_b16 a[150:151], v233 offset:4608\n"
      "v_fma_f32 v203, v203, s76, -v217\n"
      "v_fma_f32 v204, v204, s76, -v217\n"
      "v_mfma_f32_16x16x32_bf16 v[196:199], a[116:119], a[36:39], v[196:199]\n"
      "ds_read_b64_tr_b16 a[152:153], v232 offset:768\n"
      "v_max3_f32 v228, v228, v203, v204\n"
      "v_mfma_f32_16x16x32_bf16 v[192:195], a[104:107], a[40:43], v[192:195]\n"
      "ds_read_b64_tr_b16 a[154:155], v232 offset:4864\n"
      "v_fma_f32 v205, v205, s76, -v217\n"
      "v_fma_f32 v206, v206, s76, -v217\n"
      "v_mfma_f32_16x16x32_bf16 v[196:199], a[120:123], a[40:43], v[196:199]\n"
      "ds_read_b64_tr_b16 a[156:157], v233 offset:768\n"
      "v_max3_f32 v228, v228, v205, v206\n"
      "v_fma_f32 v207, v207, s76, -v217\n"
      "v_mfma_f32_16x16x32_bf16 v[192:195], a[108:111], a[44:47], v[192:195]\n"
      "s_and_b32 s70, s70, 0x1ffff\n"
      "v_max_f32 v228, v228, v207\n"
      "v_cmp_nge_f32 vcc, s77, v228\n"
      "v_mfma_f32_16x16x32_bf16 v[196:199], a[124:127], a[44:47], v[196:199]\n"
      "s_waitcnt lgkmcnt(13)\n"
      "ds_read_b64_tr_b16 a[158:159], v233 offset:4864\n"
      "v_mfma_f32_16x16x32_bf16 v[0:3], a[128:131], v[208:211], v[0:3]\n"
      "s_waitcnt lgkmcnt(12)\n"
      "s_cbranch_vccnz CRa_1_%=\n"
      "BRa_1_%=:\n"
      "v_exp_f32 v200, v200\n"
      "v_mfma_f32_16x16x32_bf16 v[4:7], a[132:135], v[208:211], v[4:7]\n"
      "s_waitcnt lgkmcnt(10)\n"
      "s_add_u32 m0, s79, 4096\n"
      "v_exp_f32 v201, v201\n"
      "v_mfma_f32_16x16x32_bf16 v[8:11], a[136:139], v[208:211], v[8:11]\n"
      "s_waitcnt lgkmcnt(8)\n"
      "v_exp_f32 v202, v202\n"
      "v_cvt_pk_bf16_f32 v212, v200, v201\n"
      "v_mfma_f32_16x16x32_bf16 v[12:15], a[140:143], v[208:211], v[12:15]\n"
      "s_waitcnt lgkmcnt(6)\n"
      "v_exp_f32 v203, v203\n"
      "v_mfma_f32_16x16x32_bf16 v[16:19], a[144:147], v[208:211], v[16:19]\n"
      "s_waitcnt lgkmcnt(4)\n"
      "buffer_load_dwordx4 v234, s[44:47], s70 offen offset:0 nt lds\n"
      "v_exp_f32 v204, v204\n"
      "v_cvt_pk_bf16_f32 v213, v202, v203\n"
      "v_mfma_f32_16x16x32_bf16 v[20:23], a[148:151], v[208:211], v[20:23]\n"
      "s_waitcnt lgkmcnt(2)\n"
      "v_exp_f32 v205, v205\n"
      "v_mfma_f32_16x16x32_bf16 v[24:27], a[152:155], v[208:211], v[24:27]\n"
      "s_waitcnt lgkmcnt(0)\n"
      "v_exp_f32 v206, v206\n"
      "v_cvt_pk_bf16_f32 v214, v204, v205\n"
      "v_mfma_f32_16x16x32_bf16 v[28:31], a[156:159], v[208:211], v[28:31]\n"
      "v_exp_f32 v207, v207\n"
      "v_mfma_f32_16x16x32_bf16 a[224:227], a[248:251], v[208:211], a[224:227]\n"
      "buffer_load_dwordx4 v234, s[44:47], s70 offen offset:1024 nt lds\n"
      "v_cvt_pk_bf16_f32 v215, v206, v207\n"
      "v_mfma_f32_16x16x32_bf16 v[200:203], a[96:99], a[48:51], 0\n"
      "v_fma_f32 v192, v192, s76, -v218\n"
      "v_fma_f32 v193, v193, s76, -v218\n"
      "v_mfma_f32_16x16x32_bf16 v[204:207], a[112:115], a[48:51], 0\n"
      "v_fma_f32 v194, v194, s76, -v218\n"
      "v_max3_f32 v228, v192, v193, v194\n"
      "v_mfma_f32_16x16x32_bf16 v[200:203], a[100:103], a[52:55], v[200:203]\n"
      "v_fma_f32 v195, v195, s76, -v218\n"
      "v_fma_f32 v196, v196, s76, -v218\n"
      "v_mfma_f32_16x16x32_bf16 v[204:207], a[116:119], a[52:55], v[204:207]\n"
      "buffer_load_dwordx4 v235, s[44:47], s70 offen offset:2048 nt lds\n"
      "v_max3_f32 v228, v228, v195, v196\n"
      "v_mfma_f32_16x16x32_bf16 v[200:203], a[104:107], a[56:59], v[200:203]\n"
      "v_fma_f32 v197, v197, s76, -v218\n"
      "v_fma_f32 v198, v198, s76, -v218\n"
      "v_mfma_f32_16x16x32_bf16 v[204:207], a[120:123], a[56:59], v[204:207]\n"
      "v_max3_f32 v228, v228, v197, v198\n"
      "v_fma_f32 v199, v199, s76, -v218\n"
      "v_mfma_f32_16x16x32_bf16 v[200:203], a[108:111], a[60:63], v[200:203]\n"
      "v_max_f32 v228, v228, v199\n"
      "v_cmp_nge_f32 vcc, s77, v228\n"
      "v_mfma_f32_16x16x32_bf16 v[204:207], a[124:127], a[60:63], v[204:207]\n"
      "buffer_load_dwordx4 v235, s[44:47], s70 offen offset:3072 nt lds\n"
      "v_xor_b32 v232, 65536, v232\n"
      "v_mfma_f32_16x16x32_bf16 v[32:35], a[128:131], v[212:215], v[32:35]\n"
      "s_cbranch_vccnz CRa_2_%=\n"
      "BRa_2_%=:\n"
      "v_exp_f32 v192, v192\n"
      "v_mfma_f32_16x16x32_bf16 v[36:39], a[132:135], v[212:215], v[36:39]\n"
      "s_add_u32 s70, s70, %16\n"
      "v_exp_f32 v193, v193\n"
      "v_mfma_f32_16x16x32_bf16 v[40:43], a[136:139], v[212:215], v[40:43]\n"
      "v_exp_f32 v194, v194\n"
      "v_cvt_pk_bf16_f32 v208, v192, v193\n"
      "v_mfma_f32_16x16x32_bf16 v[44:47], a[140:143], v[212:215], v[44:47]\n"
      "v_exp_f32 v195, v195\n"
      "v_mfma_f32_16x16x32_bf16 v[48:51], a[144:147], v[212:215], v[48:51]\n"
      "s_and_b32 s70, s70, 0x1ffff\n"
      "v_exp_f32 v196, v196\n"
      "v_cvt_pk_bf16_f32 v209, v194, v195\n"
      "v_mfma_f32_16x16x32_bf16 v[52:55], a[148:151], v[212:215], v[52:55]\n"
      "v_exp_f32 v197, v197\n"
      "v_mfma_f32_16x16x32_bf16 v[56:59], a[152:155], v[212:215], v[56:59]\n"
      "v_exp_f32 v198, v198\n"
      "v_cvt_pk_bf16_f32 v210, v196, v197\n"
      "v_mfma_f32_16x16x32_bf16 v[60:63], a[156:159], v[212:215], v[60:63]\n"
      "s_xor_b32 s79, s79, 65536\n"
      "v_exp_f32 v199, v199\n"
      "v_mfma_f32_16x16x32_bf16 a[228:231], a[248:251], v[212:215], a[228:231]\n"
      "v_cvt_pk_bf16_f32 v211, v198, v199\n"
      "v_mfma_f32_16x16x32_bf16 v[192:195], a[96:99], a[64:67], 0\n"
      "v_fma_f32 v200, v200, s76, -v219\n"
      "v_fma_f32 v201, v201, s76, -v219\n"
      "v_mfma_f32_16x16x32_bf16 v[196:199], a[112:115], a[64:67], 0\n"
      "s_add_u32 m0, s80, 0\n"
      "v_fma_f32 v202, v202, s76, -v219\n"
      "v_max3_f32 v228, v200, v201, v202\n"
      "v_mfma_f32_16x16x32_bf16 v[192:195], a[100:103], a[68:71], v[192:195]\n"
      "v_fma_f32 v203, v203, s76, -v219\n"
      "v_fma_f32 v204, v204, s76, -v219\n"
      "v_mfma_f32_16x16x32_bf16 v[196:199], a[116:119], a[68:71], v[196:199]\n"
      "buffer_load_dwordx4 v234, s[48:51], s71 offen offset:0 nt lds\n"
      "v_max3_f32 v228, v228, v203, v204\n"
      "v_mfma_f32_16x16x32_bf16 v[192:195], a[104:107], a[72:75], v[192:195]\n"
      "v_fma_f32 v205, v205, s76, -v219\n"
      "v_fma_f32 v206, v206, s76, -v219\n"
      "v_mfma_f32_16x16x32_bf16 v[196:199], a[120:123], a[72:75], v[196:199]\n"
      "v_max3_f32 v228, v228, v205, v206\n"
      "v_fma_f32 v207, v207, s76, -v219\n"
      "v_mfma_f32_16x16x32_bf16 v[192:195], a[108:111], a[76:79], v[192:195]\n"
      "v_max_f32 v228, v228, v207\n"
      "v_cmp_nge_f32 vcc, s77, v228\n"
      "v_mfma_f32_16x16x32_bf16 v[196:199], a[124:127], a[76:79], v[196:199]\n"
      "buffer_load_dwordx4 v236, s[48:51], s71 offen offset:1024 nt lds\n"
      "v_xor_b32 v233, 65536, v233\n"
      "v_mfma_f32_16x16x32_bf16 v[64:67], a[128:131], v[208:211], v[64:67]\n"
      "s_cbranch_vccnz CRa_3_%=\n"
      "BRa_3_%=:\n"
      "v_exp_f32 v200, v200\n"
      "v_mfma_f32_16x16x32_bf16 v[68:71], a[132:135], v[208:211], v[68:71]\n"
      "v_exp_f32 v201, v201\n"
      "v_mfma_f32_16x16x32_bf16 v[72:75], a[136:139], v[208:211], v[72:75]\n"
      "v_exp_f32 v202, v202\n"
      "v_cvt_pk_bf16_f32 v212, v200, v201\n"
      "v_mfma_f32_16x16x32_bf16 v[76:79], a[140:143], v[208:211], v[76:79]\n"
      "v_exp_f32 v203, v203\n"
      "v_mfma_f32_16x16x32_bf16 v[80:83], a[144:147], v[208:211], v[80:83]\n"
      "buffer_load_dwordx4 v234, s[48:51], s71 offen offset:2048 nt lds\n"
      "v_exp_f32 v204, v204\n"
      "v_cvt_pk_bf16_f32 v213, v202, v203\n"
      "v_mfma_f32_16x16x32_bf16 v[84:87], a[148:151], v[208:211], v[84:87]\n"
      "v_exp_f32 v205, v205\n"
      "v_mfma_f32_16x16x32_bf16 v[88:91], a[152:155], v[208:211], v[88:91]\n"
      "v_exp_f32 v206, v206\n"
      "v_cvt_pk_bf16_f32 v214, v204, v205\n"
      "v_mfma_f32_16x16x32_bf16 v[92:95], a[156:159], v[208:211], v[92:95]\n"
      "v_exp_f32 v207, v207\n"
      "v_mfma_f32_16x16x32_bf16 a[232:235], a[248:251], v[208:211], a[232:235]\n"
      "buffer_load_dwordx4 v236, s[48:51], s71 offen offset:3072 nt lds\n"
      "v_cvt_pk_bf16_f32 v215, v206, v207\n"
      "v_mfma_f32_16x16x32_bf16 v[200:203], a[96:99], a[80:83], 0\n"
      "s_waitcnt vmcnt(20)\n"
      "ds_read_b128 a[96:99], v231 offset:0\n"
      "v_fma_f32 v192, v192, s76, -v220\n"
      "v_fma_f32 v193, v193, s76, -v220\n"
      "v_mfma_f32_16x16x32_bf16 v[204:207], a[112:115], a[80:83], 0\n"
      "ds_read_b128 a[112:115], v231 offset:4096\n"
      "v_fma_f32 v194, v194, s76, -v220\n"
      "v_max3_f32 v228, v192, v193, v194\n"
      "v_mfma_f32_16x16x32_bf16 v[200:203], a[100:103], a[84:87], v[200:203]\n"
      "s_add_u32 s71, s71, %16\n"
      "ds_read_b128 a[100:103], v231 offset:256\n"
      "v_fma_f32 v195, v195, s76, -v220\n"
      "v_fma_f32 v196, v196, s76, -v220\n"
      "v_mfma_f32_16x16x32_bf16 v[204:207], a[116:119], a[84:87], v[204:207]\n"
      "ds_read_b128 a[116:119], v231 offset:4352\n"
      "v_max3_f32 v228, v228, v195, v196\n"
      "v_mfma_f32_16x16x32_bf16 v[200:203], a[104:107], a[88:91], v[200:203]\n"
      "ds_read_b128 a[104:107], v231 offset:512\n"
      "v_fma_f32 v197, v197, s76, -v220\n"
      "v_fma_f32 v198, v198, s76, -v220\n"
      "v_mfma_f32_16x16x32_bf16 v[204:207], a[120:123], a[88:91], v[204:207]\n"
      "ds_read_b128 a[120:123], v231 offset:4608\n"
      "v_max3_f32 v228, v228, v197, v198\n"
      "v_fma_f32 v199, v199, s76, -v220\n"
      "v_mfma_f32_16x16x32_bf16 v[200:203], a[108:111], a[92:95], v[200:203]\n"
      "s_and_b32 s71, s71, 0x1ffff\n"
      "ds_read_b128 a[108:111], v231 offset:768\n"
      "v_max_f32 v228, v228, v199\n"
      "v_cmp_nge_f32 vcc, s77, v228\n"
      "v_mfma_f32_16x16x32_bf16 v[204:207], a[124:127], a[92:95], v[204:207]\n"
      "ds_read_b128 a[124:127], v231 offset:4864\n"
      "v_mfma_f32_16x16x32_bf16 v[96:99], a[128:131], v[212:215], v[96:99]\n"
      "s_cbranch_vccnz CRa_4_%=\n"
      "BRa_4_%=:\n"
      "v_exp_f32 v192, v192\n"
      "v_mfma_f32_16x16x32_bf16 v[100:103], a[132:135], v[212:215], v[100:103]\n"
      "s_add_u32 m0, s80, 4096\n"
      "v_exp_f32 v193, v193\n"
      "v_mfma_f32_16x16x32_bf16 v[104:107], a[136:139], v[212:215], v[104:107]\n"
      "v_exp_f32 v194, v194\n"
      "v_cvt_pk_bf16_f32 v208, v192, v193\n"
      "v_mfma_f32_16x16x32_bf16 v[108:111], a[140:143], v[212:215], v[108:111]\n"
      "v_exp_f32 v195, v195\n"
      "v_mfma_f32_16x16x32_bf16 v[112:115], a[144:147], v[212:215], v[112:115]\n"
      "buffer_load_dwordx4 v234, s[48:51], s71 offen offset:0 nt lds\n"
      "v_exp_f32 v196, v196\n"
      "v_cvt_pk_bf16_f32 v209, v194, v195\n"
      "v_mfma_f32_16x16x32_bf16 v[116:119], a[148:151], v[212:215], v[116:119]\n"
      "v_exp_f32 v197, v197\n"
      "v_mfma_f32_16x16x32_bf16 v[120:123], a[152:155], v[212:215], v[120:123]\n"
      "v_exp_f32 v198, v198\n"
      "v_cvt_pk_bf16_f32 v210, v196, v197\n"
      "v_mfma_f32_16x16x32_bf16 v[124:127], a[156:159], v[212:215], v[124:127]\n"
      "v_exp_f32 v199, v199\n"
      "v_mfma_f32_16x16x32_bf16 a[236:239], a[248:251], v[212:215], a[236:239]\n"
      "s_waitcnt lgkmcnt(7)\n"
      "buffer_load_dwordx4 v236, s[48:51], s71 offen offset:1024 nt lds\n"
      "v_cvt_pk_bf16_f32 v211, v198, v199\n"
      "v_mfma_f32_16x16x32_bf16 v[192:195], a[96:99], a[0:3], 0\n"
      "s_waitcnt lgkmcnt(6)\n"
      "v_fma_f32 v200, v200, s76, -v221\n"
      "v_fma_f32 v201, v201, s76, -v221\n"
      "v_mfma_f32_16x16x32_bf16 v[196:199], a[112:115], a[0:3], 0\n"
      "s_waitcnt lgkmcnt(5)\n"
      "v_fma_f32 v202, v202, s76, -v221\n"
      "v_max3_f32 v228, v200, v201, v202\n"
      "v_mfma_f32_16x16x32_bf16 v[192:195], a[100:103], a[4:7], v[192:195]\n"
      "s_waitcnt lgkmcnt(4)\n"
      "v_fma_f32 v203, v203, s76, -v221\n"
      "v_fma_f32 v204, v204, s76, -v221\n"
      "v_mfma_f32_16x16x32_bf16 v[196:199], a[116:119], a[4:7], v[196:199]\n"
      "s_waitcnt lgkmcnt(3)\n"
      "buffer_load_dwordx4 v234, s[48:51], s71 offen offset:2048 nt lds\n"
      "v_max3_f32 v228, v228, v203, v204\n"
      "v_mfma_f32_16x16x32_bf16 v[192:195], a[104:107], a[8:11], v[192:195]\n"
      "s_waitcnt lgkmcnt(2)\n"
      "v_fma_f32 v205, v205, s76, -v221\n"
      "v_fma_f32 v206, v206, s76, -v221\n"
      "v_mfma_f32_16x16x32_bf16 v[196:199], a[120:123], a[8:11], v[196:199]\n"
      "s_waitcnt lgkmcnt(1)\n"
      "v_max3_f32 v228, v228, v205, v206\n"
      "v_fma_f32 v207, v207, s76, -v221\n"
      "v_mfma_f32_16x16x32_bf16 v[192:195], a[108:111], a[12:15], v[192:195]\n"
      "s_waitcnt lgkmcnt(0)\n"
      "v_max_f32 v228, v228, v207\n"
      "v_cmp_nge_f32 vcc, s77, v228\n"
      "v_mfma_f32_16x16x32_bf16 v[196:199], a[124:127], a[12:15], v[196:199]\n"
      "buffer_load_dwordx4 v236, s[48:51], s71 offen offset:3072 nt lds\n"
      "v_xor_b32 v231, 65536, v231\n"
      "v_mfma_f32_16x16x32_bf16 v[128:131], a[128:131], v[208:211], v[128:131]\n"
      "s_cbranch_vccnz CRa_5_%=\n"
      "BRa_5_%=:\n"
      "v_exp_f32 v200, v200\n"
      "v_mfma_f32_16x16x32_bf16 v[132:135], a[132:135], v[208:211], v[132:135]\n"
      "s_add_u32 s71, s71, %16\n"
      "v_exp_f32 v201, v201\n"
      "v_mfma_f32_16x16x32_bf16 v[136:139], a[136:139], v[208:211], v[136:139]\n"
      "v_exp_f32 v202, v202\n"
      "v_cvt_pk_bf16_f32 v212, v200, v201\n"
      "v_mfma_f32_16x16x32_bf16 v[140:143], a[140:143], v[208:211], v[140:143]\n"
      "v_exp_f32 v203, v203\n"
      "v_mfma_f32_16x16x32_bf16 v[144:147], a[144:147], v[208:211], v[144:147]\n"
      "s_and_b32 s71, s71, 0x1ffff\n"
      "v_exp_f32 v204, v204\n"
      "v_cvt_pk_bf16_f32 v213, v202, v203\n"
      "v_mfma_f32_16x16x32_bf16 v[148:151], a[148:151], v[208:211], v[148:151]\n"
      "v_exp_f32 v205, v205\n"
      "v_mfma_f32_16x16x32_bf16 v[152:155], a[152:155], v[208:211], v[152:155]\n"
      "v_exp_f32 v206, v206\n"
      "v_cvt_pk_bf16_f32 v214, v204, v205\n"
      "v_mfma_f32_16x16x32_bf16 v[156:159], a[156:159], v[208:211], v[156:159]\n"
      "s_xor_b32 s80, s80, 65536\n"
      "v_exp_f32 v207, v207\n"
      "v_mfma_f32_16x16x32_bf16 a[240:243], a[248:251], v[208:211], a[240:243]\n"
      "v_cvt_pk_bf16_f32 v215, v206, v207\n"
      "s_sub_u32 s75, s75, 1\n"
      "s_cmp_lg_u32 s75, 0\n"
      "s_cbranch_scc1 LT_%=\n"
      "v_mfma_f32_16x16x32_bf16 v[200:203], a[96:99], a[16:19], 0\n"
      "v_fma_f32 v192, v192, s76, -v216\n"
      "v_fma_f32 v193, v193, s76, -v216\n"
      "v_mfma_f32_16x16x32_bf16 v[204:207], a[112:115], a[16:19], 0\n"
      "v_fma_f32 v194, v194, s76, -v216\n"
      "v_max3_f32 v228, v192, v193, v194\n"
      "v_mfma_f32_16x16x32_bf16 v[200:203], a[100:103], a[20:23], v[200:203]\n"
      "v_fma_f32 v195, v195, s76, -v216\n"
      "v_fma_f32 v196, v196, s76, -v216\n"
      "v_mfma_f32_16x16x32_bf16 v[204:207], a[116:119], a[20:23], v[204:207]\n"
      "v_max3_f32 v228, v228, v195, v196\n"
      "v_mfma_f32_16x16x32_bf16 v[200:203], a[104:107], a[24:27], v[200:203]\n"
      "v_fma_f32 v197, v197, s76, -v216\n"
      "v_fma_f32 v198, v198, s76, -v216\n"
      "v_mfma_f32_16x16x32_bf16 v[204:207], a[120:123], a[24:27], v[204:207]\n"
      "v_max3_f32 v228, v228, v197, v198\n"
      "v_fma_f32 v199, v199, s76, -v216\n"
      "v_mfma_f32_16x16x32_bf16 v[200:203], a[108:111], a[28:31], v[200:203]\n"
      "v_max_f32 v228, v228, v199\n"
      "v_cmp_nge_f32 vcc, s77, v228\n"
      "v_mfma_f32_16x16x32_bf16 v[204:207], a[124:127], a[28:31], v[204:207]\n"
      "v_mfma_f32_16x16x32_bf16 v[160:163], a[128:131], v[212:215], v[160:163]\n"
      "s_waitcnt vmcnt(16)\n"
      "s_cbranch_vccnz CRb_0_%=\n"
      "BRb_0_%=:\n"
      "ds_read_b64_tr_b16 a[128:129], v232 offset:0\n"
      "v_exp_f32 v192, v192\n"
      "v_mfma_f32_16x16x32_bf16 v[164:167], a[132:135], v[212:215], v[164:167]\n"
      "ds_read_b64_tr_b16 a[130:131], v232 offset:4096\n"
      "v_exp_f32 v193, v193\n"
      "v_mfma_f32_16x16x32_bf16 v[168:171], a[136:139], v[212:215], v[168:171]\n"
      "ds_read_b64_tr_b16 a[132:133], v233 offset:0\n"
      "v_exp_f32 v194, v194\n"
      "v_cvt_pk_bf16_f32 v208, v192, v193\n"
      "v_mfma_f32_16x16x32_bf16 v[172:175], a[140:143], v[212:215], v[172:175]\n"
      "ds_read_b64_tr_b16 a[134:135], v233 offset:4096\n"
      "v_exp_f32 v195, v195\n"
      "v_mfma_f32_16x16x32_bf16 v[176:179], a[144:147], v[212:215], v[176:179]\n"
      "ds_read_b64_tr_b16 a[136:137], v232 offset:256\n"
      "v_exp_f32 v196, v196\n"
      "v_cvt_pk_bf16_f32 v209, v194, v195\n"
      "v_mfma_f32_16x16x32_bf16 v[180:183], a[148:151], v[212:215], v[180:183]\n"
      "ds_read_b64_tr_b16 a[138:139], v232 offset:4352\n"
      "v_exp_f32 v197, v197\n"
      "v_mfma_f32_16x16x32_bf16 v[184:187], a[152:155], v[212:215], v[184:187]\n"
      "ds_read_b64_tr_b16 a[140:141], v233 offset:256\n"
      "v_exp_f32 v198, v198\n"
      "v_cvt_pk_bf16_f32 v210, v196, v197\n"
      "v_mfma_f32_16x16x32_bf16 v[188:191], a[156:159], v[212:215], v[188:191]\n"
      "ds_read_b64_tr_b16 a[142:143], v233 offset:4352\n"
      "v_exp_f32 v199, v199\n"
      "v_mfma_f32_16x16x32_bf16 a[244:247], a[248:251], v[212:215], a[244:247]\n"
      "ds_read_b64_tr_b16 a[144:145], v232 offset:512\n"
      "v_cvt_pk_bf16_f32 v211, v198, v199\n"
      "v_mfma_f32_16x16x32_bf16 v[192:195], a[96:99], a[32:35], 0\n"
      "ds_read_b64_tr_b16 a[146:147], v232 offset:4608\n"
      "v_fma_f32 v200, v200, s76, -v217\n"
      "v_fma_f32 v201, v201, s76, -v217\n"
      "v_mfma_f32_16x16x32_bf16 v[196:199], a[112:115], a[32:35], 0\n"
      "ds_read_b64_tr_b16 a[148:149], v233 offset:512\n"
      "v_fma_f32 v202, v202, s76, -v217\n"
      "v_max3_f32 v228, v200, v201, v202\n"
      "v_mfma_f32_16x16x32_bf16 v[192:195], a[100:103], a[36:39], v[192:195]\n"
      "ds_read_b64_tr_b16 a[150:151], v233 offset:4608\n"
      "v_fma_f32 v203, v203, s76, -v217\n"
      "v_fma_f32 v204, v204, s76, -v217\n"
      "v_mfma_f32_16x16x32_bf16 v[196:199], a[116:119], a[36:39], v[196:199]\n"
      "ds_read_b64_tr_b16 a[152:153], v232 offset:768\n"
      "v_max3_f32 v228, v228, v203, v204\n"
      "v_mfma_f32_16x16x32_bf16 v[192:195], a[104:107], a[40:43], v[192:195]\n"
      "ds_read_b64_tr_b16 a[154:155], v232 offset:4864\n"
      "v_fma_f32 v205, v205, s76, -v217\n"
      "v_fma_f32 v206, v206, s76, -v217\n"
      "v_mfma_f32_16x16x32_bf16 v[196:199], a[120:123], a[40:43], v[196:199]\n"
      "ds_read_b64_tr_b16 a[156:157], v233 offset:768\n"
      "v_max3_f32 v228, v228, v205, v206\n"
      "v_fma_f32 v207, v207, s76, -v217\n"
      "v_mfma_f32_16x16x32_bf16 v[192:195], a[108:111], a[44:47], v[192:195]\n"
      "v_max_f32 v228, v228, v207\n"
      "v_cmp_nge_f32 vcc, s77, v228\n"
      "v_mfma_f32_16x16x32_bf16 v[196:199], a[124:127], a[44:47], v[196:199]\n"
      "s_waitcnt lgkmcnt(13)\n"
      "ds_read_b64_tr_b16 a[158:159], v233 offset:4864\n"
      "v_mfma_f32_16x16x32_bf16 v[0:3], a[128:131], v[208:211], v[0:3]\n"
      "s_waitcnt lgkmcnt(12)\n"
      "s_cbranch_vccnz CRb_1_%=\n"
      "BRb_1_%=:\n"
      "v_exp_f32 v200, v200\n"
      "v_mfma_f32_16x16x32_bf16 v[4:7], a[132:135], v[208:211], v[4:7]\n"
      "s_waitcnt lgkmcnt(10)\n"
      "v_exp_f32 v201, v201\n"
      "v_mfma_f32_16x16x32_bf16 v[8:11], a[136:139], v[208:211], v[8:11]\n"
      "s_waitcnt lgkmcnt(8)\n"
      "v_exp_f32 v202, v202\n"
      "v_cvt_pk_bf16_f32 v212, v200, v201\n"
      "v_mfma_f32_16x16x32_bf16 v[12:15], a[140:143], v[208:211], v[12:15]\n"
      "s_waitcnt lgkmcnt(6)\n"
      "v_exp_f32 v203, v203\n"
      "v_mfma_f32_16x16x32_bf16 v[16:19], a[144:147], v[208:211], v[16:19]\n"
      "s_waitcnt lgkmcnt(4)\n"
      "v_exp_f32 v204, v204\n"
      "v_cvt_pk_bf16_f32 v213, v202, v203\n"
      "v_mfma_f32_16x16x32_bf16 v[20:23], a[148:151], v[208:211], v[20:23]\n"
      "s_waitcnt lgkmcnt(2)\n"
      "v_exp_f32 v205, v205\n"
      "v_mfma_f32_16x16x32_bf16 v[24:27], a[152:155], v[208:211], v[24:27]\n"
      "s_waitcnt lgkmcnt(0)\n"
      "v_exp_f32 v206, v206\n"
      "v_cvt_pk_bf16_f32 v214, v204, v205\n"
      "v_mfma_f32_16x16x32_bf16 v[28:31], a[156:159], v[208:211], v[28:31]\n"
      "v_exp_f32 v207, v207\n"
      "v_mfma_f32_16x16x32_bf16 a[224:227], a[248:251], v[208:211], a[224:227]\n"
      "v_cvt_pk_bf16_f32 v215, v206, v207\n"
      "v_mfma_f32_16x16x32_bf16 v[200:203], a[96:99], a[48:51], 0\n"
      "v_fma_f32 v192, v192, s76, -v218\n"
      "v_fma_f32 v193, v193, s76, -v218\n"
      "v_mfma_f32_16x16x32_bf16 v[204:207], a[112:115], a[48:51], 0\n"
      "v_fma_f32 v194, v194, s76, -v218\n"
      "v_max3_f32 v228, v192, v193, v194\n"
      "v_mfma_f32_16x16x32_bf16 v[200:203], a[100:103], a[52:55], v[200:203]\n"
      "v_fma_f32 v195, v195, s76, -v218\n"
      "v_fma_f32 v196, v196, s76, -v218\n"
      "v_mfma_f32_16x16x32_bf16 v[204:207], a[116:119], a[52:55], v[204:207]\n"
      "v_max3_f32 v228, v228, v195, v196\n"
      "v_mfma_f32_16x16x32_bf16 v[200:203], a[104:107], a[56:59], v[200:203]\n"
      "v_fma_f32 v197, v197, s76, -v218\n"
      "v_fma_f32 v198, v198, s76, -v218\n"
      "v_mfma_f32_16x16x32_bf16 v[204:207], a[120:123], a[56:59], v[204:207]\n"
      "v_max3_f32 v228, v228, v197, v198\n"
      "v_fma_f32 v199, v199, s76, -v218\n"
      "v_mfma_f32_16x16x32_bf16 v[200:203], a[108:111], a[60:63], v[200:203]\n"
      "v_max_f32 v228, v228, v199\n"
      "v_cmp_nge_f32 vcc, s77, v228\n"
      "v_mfma_f32_16x16x32_bf16 v[204:207], a[124:127], a[60:63], v[204:207]\n"
      "v_xor_b32 v232, 65536, v232\n"
      "v_mfma_f32_16x16x32_bf16 v[32:35], a[128:131], v[212:215], v[32:35]\n"
      "s_cbranch_vccnz CRb_2_%=\n"
      "BRb_2_%=:\n"
      "v_exp_f32 v192, v192\n"
      "v_mfma_f32_16x16x32_bf16 v[36:39], a[132:135], v[212:215], v[36:39]\n"
      "v_exp_f32 v193, v193\n"
      "v_mfma_f32_16x16x32_bf16 v[40:43], a[136:139], v[212:215], v[40:43]\n"
      "v_exp_f32 v194, v194\n"
      "v_cvt_pk_bf16_f32 v208, v192, v193\n"
      "v_mfma_f32_16x16x32_bf16 v[44:47], a[140:143], v[212:215], v[44:47]\n"
      "v_exp_f32 v195, v195\n"
      "v_mfma_f32_16x16x32_bf16 v[48:51], a[144:147], v[212:215], v[48:51]\n"
      "v_exp_f32 v196, v196\n"
      "v_cvt_pk_bf16_f32 v209, v194, v195\n"
      "v_mfma_f32_16x16x32_bf16 v[52:55], a[148:151], v[212:215], v[52:55]\n"
      "v_exp_f32 v197, v197\n"
      "v_mfma_f32_16x16x32_bf16 v[56:59], a[152:155], v[212:215], v[56:59]\n"
      "v_exp_f32 v198, v198\n"
      "v_cvt_pk_bf16_f32 v210, v196, v197\n"
      "v_mfma_f32_16x16x32_bf16 v[60:63], a[156:159], v[212:215], v[60:63]\n"
      "v_exp_f32 v199, v199\n"
      "v_mfma_f32_16x16x32_bf16 a[228:231], a[248:251], v[212:215], a[228:231]\n"
      "v_cvt_pk_bf16_f32 v211, v198, v199\n"
      "v_mfma_f32_16x16x32_bf16 v[192:195], a[96:99], a[64:67], 0\n"
      "v_fma_f32 v200, v200, s76, -v219\n"
      "v_fma_f32 v201, v201, s76, -v219\n"
      "v_mfma_f32_16x16x32_bf16 v[196:199], a[112:115], a[64:67], 0\n"
      "v_fma_f32 v202, v202, s76, -v219\n"
      "v_max3_f32 v228, v200, v201, v202\n"
      "v_mfma_f32_16x16x32_bf16 v[192:195], a[100:103], a[68:71], v[192:195]\n"
      "v_fma_f32 v203, v203, s76, -v219\n"
      "v_fma_f32 v204, v204, s76, -v219\n"
      "v_mfma_f32_16x16x32_bf16 v[196:199], a[116:119], a[68:71], v[196:199]\n"
      "v_max3_f32 v228, v228, v203, v204\n"
      "v_mfma_f32_16x16x32_bf16 v[192:195], a[104:107], a[72:75], v[192:195]\n"
      "v_fma_f32 v205, v205, s76, -v219\n"
      "v_fma_f32 v206, v206, s76, -v219\n"
      "v_mfma_f32_16x16x32_bf16 v[196:199], a[120:123], a[72:75], v[196:199]\n"
      "v_max3_f32 v228, v228, v205, v206\n"
      "v_fma_f32 v207, v207, s76, -v219\n"
      "v_mfma_f32_16x16x32_bf16 v[192:195], a[108:111], a[76:79], v[192:195]\n"
      "v_max_f32 v228, v228, v207\n"
      "v_cmp_nge_f32 vcc, s77, v228\n"
      "v_mfma_f32_16x16x32_bf16 v[196:199], a[124:127], a[76:79], v[196:199]\n"
      "v_xor_b32 v233, 65536, v233\n"
      "v_mfma_f32_16x16x32_bf16 v[64:67], a[128:131], v[208:211], v[64:67]\n"
      "s_cbranch_vccnz CRb_3_%=\n"
      "BRb_3_%=:\n"
      "v_exp_f32 v200, v200\n"
      "v_mfma_f32_16x16x32_bf16 v[68:71], a[132:135], v[208:211], v[68:71]\n"
      "v_exp_f32 v201, v201\n"
      "v_mfma_f32_16x16x32_bf16 v[72:75], a[136:139], v[208:211], v[72:75]\n"
      "v_exp_f32 v202, v202\n"
      "v_cvt_pk_bf16_f32 v212, v200, v201\n"
      "v_mfma_f32_16x16x32_bf16 v[76:79], a[140:143], v[208:211], v[76:79]\n"
      "v_exp_f32 v203, v203\n"
      "v_mfma_f32_16x16x32_bf16 v[80:83], a[144:147], v[208:211], v[80:83]\n"
      "v_exp_f32 v204, v204\n"
      "v_cvt_pk_bf16_f32 v213, v202, v203\n"
      "v_mfma_f32_16x16x32_bf16 v[84:87], a[148:151], v[208:211], v[84:87]\n"
      "v_exp_f32 v205, v205\n"
      "v_mfma_f32_16x16x32_bf16 v[88:91], a[152:155], v[208:211], v[88:91]\n"
      "v_exp_f32 v206, v206\n"
      "v_cvt_pk_bf16_f32 v214, v204, v205\n"
      "v_mfma_f32_16x16x32_bf16 v[92:95], a[156:159], v[208:211], v[92:95]\n"
      "v_exp_f32 v207, v207\n"
      "v_mfma_f32_16x16x32_bf16 a[232:235], a[248:251], v[208:211], a[232:235]\n"
      "v_cvt_pk_bf16_f32 v215, v206, v207\n"
      "v_mfma_f32_16x16x32_bf16 v[200:203], a[96:99], a[80:83], 0\n"
      "s_waitcnt vmcnt(8)\n"
      "ds_read_b128 a[96:99], v231 offset:0\n"
      "v_fma_f32 v192, v192, s76, -v220\n"
      "v_fma_f32 v193, v193, s76, -v220\n"
      "v_mfma_f32_16x16x32_bf16 v[204:207], a[112:115], a[80:83], 0\n"
      "ds_read_b128 a[112:115], v231 offset:4096\n"
      "v_fma_f32 v194, v194, s76, -v220\n"
      "v_max3_f32 v228, v192, v193, v194\n"
      "v_mfma_f32_16x16x32_bf16 v[200:203], a[100:103], a[84:87], v[200:203]\n"
      "ds_read_b128 a[100:103], v231 offset:256\n"
      "v_fma_f32 v195, v195, s76, -v220\n"
      "v_fma_f32 v196, v196, s76, -v220\n"
      "v_mfma_f32_16x16x32_bf16 v[204:207], a[116:119], a[84:87], v[204:207]\n"
      "ds_read_b128 a[116:119], v231 offset:4352\n"
      "v_max3_f32 v228, v228, v195, v196\n"
      "v_mfma_f32_16x16x32_bf16 v[200:203], a[104:107], a[88:91], v[200:203]\n"
      "ds_read_b128 a[104:107], v231 offset:512\n"
      "v_fma_f32 v197, v197, s76, -v220\n"
      "v_fma_f32 v198, v198, s76, -v220\n"
      "v_mfma_f32_16x16x32_bf16 v[204:207], a[120:123], a[88:91], v[204:207]\n"
      "ds_read_b128 a[120:123], v231 offset:4608\n"
      "v_max3_f32 v228, v228, v197, v198\n"
      "v_fma_f32 v199, v199, s76, -v220\n"
      "v_mfma_f32_16x16x32_bf16 v[200:203], a[108:111], a[92:95], v[200:203]\n"
      "ds_read_b128 a[108:111], v231 offset:768\n"
      "v_max_f32 v228, v228, v199\n"
      "v_cmp_nge_f32 vcc, s77, v228\n"
      "v_mfma_f32_16x16x32_bf16 v[204:207], a[124:127], a[92:95], v[204:207]\n"
      "ds_read_b128 a[124:127], v231 offset:4864\n"
      "v_mfma_f32_16x16x32_bf16 v[96:99], a[128:131], v[212:215], v[96:99]\n"
      "s_cbranch_vccnz CRb_4_%=\n"
      "BRb_4_%=:\n"
      "v_exp_f32 v192, v192\n"
      "v_mfma_f32_16x16x32_bf16 v[100:103], a[132:135], v[212:215], v[100:103]\n"
      "v_exp_f32 v193, v193\n"
      "v_mfma_f32_16x16x32_bf16 v[104:107], a[136:139], v[212:215], v[104:107]\n"
      "v_exp_f32 v194, v194\n"
      "v_cvt_pk_bf16_f32 v208, v192, v193\n"
      "v_mfma_f32_16x16x32_bf16 v[108:111], a[140:143], v[212:215], v[108:111]\n"
      "v_exp_f32 v195, v195\n"
      "v_mfma_f32_16x16x32_bf16 v[112:115], a[144:147], v[212:215], v[112:115]\n"
      "v_exp_f32 v196, v196\n"
      "v_cvt_pk_bf16_f32 v209, v194, v195\n"
      "v_mfma_f32_16x16x32_bf16 v[116:119], a[148:151], v[212:215], v[116:119]\n"
      "v_exp_f32 v197, v197\n"
      "v_mfma_f32_16x16x32_bf16 v[120:123], a[152:155], v[212:215], v[120:123]\n"
      "v_exp_f32 v198, v198\n"
      "v_cvt_pk_bf16_f32 v210, v196, v197\n"
      "v_mfma_f32_16x16x32_bf16 v[124:127], a[156:159], v[212:215], v[124:127]\n"
      "v_exp_f32 v199, v199\n"
      "v_mfma_f32_16x16x32_bf16 a[236:239], a[248:251], v[212:215], a[236:239]\n"
      "s_waitcnt lgkmcnt(7)\n"
      "v_cvt_pk_bf16_f32 v211, v198, v199\n"
      "v_mfma_f32_16x16x32_bf16 v[192:195], a[96:99], a[0:3], 0\n"
      "s_waitcnt lgkmcnt(6)\n"
      "v_fma_f32 v200, v200, s76, -v221\n"
      "v_fma_f32 v201, v201, s76, -v221\n"
      "v_mfma_f32_16x16x32_bf16 v[196:199], a[112:115], a[0:3], 0\n"
      "s_waitcnt lgkmcnt(5)\n"
      "v_fma_f32 v202, v202, s76, -v221\n"
      "v_max3_f32 v228, v200, v201, v202\n"
      "v_mfma_f32_16x16x32_bf16 v[192:195], a[100:103], a[4:7], v[192:195]\n"
      "s_waitcnt lgkmcnt(4)\n"
      "v_fma_f32 v203, v203, s76, -v221\n"
      "v_fma_f32 v204, v204, s76, -v221\n"
      "v_mfma_f32_16x16x32_bf16 v[196:199], a[116:119], a[4:7], v[196:199]\n"
      "s_waitcnt lgkmcnt(3)\n"
      "v_max3_f32 v228, v228, v203, v204\n"
      "v_mfma_f32_16x16x32_bf16 v[192:195], a[104:107], a[8:11], v[192:195]\n"
      "s_waitcnt lgkmcnt(2)\n"
      "v_fma_f32 v205, v205, s76, -v221\n"
      "v_fma_f32 v206, v206, s76, -v221\n"
      "v_mfma_f32_16x16x32_bf16 v[196:199], a[120:123], a[8:11], v[196:199]\n"
      "s_waitcnt lgkmcnt(1)\n"
      "v_max3_f32 v228, v228, v205, v206\n"
      "v_fma_f32 v207, v207, s76, -v221\n"
      "v_mfma_f32_16x16x32_bf16 v[192:195], a[108:111], a[12:15], v[192:195]\n"
      "s_waitcnt lgkmcnt(0)\n"
      "v_max_f32 v228, v228, v207\n"
      "v_cmp_nge_f32 vcc, s77, v228\n"
      "v_mfma_f32_16x16x32_bf16 v[196:199], a[124:127], a[12:15], v[196:199]\n"
      "v_xor_b32 v231, 65536, v231\n"
      "v_mfma_f32_16x16x32_bf16 v[128:131], a[128:131], v[208:211], v[128:131]\n"
      "s_cbranch_vccnz CRb_5_%=\n"
      "BRb_5_%=:\n"
      "v_exp_f32 v200, v200\n"
      "v_mfma_f32_16x16x32_bf16 v[132:135], a[132:135], v[208:211], v[132:135]\n"
      "v_exp_f32 v201, v201\n"
      "v_mfma_f32_16x16x32_bf16 v[136:139], a[136:139], v[208:211], v[136:139]\n"
      "v_exp_f32 v202, v202\n"
      "v_cvt_pk_bf16_f32 v212, v200, v201\n"
      "v_mfma_f32_16x16x32_bf16 v[140:143], a[140:143], v[208:211], v[140:143]\n"
      "v_exp_f32 v203, v203\n"
      "v_mfma_f32_16x16x32_bf16 v[144:147], a[144:147], v[208:211], v[144:147]\n"
      "v_exp_f32 v204, v204\n"
      "v_cvt_pk_bf16_f32 v213, v202, v203\n"
      "v_mfma_f32_16x16x32_bf16 v[148:151], a[148:151], v[208:211], v[148:151]\n"
      "v_exp_f32 v205, v205\n"
      "v_mfma_f32_16x16x32_bf16 v[152:155], a[152:155], v[208:211], v[152:155]\n"
      "v_exp_f32 v206, v206\n"
      "v_cvt_pk_bf16_f32 v214, v204, v205\n"
      "v_mfma_f32_16x16x32_bf16 v[156:159], a[156:159], v[208:211], v[156:159]\n"
      "v_exp_f32 v207, v207\n"
      "v_mfma_f32_16x16x32_bf16 a[240:243], a[248:251], v[208:211], a[240:243]\n"
      "v_cvt_pk_bf16_f32 v215, v206, v207\n"
      "v_mfma_f32_16x16x32_bf16 v[200:203], a[96:99], a[16:19], 0\n"
      "s_mul_i32 s84, s83, 655360\n"
      "v_fma_f32 v192, v192, s76, -v216\n"
      "v_fma_f32 v193, v193, s76, -v216\n"
      "v_mfma_f32_16x16x32_bf16 v[204:207], a[112:115], a[16:19], 0\n"
      "s_lshl_b32 s73, s82, 8\n"
      "v_fma_f32 v194, v194, s76, -v216\n"
      "v_max3_f32 v228, v192, v193, v194\n"
      "v_mfma_f32_16x16x32_bf16 v[200:203], a[100:103], a[20:23], v[200:203]\n"
      "s_add_u32 m0, s72, 131072\n"
      "v_fma_f32 v195, v195, s76, -v216\n"
      "v_fma_f32 v196, v196, s76, -v216\n"
      "v_mfma_f32_16x16x32_bf16 v[204:207], a[116:119], a[20:23], v[204:207]\n"
      "s_add_u32 s84, s84, s73\n"
      "buffer_load_dwordx4 v247, s[52:55], s84 offen offset:0  lds\n"
      "v_max3_f32 v228, v228, v195, v196\n"
      "v_mfma_f32_16x16x32_bf16 v[200:203], a[104:107], a[24:27], v[200:203]\n"
      "v_fma_f32 v197, v197, s76, -v216\n"
      "v_fma_f32 v198, v198, s76, -v216\n"
      "v_mfma_f32_16x16x32_bf16 v[204:207], a[120:123], a[24:27], v[204:207]\n"
      "v_max3_f32 v228, v228, v197, v198\n"
      "v_fma_f32 v199, v199, s76, -v216\n"
      "v_mfma_f32_16x16x32_bf16 v[200:203], a[108:111], a[28:31], v[200:203]\n"
      "v_max_f32 v228, v228, v199\n"
      "v_cmp_nge_f32 vcc, s77, v228\n"
      "v_mfma_f32_16x16x32_bf16 v[204:207], a[124:127], a[28:31], v[204:207]\n"
      "buffer_load_dwordx4 v248, s[52:55], s84 offen offset:1024  lds\n"
      "v_mfma_f32_16x16x32_bf16 v[160:163], a[128:131], v[212:215], v[160:163]\n"
      "s_waitcnt vmcnt(2)\n"
      "s_cbranch_vccnz CRc_0_%=\n"
      "BRc_0_%=:\n"
      "ds_read_b64_tr_b16 a[128:129], v232 offset:0\n"
      "v_exp_f32 v192, v192\n"
      "v_mfma_f32_16x16x32_bf16 v[164:167], a[132:135], v[212:215], v[164:167]\n"
      "ds_read_b64_tr_b16 a[130:131], v232 offset:4096\n"
      "v_exp_f32 v193, v193\n"
      "v_mfma_f32_16x16x32_bf16 v[168:171], a[136:139], v[212:215], v[168:171]\n"
      "ds_read_b64_tr_b16 a[132:133], v233 offset:0\n"
      "v_exp_f32 v194, v194\n"
      "v_cvt_pk_bf16_f32 v208, v192, v193\n"
      "v_mfma_f32_16x16x32_bf16 v[172:175], a[140:143], v[212:215], v[172:175]\n"
      "ds_read_b64_tr_b16 a[134:135], v233 offset:4096\n"
      "v_exp_f32 v195, v195\n"
      "v_mfma_f32_16x16x32_bf16 v[176:179], a[144:147], v[212:215], v[176:179]\n"
      "ds_read_b64_tr_b16 a[136:137], v232 offset:256\n"
      "v_exp_f32 v196, v196\n"
      "v_cvt_pk_bf16_f32 v209, v194, v195\n"
      "v_mfma_f32_16x16x32_bf16 v[180:183], a[148:151], v[212:215], v[180:183]\n"
      "ds_read_b64_tr_b16 a[138:139], v232 offset:4352\n"
      "v_exp_f32 v197, v197\n"
      "v_mfma_f32_16x16x32_bf16 v[184:187], a[152:155], v[212:215], v[184:187]\n"
      "ds_read_b64_tr_b16 a[140:141], v233 offset:256\n"
      "v_exp_f32 v198, v198\n"
      "v_cvt_pk_bf16_f32 v210, v196, v197\n"
      "v_mfma_f32_16x16x32_bf16 v[188:191], a[156:159], v[212:215], v[188:191]\n"
      "ds_read_b64_tr_b16 a[142:143], v233 offset:4352\n"
      "v_exp_f32 v199, v199\n"
      "v_mfma_f32_16x16x32_bf16 a[244:247], a[248:251], v[212:215], a[244:247]\n"
      "ds_read_b64_tr_b16 a[144:145], v232 offset:512\n"
      "v_cvt_pk_bf16_f32 v211, v198, v199\n"
      "v_mfma_f32_16x16x32_bf16 v[192:195], a[96:99], a[32:35], 0\n"
      "ds_read_b64_tr_b16 a[146:147], v232 offset:4608\n"
      "v_fma_f32 v200, v200, s76, -v217\n"
      "v_fma_f32 v201, v201, s76, -v217\n"
      "v_mfma_f32_16x16x32_bf16 v[196:199], a[112:115], a[32:35], 0\n"
      "ds_read_b64_tr_b16 a[148:149], v233 offset:512\n"
      "v_fma_f32 v202, v202, s76, -v217\n"
      "v_max3_f32 v228, v200, v201, v202\n"
      "v_mfma_f32_16x16x32_bf16 v[192:195], a[100:103], a[36:39], v[192:195]\n"
      "ds_read_b64_tr_b16 a[150:151], v233 offset:4608\n"
      "v_fma_f32 v203, v203, s76, -v217\n"
      "v_fma_f32 v204, v204, s76, -v217\n"
      "v_mfma_f32_16x16x32_bf16 v[196:199], a[116:119], a[36:39], v[196:199]\n"
      "ds_read_b64_tr_b16 a[152:153], v232 offset:768\n"
      "v_max3_f32 v228, v228, v203, v204\n"
      "v_mfma_f32_16x16x32_bf16 v[192:195], a[104:107], a[40:43], v[192:195]\n"
      "ds_read_b64_tr_b16 a[154:155], v232 offset:4864\n"
      "v_fma_f32 v205, v205, s76, -v217\n"
      "v_fma_f32 v206, v206, s76, -v217\n"
      "v_mfma_f32_16x16x32_bf16 v[196:199], a[120:123], a[40:43], v[196:199]\n"
      "ds_read_b64_tr_b16 a[156:157], v233 offset:768\n"
      "v_max3_f32 v228, v228, v205, v206\n"
      "v_fma_f32 v207, v207, s76, -v217\n"
      "v_mfma_f32_16x16x32_bf16 v[192:195], a[108:111], a[44:47], v[192:195]\n"
      "buffer_load_dwordx4 v249, s[52:55], s84 offen offset:2048  lds\n"
      "v_max_f32 v228, v228, v207\n"
      "v_cmp_nge_f32 vcc, s77, v228\n"
      "v_mfma_f32_16x16x32_bf16 v[196:199], a[124:127], a[44:47], v[196:199]\n"
      "s_waitcnt lgkmcnt(13)\n"
      "ds_read_b64_tr_b16 a[158:159], v233 offset:4864\n"
      "buffer_load_dwordx4 v250, s[52:55], s84 offen offset:3072  lds\n"
      "s_add_u32 m0, s72, 135168\n"
      "v_mfma_f32_16x16x32_bf16 v[0:3], a[128:131], v[208:211], v[0:3]\n"
      "s_waitcnt lgkmcnt(12)\n"
      "s_cbranch_vccnz CRc_1_%=\n"
      "BRc_1_%=:\n"
      "v_exp_f32 v200, v200\n"
      "v_mfma_f32_16x16x32_bf16 v[4:7], a[132:135], v[208:211], v[4:7]\n"
      "s_waitcnt lgkmcnt(10)\n"
      "s_add_u32 s84, s84, 20480\n"
      "v_exp_f32 v201, v201\n"
      "v_mfma_f32_16x16x32_bf16 v[8:11], a[136:139], v[208:211], v[8:11]\n"
      "s_waitcnt lgkmcnt(8)\n"
      "buffer_load_dwordx4 v247, s[52:55], s84 offen offset:0  lds\n"
      "v_exp_f32 v202, v202\n"
      "v_cvt_pk_bf16_f32 v212, v200, v201\n"
      "v_mfma_f32_16x16x32_bf16 v[12:15], a[140:143], v[208:211], v[12:15]\n"
      "s_waitcnt lgkmcnt(6)\n"
      "v_exp_f32 v203, v203\n"
      "v_mfma_f32_16x16x32_bf16 v[16:19], a[144:147], v[208:211], v[16:19]\n"
      "s_waitcnt lgkmcnt(4)\n"
      "v_exp_f32 v204, v204\n"
      "v_cvt_pk_bf16_f32 v213, v202, v203\n"
      "v_mfma_f32_16x16x32_bf16 v[20:23], a[148:151], v[208:211], v[20:23]\n"
      "s_waitcnt lgkmcnt(2)\n"
      "v_exp_f32 v205, v205\n"
      "v_mfma_f32_16x16x32_bf16 v[24:27], a[152:155], v[208:211], v[24:27]\n"
      "s_waitcnt lgkmcnt(0)\n"
      "v_exp_f32 v206, v206\n"
      "v_cvt_pk_bf16_f32 v214, v204, v205\n"
      "v_mfma_f32_16x16x32_bf16 v[28:31], a[156:159], v[208:211], v[28:31]\n"
      "v_exp_f32 v207, v207\n"
      "v_mfma_f32_16x16x32_bf16 a[224:227], a[248:251], v[208:211], a[224:227]\n"
      "buffer_load_dwordx4 v248, s[52:55], s84 offen offset:1024  lds\n"
      "v_cvt_pk_bf16_f32 v215, v206, v207\n"
      "v_mfma_f32_16x16x32_bf16 v[200:203], a[96:99], a[48:51], 0\n"
      "v_fma_f32 v192, v192, s76, -v218\n"
      "v_fma_f32 v193, v193, s76, -v218\n"
      "v_mfma_f32_16x16x32_bf16 v[204:207], a[112:115], a[48:51], 0\n"
      "v_fma_f32 v194, v194, s76, -v218\n"
      "v_max3_f32 v228, v192, v193, v194\n"
      "v_mfma_f32_16x16x32_bf16 v[200:203], a[100:103], a[52:55], v[200:203]\n"
      "v_fma_f32 v195, v195, s76, -v218\n"
      "v_fma_f32 v196, v196, s76, -v218\n"
      "v_mfma_f32_16x16x32_bf16 v[204:207], a[116:119], a[52:55], v[204:207]\n"
      "buffer_load_dwordx4 v249, s[52:55], s84 offen offset:2048  lds\n"
      "v_max3_f32 v228, v228, v195, v196\n"
      "v_mfma_f32_16x16x32_bf16 v[200:203], a[104:107], a[56:59], v[200:203]\n"
      "v_fma_f32 v197, v197, s76, -v218\n"
      "v_fma_f32 v198, v198, s76, -v218\n"
      "v_mfma_f32_16x16x32_bf16 v[204:207], a[120:123], a[56:59], v[204:207]\n"
      "v_max3_f32 v228, v228, v197, v198\n"
      "v_fma_f32 v199, v199, s76, -v218\n"
      "v_mfma_f32_16x16x32_bf16 v[200:203], a[108:111], a[60:63], v[200:203]\n"
      "v_max_f32 v228, v228, v199\n"
      "v_cmp_nge_f32 vcc, s77, v228\n"
      "v_mfma_f32_16x16x32_bf16 v[204:207], a[124:127], a[60:63], v[204:207]\n"
      "buffer_load_dwordx4 v250, s[52:55], s84 offen offset:3072  lds\n"
      "s_add_u32 s83, s83, 1\n"
      "v_xor_b32 v232, 65536, v232\n"
      "v_mfma_f32_16x16x32_bf16 v[32:35], a[128:131], v[212:215], v[32:35]\n"
      "s_cbranch_vccnz CRc_2_%=\n"
      "BRc_2_%=:\n"
      "v_exp_f32 v192, v192\n"
      "v_mfma_f32_16x16x32_bf16 v[36:39], a[132:135], v[212:215], v[36:39]\n"
      "s_and_b32 s83, s83, 3\n"
      "v_exp_f32 v193, v193\n"
      "v_mfma_f32_16x16x32_bf16 v[40:43], a[136:139], v[212:215], v[40:43]\n"
      "s_mul_i32 s84, s83, 655360\n"
      "v_exp_f32 v194, v194\n"
      "v_cvt_pk_bf16_f32 v208, v192, v193\n"
      "v_mfma_f32_16x16x32_bf16 v[44:47], a[140:143], v[212:215], v[44:47]\n"
      "v_exp_f32 v195, v195\n"
      "v_mfma_f32_16x16x32_bf16 v[48:51], a[144:147], v[212:215], v[48:51]\n"
      "s_lshl_b32 s73, s82, 8\n"
      "v_exp_f32 v196, v196\n"
      "v_cvt_pk_bf16_f32 v209, v194, v195\n"
      "v_mfma_f32_16x16x32_bf16 v[52:55], a[148:151], v[212:215], v[52:55]\n"
      "v_exp_f32 v197, v197\n"
      "v_mfma_f32_16x16x32_bf16 v[56:59], a[152:155], v[212:215], v[56:59]\n"
      "v_exp_f32 v198, v198\n"
      "v_cvt_pk_bf16_f32 v210, v196, v197\n"
      "v_mfma_f32_16x16x32_bf16 v[60:63], a[156:159], v[212:215], v[60:63]\n"
      "s_add_u32 m0, s72, 0\n"
      "v_exp_f32 v199, v199\n"
      "v_mfma_f32_16x16x32_bf16 a[228:231], a[248:251], v[212:215], a[228:231]\n"
      "s_add_u32 s84, s84, s73\n"
      "buffer_load_dwordx4 v247, s[52:55], s84 offen offset:0  lds\n"
      "v_cvt_pk_bf16_f32 v211, v198, v199\n"
      "v_mfma_f32_16x16x32_bf16 v[192:195], a[96:99], a[64:67], 0\n"
      "v_fma_f32 v200, v200, s76, -v219\n"
      "v_fma_f32 v201, v201, s76, -v219\n"
      "v_mfma_f32_16x16x32_bf16 v[196:199], a[112:115], a[64:67], 0\n"
      "v_fma_f32 v202, v202, s76, -v219\n"
      "v_max3_f32 v228, v200, v201, v202\n"
      "v_mfma_f32_16x16x32_bf16 v[192:195], a[100:103], a[68:71], v[192:195]\n"
      "v_fma_f32 v203, v203, s76, -v219\n"
      "v_fma_f32 v204, v204, s76, -v219\n"
      "v_mfma_f32_16x16x32_bf16 v[196:199], a[116:119], a[68:71], v[196:199]\n"
      "buffer_load_dwordx4 v248, s[52:55], s84 offen offset:1024  lds\n"
      "v_max3_f32 v228, v228, v203, v204\n"
      "v_mfma_f32_16x16x32_bf16 v[192:195], a[104:107], a[72:75], v[192:195]\n"
      "v_fma_f32 v205, v205, s76, -v219\n"
      "v_fma_f32 v206, v206, s76, -v219\n"
      "v_mfma_f32_16x16x32_bf16 v[196:199], a[120:123], a[72:75], v[196:199]\n"
      "v_max3_f32 v228, v228, v205, v206\n"
      "v_fma_f32 v207, v207, s76, -v219\n"
      "v_mfma_f32_16x16x32_bf16 v[192:195], a[108:111], a[76:79], v[192:195]\n"
      "v_max_f32 v228, v228, v207\n"
      "v_cmp_nge_f32 vcc, s77, v228\n"
      "v_mfma_f32_16x16x32_bf16 v[196:199], a[124:127], a[76:79], v[196:199]\n"
      "buffer_load_dwordx4 v249, s[52:55], s84 offen offset:2048  lds\n"
      "v_xor_b32 v233, 65536, v233\n"
      "v_mfma_f32_16x16x32_bf16 v[64:67], a[128:131], v[208:211], v[64:67]\n"
      "s_cbranch_vccnz CRc_3_%=\n"
      "BRc_3_%=:\n"
      "v_exp_f32 v200, v200\n"
      "v_mfma_f32_16x16x32_bf16 v[68:71], a[132:135], v[208:211], v[68:71]\n"
      "v_exp_f32 v201, v201\n"
      "v_mfma_f32_16x16x32_bf16 v[72:75], a[136:139], v[208:211], v[72:75]\n"
      "v_exp_f32 v202, v202\n"
      "v_cvt_pk_bf16_f32 v212, v200, v201\n"
      "v_mfma_f32_16x16x32_bf16 v[76:79], a[140:143], v[208:211], v[76:79]\n"
      "buffer_load_dwordx4 v250, s[52:55], s84 offen offset:3072  lds\n"
      "v_exp_f32 v203, v203\n"
      "v_mfma_f32_16x16x32_bf16 v[80:83], a[144:147], v[208:211], v[80:83]\n"
      "v_exp_f32 v204, v204\n"
      "v_cvt_pk_bf16_f32 v213, v202, v203\n"
      "v_mfma_f32_16x16x32_bf16 v[84:87], a[148:151], v[208:211], v[84:87]\n"
      "v_exp_f32 v205, v205\n"
      "v_mfma_f32_16x16x32_bf16 v[88:91], a[152:155], v[208:211], v[88:91]\n"
      "s_add_u32 m0, s72, 4096\n"
      "v_exp_f32 v206, v206\n"
      "v_cvt_pk_bf16_f32 v214, v204, v205\n"
      "v_mfma_f32_16x16x32_bf16 v[92:95], a[156:159], v[208:211], v[92:95]\n"
      "v_exp_f32 v207, v207\n"
      "v_mfma_f32_16x16x32_bf16 a[232:235], a[248:251], v[208:211], a[232:235]\n"
      "s_add_u32 s84, s84, 20480\n"
      "buffer_load_dwordx4 v247, s[52:55], s84 offen offset:0  lds\n"
      "v_cvt_pk_bf16_f32 v215, v206, v207\n"
      "v_mfma_f32_16x16x32_bf16 v[200:203], a[96:99], a[80:83], 0\n"
      "v_fma_f32 v192, v192, s76, -v220\n"
      "v_fma_f32 v193, v193, s76, -v220\n"
      "v_mfma_f32_16x16x32_bf16 v[204:207], a[112:115], a[80:83], 0\n"
      "v_fma_f32 v194, v194, s76, -v220\n"
      "v_max3_f32 v228, v192, v193, v194\n"
      "v_mfma_f32_16x16x32_bf16 v[200:203], a[100:103], a[84:87], v[200:203]\n"
      "v_fma_f32 v195, v195, s76, -v220\n"
      "v_fma_f32 v196, v196, s76, -v220\n"
      "v_mfma_f32_16x16x32_bf16 v[204:207], a[116:119], a[84:87], v[204:207]\n"
      "buffer_load_dwordx4 v248, s[52:55], s84 offen offset:1024  lds\n"
      "v_max3_f32 v228, v228, v195, v196\n"
      "v_mfma_f32_16x16x32_bf16 v[200:203], a[104:107], a[88:91], v[200:203]\n"
      "v_fma_f32 v197, v197, s76, -v220\n"
      "v_fma_f32 v198, v198, s76, -v220\n"
      "v_mfma_f32_16x16x32_bf16 v[204:207], a[120:123], a[88:91], v[204:207]\n"
      "v_max3_f32 v228, v228, v197, v198\n"
      "v_fma_f32 v199, v199, s76, -v220\n"
      "v_mfma_f32_16x16x32_bf16 v[200:203], a[108:111], a[92:95], v[200:203]\n"
      "v_max_f32 v228, v228, v199\n"
      "v_cmp_nge_f32 vcc, s77, v228\n"
      "v_mfma_f32_16x16x32_bf16 v[204:207], a[124:127], a[92:95], v[204:207]\n"
      "buffer_load_dwordx4 v249, s[52:55], s84 offen offset:2048  lds\n"
      "v_mfma_f32_16x16x32_bf16 v[96:99], a[128:131], v[212:215], v[96:99]\n"
      "s_cbranch_vccnz CRc_4_%=\n"
      "BRc_4_%=:\n"
      "v_exp_f32 v192, v192\n"
      "v_mfma_f32_16x16x32_bf16 v[100:103], a[132:135], v[212:215], v[100:103]\n"
      "v_exp_f32 v193, v193\n"
      "v_mfma_f32_16x16x32_bf16 v[104:107], a[136:139], v[212:215], v[104:107]\n"
      "buffer_load_dwordx4 v250, s[52:55], s84 offen offset:3072  lds\n"
      "v_exp_f32 v194, v194\n"
      "v_cvt_pk_bf16_f32 v208, v192, v193\n"
      "v_mfma_f32_16x16x32_bf16 v[108:111], a[140:143], v[212:215], v[108:111]\n"
      "v_exp_f32 v195, v195\n"
      "v_mfma_f32_16x16x32_bf16 v[112:115], a[144:147], v[212:215], v[112:115]\n"
      "v_exp_f32 v196, v196\n"
      "v_cvt_pk_bf16_f32 v209, v194, v195\n"
      "v_mfma_f32_16x16x32_bf16 v[116:119], a[148:151], v[212:215], v[116:119]\n"
      "s_add_u32 s83, s83, 1\n"
      "v_exp_f32 v197, v197\n"
      "v_mfma_f32_16x16x32_bf16 v[120:123], a[152:155], v[212:215], v[120:123]\n"
      "v_exp_f32 v198, v198\n"
      "v_cvt_pk_bf16_f32 v210, v196, v197\n"
      "v_mfma_f32_16x16x32_bf16 v[124:127], a[156:159], v[212:215], v[124:127]\n"
      "s_and_b32 s83, s83, 3\n"
      "v_exp_f32 v199, v199\n"
      "v_mfma_f32_16x16x32_bf16 a[236:239], a[248:251], v[212:215], a[236:239]\n"
      "v_cvt_pk_bf16_f32 v211, v198, v199\n"
      "s_nop 1\n"
      "v_mfma_f32_16x16x32_bf16 v[128:131], a[128:131], v[208:211], v[128:131]\n"
      "v_fma_f32 v200, v200, s76, -v221\n"
      "v_fma_f32 v201, v201, s76, -v221\n"
      "v_fma_f32 v202, v202, s76, -v221\n"
      "v_max3_f32 v228, v200, v201, v202\n"
      "v_mfma_f32_16x16x32_bf16 v[132:135], a[132:135], v[208:211], v[132:135]\n"
      "v_fma_f32 v203, v203, s76, -v221\n"
      "v_fma_f32 v204, v204, s76, -v221\n"
      "v_max3_f32 v228, v228, v203, v204\n"
      "v_fma_f32 v205, v205, s76, -v221\n"
      "v_fma_f32 v206, v206, s76, -v221\n"
      "v_mfma_f32_16x16x32_bf16 v[136:139], a[136:139], v[208:211], v[136:139]\n"
      "v_max3_f32 v228, v228, v205, v206\n"
      "v_fma_f32 v207, v207, s76, -v221\n"
      "v_max_f32 v228, v228, v207\n"
      "v_cmp_nge_f32 vcc, s77, v228\n"
      "v_mfma_f32_16x16x32_bf16 v[140:143], a[140:143], v[208:211], v[140:143]\n"
      "v_mfma_f32_16x16x32_bf16 v[144:147], a[144:147], v[208:211], v[144:147]\n"
      "s_cbranch_vccnz CRc_5_%=\n"
      "BRc_5_%=:\n"
      "v_exp_f32 v200, v200\n"
      "v_exp_f32 v201, v201\n"
      "v_exp_f32 v202, v202\n"
      "v_cvt_pk_bf16_f32 v212, v200, v201\n"
      "v_mfma_f32_16x16x32_bf16 v[148:151], a[148:151], v[208:211], v[148:151]\n"
      "v_exp_f32 v203, v203\n"
      "v_exp_f32 v204, v204\n"
      "v_cvt_pk_bf16_f32 v213, v202, v203\n"
      "v_mfma_f32_16x16x32_bf16 v[152:155], a[152:155], v[208:211], v[152:155]\n"
      "v_exp_f32 v205, v205\n"
      "v_mfma_f32_16x16x32_bf16 v[156:159], a[156:159], v[208:211], v[156:159]\n"
      "v_exp_f32 v206, v206\n"
      "v_cvt_pk_bf16_f32 v214, v204, v205\n"
      "v_mfma_f32_16x16x32_bf16 a[240:243], a[248:251], v[208:211], a[240:243]\n"
      "v_exp_f32 v207, v207\n"
      "s_nop 0\n"
      "v_cvt_pk_bf16_f32 v215, v206, v207\n"
      "s_nop 1\n"
      "v_mfma_f32_16x16x32_bf16 v[160:163], a[128:131], v[212:215], v[160:163]\n"
      "v_mfma_f32_16x16x32_bf16 v[164:167], a[132:135], v[212:215], v[164:167]\n"
      "v_mfma_f32_16x16x32_bf16 v[168:171], a[136:139], v[212:215], v[168:171]\n"
      "v_mfma_f32_16x16x32_bf16 v[172:175], a[140:143], v[212:215], v[172:175]\n"
      "v_mfma_f32_16x16x32_bf16 v[176:179], a[144:147], v[212:215], v[176:179]\n"
      "v_mfma_f32_16x16x32_bf16 v[180:183], a[148:151], v[212:215], v[180:183]\n"
      "v_mfma_f32_16x16x32_bf16 v[184:187], a[152:155], v[212:215], v[184:187]\n"
      "v_mfma_f32_16x16x32_bf16 v[188:191], a[156:159], v[212:215], v[188:191]\n"
      "v_mfma_f32_16x16x32_bf16 a[244:247], a[248:251], v[212:215], a[244:247]\n"
      "v_add_u32 v240, s72, v242\n"
      "v_add_u32 v240, 32768, v240\n"
      "v_add_u32 v241, 65536, v240\n"
      "v_accvgpr_read_b32 v222, a224\n"
      "v_accvgpr_read_b32 v223, a228\n"
      "v_accvgpr_read_b32 v224, a232\n"
      "v_accvgpr_read_b32 v225, a236\n"
      "v_accvgpr_read_b32 v226, a240\n"
      "v_accvgpr_read_b32 v227, a244\n"
      "v_rcp_f32 v222, v222\n"
      "v_rcp_f32 v223, v223\n"
      "v_rcp_f32 v224, v224\n"
      "v_rcp_f32 v225, v225\n"
      "v_rcp_f32 v226, v226\n"
      "v_rcp_f32 v227, v227\n"
      "v_pk_mul_f32 v[0:1], v[0:1], v[222:223] op_sel:[0,0] op_sel_hi:[1,0]\n"
      "v_pk_mul_f32 v[2:3], v[2:3], v[222:223] op_sel:[0,0] op_sel_hi:[1,0]\n"
      "v_pk_mul_f32 v[4:5], v[4:5], v[222:223] op_sel:[0,0] op_sel_hi:[1,0]\n"
      "v_pk_mul_f32 v[6:7], v[6:7], v[222:223] op_sel:[0,0] op_sel_hi:[1,0]\n"
      "v_cvt_pk_bf16_f32 v0, v0, v1\n"
      "v_cvt_pk_bf16_f32 v1, v2, v3\n"
      "v_cvt_pk_bf16_f32 v2, v4, v5\n"
      "v_cvt_pk_bf16_f32 v3, v6, v7\n"
      "ds_write_b128 v240, v[0:3] offset:0\n"
      "v_pk_mul_f32 v[8:9], v[8:9], v[222:223] op_sel:[0,0] op_sel_hi:[1,0]\n"
      "v_pk_mul_f32 v[10:11], v[10:11], v[222:223] op_sel:[0,0] op_sel_hi:[1,0]\n"
      "v_pk_mul_f32 v[12:13], v[12:13], v[222:223] op_sel:[0,0] op_sel_hi:[1,0]\n"
      "v_pk_mul_f32 v[14:15], v[14:15], v[222:223] op_sel:[0,0] op_sel_hi:[1,0]\n"
      "v_cvt_pk_bf16_f32 v8, v8, v9\n"
      "v_cvt_pk_bf16_f32 v9, v10, v11\n"
      "v_cvt_pk_bf16_f32 v10, v12, v13\n"
      "v_cvt_pk_bf16_f32 v11, v14, v15\n"
      "ds_write_b128 v240, v[8:11] offset:1024\n"
      "v_pk_mul_f32 v[16:17], v[16:17], v[222:223] op_sel:[0,0] op_sel_hi:[1,0]\n"
      "v_pk_mul_f32 v[18:19], v[18:19], v[222:223] op_sel:[0,0] op_sel_hi:[1,0]\n"
      "v_pk_mul_f32 v[20:21], v[20:21], v[222:223] op_sel:[0,0] op_sel_hi:[1,0]\n"
      "v_pk_mul_f32 v[22:23], v[22:23], v[222:223] op_sel:[0,0] op_sel_hi:[1,0]\n"
      "v_cvt_pk_bf16_f32 v16, v16, v17\n"
      "v_cvt_pk_bf16_f32 v17, v18, v19\n"
      "v_cvt_pk_bf16_f32 v18, v20, v21\n"
      "v_cvt_pk_bf16_f32 v19, v22, v23\n"
      "ds_write_b128 v240, v[16:19] offset:2048\n"
      "v_pk_mul_f32 v[24:25], v[24:25], v[222:223] op_sel:[0,0] op_sel_hi:[1,0]\n"
      "v_pk_mul_f32 v[26:27], v[26:27], v[222:223] op_sel:[0,0] op_sel_hi:[1,0]\n"
      "v_pk_mul_f32 v[28:29], v[28:29], v[222:223] op_sel:[0,0] op_sel_hi:[1,0]\n"
      "v_pk_mul_f32 v[30:31], v[30:31], v[222:223] op_sel:[0,0] op_sel_hi:[1,0]\n"
      "v_cvt_pk_bf16_f32 v24, v24, v25\n"
      "v_cvt_pk_bf16_f32 v25, v26, v27\n"
      "v_cvt_pk_bf16_f32 v26, v28, v29\n"
      "v_cvt_pk_bf16_f32 v27, v30, v31\n"
      "ds_write_b128 v240, v[24:27] offset:3072\n"
      "v_pk_mul_f32 v[32:33], v[32:33], v[222:223] op_sel:[0,1] op_sel_hi:[1,1]\n"
      "v_pk_mul_f32 v[34:35], v[34:35], v[222:223] op_sel:[0,1] op_sel_hi:[1,1]\n"
      "v_pk_mul_f32 v[36:37], v[36:37], v[222:223] op_sel:[0,1] op_sel_hi:[1,1]\n"
      "v_pk_mul_f32 v[38:39], v[38:39], v[222:223] op_sel:[0,1] op_sel_hi:[1,1]\n"
      "v_cvt_pk_bf16_f32 v32, v32, v33\n"
      "v_cvt_pk_bf16_f32 v33, v34, v35\n"
      "v_cvt_pk_bf16_f32 v34, v36, v37\n"
      "v_cvt_pk_bf16_f32 v35, v38, v39\n"
      "ds_write_b128 v240, v[32:35] offset:4096\n"
      "v_pk_mul_f32 v[40:41], v[40:41], v[222:223] op_sel:[0,1] op_sel_hi:[1,1]\n"
      "v_pk_mul_f32 v[42:43], v[42:43], v[222:223] op_sel:[0,1] op_sel_hi:[1,1]\n"
      "v_pk_mul_f32 v[44:45], v[44:45], v[222:223] op_sel:[0,1] op_sel_hi:[1,1]\n"
      "v_pk_mul_f32 v[46:47], v[46:47], v[222:223] op_sel:[0,1] op_sel_hi:[1,1]\n"
      "v_cvt_pk_bf16_f32 v40, v40, v41\n"
      "v_cvt_pk_bf16_f32 v41, v42, v43\n"
      "v_cvt_pk_bf16_f32 v42, v44, v45\n"
      "v_cvt_pk_bf16_f32 v43, v46, v47\n"
      "ds_write_b128 v240, v[40:43] offset:5120\n"
      "v_pk_mul_f32 v[48:49], v[48:49], v[222:223] op_sel:[0,1] op_sel_hi:[1,1]\n"
      "v_pk_mul_f32 v[50:51], v[50:51], v[222:223] op_sel:[0,1] op_sel_hi:[1,1]\n"
      "v_pk_mul_f32 v[52:53], v[52:53], v[222:223] op_sel:[0,1] op_sel_hi:[1,1]\n"
      "v_pk_mul_f32 v[54:55], v[54:55], v[222:223] op_sel:[0,1] op_sel_hi:[1,1]\n"
      "v_cvt_pk_bf16_f32 v48, v48, v49\n"
      "v_cvt_pk_bf16_f32 v49, v50, v51\n"
      "v_cvt_pk_bf16_f32 v50, v52, v53\n"
      "v_cvt_pk_bf16_f32 v51, v54, v55\n"
      "ds_write_b128 v240, v[48:51] offset:6144\n"
      "v_pk_mul_f32 v[56:57], v[56:57], v[222:223] op_sel:[0,1] op_sel_hi:[1,1]\n"
      "v_pk_mul_f32 v[58:59], v[58:59], v[222:223] op_sel:[0,1] op_sel_hi:[1,1]\n"
      "v_pk_mul_f32 v[60:61], v[60:61], v[222:223] op_sel:[0,1] op_sel_hi:[1,1]\n"
      "v_pk_mul_f32 v[62:63], v[62:63], v[222:223] op_sel:[0,1] op_sel_hi:[1,1]\n"
      "v_cvt_pk_bf16_f32 v56, v56, v57\n"
      "v_cvt_pk_bf16_f32 v57, v58, v59\n"
      "v_cvt_pk_bf16_f32 v58, v60, v61\n"
      "v_cvt_pk_bf16_f32 v59, v62, v63\n"
      "ds_write_b128 v240, v[56:59] offset:7168\n"
      "v_pk_mul_f32 v[64:65], v[64:65], v[224:225] op_sel:[0,0] op_sel_hi:[1,0]\n"
      "v_pk_mul_f32 v[66:67], v[66:67], v[224:225] op_sel:[0,0] op_sel_hi:[1,0]\n"
      "v_pk_mul_f32 v[68:69], v[68:69], v[224:225] op_sel:[0,0] op_sel_hi:[1,0]\n"
      "v_pk_mul_f32 v[70:71], v[70:71], v[224:225] op_sel:[0,0] op_sel_hi:[1,0]\n"
      "v_cvt_pk_bf16_f32 v64, v64, v65\n"
      "v_cvt_pk_bf16_f32 v65, v66, v67\n"
      "v_cvt_pk_bf16_f32 v66, v68, v69\n"
      "v_cvt_pk_bf16_f32 v67, v70, v71\n"
      "ds_write_b128 v240, v[64:67] offset:32768\n"
      "v_pk_mul_f32 v[72:73], v[72:73], v[224:225] op_sel:[0,0] op_sel_hi:[1,0]\n"
      "v_pk_mul_f32 v[74:75], v[74:75], v[224:225] op_sel:[0,0] op_sel_hi:[1,0]\n"
      "v_pk_mul_f32 v[76:77], v[76:77], v[224:225] op_sel:[0,0] op_sel_hi:[1,0]\n"
      "v_pk_mul_f32 v[78:79], v[78:79], v[224:225] op_sel:[0,0] op_sel_hi:[1,0]\n"
      "v_cvt_pk_bf16_f32 v72, v72, v73\n"
      "v_cvt_pk_bf16_f32 v73, v74, v75\n"
      "v_cvt_pk_bf16_f32 v74, v76, v77\n"
      "v_cvt_pk_bf16_f32 v75, v78, v79\n"
      "ds_write_b128 v240, v[72:75] offset:33792\n"
      "v_pk_mul_f32 v[80:81], v[80:81], v[224:225] op_sel:[0,0] op_sel_hi:[1,0]\n"
      "v_pk_mul_f32 v[82:83], v[82:83], v[224:225] op_sel:[0,0] op_sel_hi:[1,0]\n"
      "v_pk_mul_f32 v[84:85], v[84:85], v[224:225] op_sel:[0,0] op_sel_hi:[1,0]\n"
      "v_pk_mul_f32 v[86:87], v[86:87], v[224:225] op_sel:[0,0] op_sel_hi:[1,0]\n"
      "v_cvt_pk_bf16_f32 v80, v80, v81\n"
      "v_cvt_pk_bf16_f32 v81, v82, v83\n"
      "v_cvt_pk_bf16_f32 v82, v84, v85\n"
      "v_cvt_pk_bf16_f32 v83, v86, v87\n"
      "ds_write_b128 v240, v[80:83] offset:34816\n"
      "v_pk_mul_f32 v[88:89], v[88:89], v[224:225] op_sel:[0,0] op_sel_hi:[1,0]\n"
      "v_pk_mul_f32 v[90:91], v[90:91], v[224:225] op_sel:[0,0] op_sel_hi:[1,0]\n"
      "v_pk_mul_f32 v[92:93], v[92:93], v[224:225] op_sel:[0,0] op_sel_hi:[1,0]\n"
      "v_pk_mul_f32 v[94:95], v[94:95], v[224:225] op_sel:[0,0] op_sel_hi:[1,0]\n"
      "v_cvt_pk_bf16_f32 v88, v88, v89\n"
      "v_cvt_pk_bf16_f32 v89, v90, v91\n"
      "v_cvt_pk_bf16_f32 v90, v92, v93\n"
      "v_cvt_pk_bf16_f32 v91, v94, v95\n"
      "ds_write_b128 v240, v[88:91] offset:35840\n"
      "v_pk_mul_f32 v[96:97], v[96:97], v[224:225] op_sel:[0,1] op_sel_hi:[1,1]\n"
      "v_pk_mul_f32 v[98:99], v[98:99], v[224:225] op_sel:[0,1] op_sel_hi:[1,1]\n"
      "v_pk_mul_f32 v[100:101], v[100:101], v[224:225] op_sel:[0,1] op_sel_hi:[1,1]\n"
      "v_pk_mul_f32 v[102:103], v[102:103], v[224:225] op_sel:[0,1] op_sel_hi:[1,1]\n"
      "v_cvt_pk_bf16_f32 v96, v96, v97\n"
      "v_cvt_pk_bf16_f32 v97, v98, v99\n"
      "v_cvt_pk_bf16_f32 v98, v100, v101\n"
      "v_cvt_pk_bf16_f32 v99, v102, v103\n"
      "ds_write_b128 v240, v[96:99] offset:36864\n"
      "v_pk_mul_f32 v[104:105], v[104:105], v[224:225] op_sel:[0,1] op_sel_hi:[1,1]\n"
      "v_pk_mul_f32 v[106:107], v[106:107], v[224:225] op_sel:[0,1] op_sel_hi:[1,1]\n"
      "v_pk_mul_f32 v[108:109], v[108:109], v[224:225] op_sel:[0,1] op_sel_hi:[1,1]\n"
      "v_pk_mul_f32 v[110:111], v[110:111], v[224:225] op_sel:[0,1] op_sel_hi:[1,1]\n"
      "v_cvt_pk_bf16_f32 v104, v104, v105\n"
      "v_cvt_pk_bf16_f32 v105, v106, v107\n"
      "v_cvt_pk_bf16_f32 v106, v108, v109\n"
      "v_cvt_pk_bf16_f32 v107, v110, v111\n"
      "ds_write_b128 v240, v[104:107] offset:37888\n"
      "v_pk_mul_f32 v[112:113], v[112:113], v[224:225] op_sel:[0,1] op_sel_hi:[1,1]\n"
      "v_pk_mul_f32 v[114:115], v[114:115], v[224:225] op_sel:[0,1] op_sel_hi:[1,1]\n"
      "v_pk_mul_f32 v[116:117], v[116:117], v[224:225] op_sel:[0,1] op_sel_hi:[1,1]\n"
      "v_pk_mul_f32 v[118:119], v[118:119], v[224:225] op_sel:[0,1] op_sel_hi:[1,1]\n"
      "v_cvt_pk_bf16_f32 v112, v112, v113\n"
      "v_cvt_pk_bf16_f32 v113, v114, v115\n"
      "v_cvt_pk_bf16_f32 v114, v116, v117\n"
      "v_cvt_pk_bf16_f32 v115, v118, v119\n"
      "ds_write_b128 v240, v[112:115] offset:38912\n"
      "v_pk_mul_f32 v[120:121], v[120:121], v[224:225] op_sel:[0,1] op_sel_hi:[1,1]\n"
      "v_pk_mul_f32 v[122:123], v[122:123], v[224:225] op_sel:[0,1] op_sel_hi:[1,1]\n"
      "v_pk_mul_f32 v[124:125], v[124:125], v[224:225] op_sel:[0,1] op_sel_hi:[1,1]\n"
      "v_pk_mul_f32 v[126:127], v[126:127], v[224:225] op_sel:[0,1] op_sel_hi:[1,1]\n"
      "v_cvt_pk_bf16_f32 v120, v120, v121\n"
      "v_cvt_pk_bf16_f32 v121, v122, v123\n"
      "v_cvt_pk_bf16_f32 v122, v124, v125\n"
      "v_cvt_pk_bf16_f32 v123, v126, v127\n"
      "ds_write_b128 v240, v[120:123] offset:39936\n"
      "v_pk_mul_f32 v[128:129], v[128:129], v[226:227] op_sel:[0,0] op_sel_hi:[1,0]\n"
      "v_pk_mul_f32 v[130:131], v[130:131], v[226:227] op_sel:[0,0] op_sel_hi:[1,0]\n"
      "v_pk_mul_f32 v[132:133], v[132:133], v[226:227] op_sel:[0,0] op_sel_hi:[1,0]\n"
      "v_pk_mul_f32 v[134:135], v[134:135], v[226:227] op_sel:[0,0] op_sel_hi:[1,0]\n"
      "v_cvt_pk_bf16_f32 v128, v128, v129\n"
      "v_cvt_pk_bf16_f32 v129, v130, v131\n"
      "v_cvt_pk_bf16_f32 v130, v132, v133\n"
      "v_cvt_pk_bf16_f32 v131, v134, v135\n"
      "ds_write_b128 v241, v[128:131] offset:0\n"
      "v_pk_mul_f32 v[136:137], v[136:137], v[226:227] op_sel:[0,0] op_sel_hi:[1,0]\n"
      "v_pk_mul_f32 v[138:139], v[138:139], v[226:227] op_sel:[0,0] op_sel_hi:[1,0]\n"
      "v_pk_mul_f32 v[140:141], v[140:141], v[226:227] op_sel:[0,0] op_sel_hi:[1,0]\n"
      "v_pk_mul_f32 v[142:143], v[142:143], v[226:227] op_sel:[0,0] op_sel_hi:[1,0]\n"
      "v_cvt_pk_bf16_f32 v136, v136, v137\n"
      "v_cvt_pk_bf16_f32 v137, v138, v139\n"
      "v_cvt_pk_bf16_f32 v138, v140, v141\n"
      "v_cvt_pk_bf16_f32 v139, v142, v143\n"
      "ds_write_b128 v241, v[136:139] offset:1024\n"
      "v_pk_mul_f32 v[144:145], v[144:145], v[226:227] op_sel:[0,0] op_sel_hi:[1,0]\n"
      "v_pk_mul_f32 v[146:147], v[146:147], v[226:227] op_sel:[0,0] op_sel_hi:[1,0]\n"
      "v_pk_mul_f32 v[148:149], v[148:149], v[226:227] op_sel:[0,0] op_sel_hi:[1,0]\n"
      "v_pk_mul_f32 v[150:151], v[150:151], v[226:227] op_sel:[0,0] op_sel_hi:[1,0]\n"
      "v_cvt_pk_bf16_f32 v144, v144, v145\n"
      "v_cvt_pk_bf16_f32 v145, v146, v147\n"
      "v_cvt_pk_bf16_f32 v146, v148, v149\n"
      "v_cvt_pk_bf16_f32 v147, v150, v151\n"
      "ds_write_b128 v241, v[144:147] offset:2048\n"
      "v_pk_mul_f32 v[152:153], v[152:153], v[226:227] op_sel:[0,0] op_sel_hi:[1,0]\n"
      "v_pk_mul_f32 v[154:155], v[154:155], v[226:227] op_sel:[0,0] op_sel_hi:[1,0]\n"
      "v_pk_mul_f32 v[156:157], v[156:157], v[226:227] op_sel:[0,0] op_sel_hi:[1,0]\n"
      "v_pk_mul_f32 v[158:159], v[158:159], v[226:227] op_sel:[0,0] op_sel_hi:[1,0]\n"
      "v_cvt_pk_bf16_f32 v152, v152, v153\n"
      "v_cvt_pk_bf16_f32 v153, v154, v155\n"
      "v_cvt_pk_bf16_f32 v154, v156, v157\n"
      "v_cvt_pk_bf16_f32 v155, v158, v159\n"
      "ds_write_b128 v241, v[152:155] offset:3072\n"
      "v_pk_mul_f32 v[160:161], v[160:161], v[226:227] op_sel:[0,1] op_sel_hi:[1,1]\n"
      "v_pk_mul_f32 v[162:163], v[162:163], v[226:227] op_sel:[0,1] op_sel_hi:[1,1]\n"
      "v_pk_mul_f32 v[164:165], v[164:165], v[226:227] op_sel:[0,1] op_sel_hi:[1,1]\n"
      "v_pk_mul_f32 v[166:167], v[166:167], v[226:227] op_sel:[0,1] op_sel_hi:[1,1]\n"
      "v_cvt_pk_bf16_f32 v160, v160, v161\n"
      "v_cvt_pk_bf16_f32 v161, v162, v163\n"
      "v_cvt_pk_bf16_f32 v162, v164, v165\n"
      "v_cvt_pk_bf16_f32 v163, v166, v167\n"
      "ds_write_b128 v241, v[160:163] offset:4096\n"
      "v_pk_mul_f32 v[168:169], v[168:169], v[226:227] op_sel:[0,1] op_sel_hi:[1,1]\n"
      "v_pk_mul_f32 v[170:171], v[170:171], v[226:227] op_sel:[0,1] op_sel_hi:[1,1]\n"
      "v_pk_mul_f32 v[172:173], v[172:173], v[226:227] op_sel:[0,1] op_sel_hi:[1,1]\n"
      "v_pk_mul_f32 v[174:175], v[174:175], v[226:227] op_sel:[0,1] op_sel_hi:[1,1]\n"
      "v_cvt_pk_bf16_f32 v168, v168, v169\n"
      "v_cvt_pk_bf16_f32 v169, v170, v171\n"
      "v_cvt_pk_bf16_f32 v170, v172, v173\n"
      "v_cvt_pk_bf16_f32 v171, v174, v175\n"
      "ds_write_b128 v241, v[168:171] offset:5120\n"
      "v_pk_mul_f32 v[176:177], v[176:177], v[226:227] op_sel:[0,1] op_sel_hi:[1,1]\n"
      "v_pk_mul_f32 v[178:179], v[178:179], v[226:227] op_sel:[0,1] op_sel_hi:[1,1]\n"
      "v_pk_mul_f32 v[180:181], v[180:181], v[226:227] op_sel:[0,1] op_sel_hi:[1,1]\n"
      "v_pk_mul_f32 v[182:183], v[182:183], v[226:227] op_sel:[0,1] op_sel_hi:[1,1]\n"
      "v_cvt_pk_bf16_f32 v176, v176, v177\n"
      "v_cvt_pk_bf16_f32 v177, v178, v179\n"
      "v_cvt_pk_bf16_f32 v178, v180, v181\n"
      "v_cvt_pk_bf16_f32 v179, v182, v183\n"
      "ds_write_b128 v241, v[176:179] offset:6144\n"
      "v_pk_mul_f32 v[184:185], v[184:185], v[226:227] op_sel:[0,1] op_sel_hi:[1,1]\n"
      "v_pk_mul_f32 v[186:187], v[186:187], v[226:227] op_sel:[0,1] op_sel_hi:[1,1]\n"
      "v_pk_mul_f32 v[188:189], v[188:189], v[226:227] op_sel:[0,1] op_sel_hi:[1,1]\n"
      "v_pk_mul_f32 v[190:191], v[190:191], v[226:227] op_sel:[0,1] op_sel_hi:[1,1]\n"
      "v_cvt_pk_bf16_f32 v184, v184, v185\n"
      "v_cvt_pk_bf16_f32 v185, v186, v187\n"
      "v_cvt_pk_bf16_f32 v186, v188, v189\n"
      "v_cvt_pk_bf16_f32 v187, v190, v191\n"
      "ds_write_b128 v241, v[184:187] offset:7168\n"
      "s_cmp_eq_u32 %8, 0\n"
      "s_cselect_b64 vcc, s[94:95], 0\n"
      "s_nop 0\n"
      "s_cbranch_vccnz HSLOOP_%=\n"
      "HSDONE_%=:\n"
      "s_waitcnt vmcnt(8) lgkmcnt(0)\n"
      "s_barrier\n"
      "s_lshl_b32 s74, s87, 13\n"
      "s_add_u32 s74, s74, 32768\n"
      "s_add_u32 s87, s87, 1\n"
      "s_and_b32 s87, s87, 3\n"
      "v_add_u32 v240, s74, v242\n"
      "v_add_u32 v241, 65536, v240\n"
      "ds_read_b64_tr_b16 v[176:177], v245 offset:32768\n"
      "ds_read_b64_tr_b16 v[178:179], v245 offset:36864\n"
      "ds_read_b64_tr_b16 v[180:181], v246 offset:32768\n"
      "ds_read_b64_tr_b16 v[182:183], v246 offset:36864\n"
      "ds_read_b128 a[0:3], v240 offset:0\n"
      "ds_read_b128 a[16:19], v240 offset:4096\n"
      "ds_read_b128 a[32:35], v240 offset:32768\n"
      "ds_read_b128 a[48:51], v240 offset:36864\n"
      "ds_read_b128 a[64:67], v241 offset:0\n"
      "ds_read_b128 a[80:83], v241 offset:4096\n"
      "ds_read_b64_tr_b16 v[184:185], v245 offset:40960\n"
      "ds_read_b64_tr_b16 v[186:187], v245 offset:45056\n"
      "ds_read_b64_tr_b16 v[188:189], v246 offset:40960\n"
      "ds_read_b64_tr_b16 v[190:191], v246 offset:45056\n"
      "ds_read_b128 a[4:7], v240 offset:1024\n"
      "ds_read_b128 a[20:23], v240 offset:5120\n"
      "ds_read_b128 a[36:39], v240 offset:33792\n"
      "ds_read_b128 a[52:55], v240 offset:37888\n"
      "ds_read_b128 a[68:71], v241 offset:1024\n"
      "ds_read_b128 a[84:87], v241 offset:5120\n"
      "s_waitcnt lgkmcnt(14)\n"
      "v_mfma_f32_16x16x32_bf16 v[128:131], v[176:179], a[0:3], 0\n"
      "ds_read_b128 a[8:11], v240 offset:2048\n"
      "ds_read_b128 a[24:27], v240 offset:6144\n"
      "v_mfma_f32_16x16x32_bf16 v[136:139], v[176:179], a[16:19], 0\n"
      "s_waitcnt lgkmcnt(14)\n"
      "ds_read_b64_tr_b16 v[192:193], v245 offset:49152\n"
      "ds_read_b128 a[40:43], v240 offset:34816\n"
      "v_mfma_f32_16x16x32_bf16 v[144:147], v[176:179], a[32:35], 0\n"
      "ds_read_b128 a[56:59], v240 offset:38912\n"
      "ds_read_b128 a[72:75], v241 offset:2048\n"
      "v_mfma_f32_16x16x32_bf16 v[152:155], v[176:179], a[48:51], 0\n"
      "s_waitcnt lgkmcnt(14)\n"
      "ds_read_b64_tr_b16 v[194:195], v245 offset:53248\n"
      "ds_read_b128 a[88:91], v241 offset:6144\n"
      "v_mfma_f32_16x16x32_bf16 v[160:163], v[176:179], a[64:67], 0\n"
      "ds_read_b128 a[12:15], v240 offset:3072\n"
      "ds_read_b128 a[28:31], v240 offset:7168\n"
      "v_mfma_f32_16x16x32_bf16 v[168:171], v[176:179], a[80:83], 0\n"
      "ds_read_b128 a[44:47], v240 offset:35840\n"
      "ds_read_b128 a[60:63], v240 offset:39936\n"
      "v_mfma_f32_16x16x32_bf16 v[132:135], v[180:183], a[0:3], 0\n"
      "ds_read_b64_tr_b16 v[196:197], v246 offset:49152\n"
      "ds_read_b128 a[76:79], v241 offset:3072\n"
      "v_mfma_f32_16x16x32_bf16 v[140:143], v[180:183], a[16:19], 0\n"
      "ds_read_b128 a[92:95], v241 offset:7168\n"
      "s_lshl_b32 s74, s87, 13\n"
      "s_add_u32 s74, s74, 32768\n"
      "v_mfma_f32_16x16x32_bf16 v[148:151], v[180:183], a[32:35], 0\n"
      "s_add_u32 s87, s87, 1\n"
      "s_and_b32 s87, s87, 3\n"
      "ds_read_b64_tr_b16 v[198:199], v246 offset:53248\n"
      "v_mfma_f32_16x16x32_bf16 v[156:159], v[180:183], a[48:51], 0\n"
      "v_add_u32 v240, s74, v242\n"
      "v_add_u32 v241, 65536, v240\n"
      "v_mfma_f32_16x16x32_bf16 v[164:167], v[180:183], a[64:67], 0\n"
      "ds_read_b128 a[96:99], v240 offset:0\n"
      "ds_read_b128 a[112:115], v240 offset:4096\n"
      "v_mfma_f32_16x16x32_bf16 v[172:175], v[180:183], a[80:83], 0\n"
      "s_waitcnt lgkmcnt(14)\n"
      "ds_read_b128 a[128:131], v240 offset:32768\n"
      "ds_read_b128 a[144:147], v240 offset:36864\n"
      "v_mfma_f32_16x16x32_bf16 v[128:131], v[184:187], a[4:7], v[128:131]\n"
      "ds_read_b64_tr_b16 v[200:201], v245 offset:57344\n"
      "ds_read_b128 a[160:163], v241 offset:0\n"
      "v_mfma_f32_16x16x32_bf16 v[136:139], v[184:187], a[20:23], v[136:139]\n"
      "ds_read_b128 a[176:179], v241 offset:4096\n"
      "ds_read_b128 a[100:103], v240 offset:1024\n"
      "v_mfma_f32_16x16x32_bf16 v[144:147], v[184:187], a[36:39], v[144:147]\n"
      "ds_read_b64_tr_b16 v[202:203], v245 offset:61440\n"
      "ds_read_b128 a[116:119], v240 offset:5120\n"
      "v_mfma_f32_16x16x32_bf16 v[152:155], v[184:187], a[52:55], v[152:155]\n"
      "ds_read_b64_tr_b16 v[204:205], v246 offset:57344\n"
      "ds_read_b128 a[132:135], v240 offset:33792\n"
      "v_mfma_f32_16x16x32_bf16 v[160:163], v[184:187], a[68:71], v[160:163]\n"
      "ds_read_b128 a[148:151], v240 offset:37888\n"
      "ds_read_b128 a[164:167], v241 offset:1024\n"
      "v_mfma_f32_16x16x32_bf16 v[168:171], v[184:187], a[84:87], v[168:171]\n"
      "ds_read_b64_tr_b16 v[206:207], v246 offset:61440\n"
      "ds_read_b128 a[180:183], v241 offset:5120\n"
      "v_mfma_f32_16x16x32_bf16 v[132:135], v[188:191], a[4:7], v[132:135]\n"
      "ds_read_b128 a[104:107], v240 offset:2048\n"
      "ds_read_b128 a[120:123], v240 offset:6144\n"
      "v_mfma_f32_16x16x32_bf16 v[140:143], v[188:191], a[20:23], v[140:143]\n"
      "ds_read_b128 a[136:139], v240 offset:34816\n"
      "ds_read_b128 a[152:155], v240 offset:38912\n"
      "v_mfma_f32_16x16x32_bf16 v[148:151], v[188:191], a[36:39], v[148:151]\n"
      "ds_read_b128 a[168:171], v241 offset:2048\n"
      "ds_read_b128 a[184:187], v241 offset:6144\n"
      "v_mfma_f32_16x16x32_bf16 v[156:159], v[188:191], a[52:55], v[156:159]\n"
      "ds_read_b128 a[108:111], v240 offset:3072\n"
      "ds_read_b128 a[124:127], v240 offset:7168\n"
      "v_mfma_f32_16x16x32_bf16 v[164:167], v[188:191], a[68:71], v[164:167]\n"
      "ds_read_b128 a[140:143], v240 offset:35840\n"
      "ds_read_b128 a[156:159], v240 offset:39936\n"
      "v_mfma_f32_16x16x32_bf16 v[172:175], v[188:191], a[84:87], v[172:175]\n"
      "s_waitcnt vmcnt(0) lgkmcnt(11)\n"
      "s_barrier\n"
      "ds_read_b128 a[172:175], v241 offset:3072\n"
      "v_mfma_f32_16x16x32_bf16 v[128:131], v[192:195], a[8:11], v[128:131]\n"
      "s_mul_i32 s84, s83, 655360\n"
      "s_lshl_b32 s73, s82, 8\n"
      "ds_read_b64_tr_b16 v[176:177], v243 offset:0\n"
      "v_mfma_f32_16x16x32_bf16 v[136:139], v[192:195], a[24:27], v[136:139]\n"
      "ds_read_b128 a[188:191], v241 offset:7168\n"
      "s_lshl_b32 s74, s87, 13\n"
      "s_add_u32 s74, s74, 32768\n"
      "v_mfma_f32_16x16x32_bf16 v[144:147], v[192:195], a[40:43], v[144:147]\n"
      "s_add_u32 s87, s87, 1\n"
      "s_and_b32 s87, s87, 3\n"
      "v_add_u32 v240, s74, v242\n"
      "v_mfma_f32_16x16x32_bf16 v[152:155], v[192:195], a[56:59], v[152:155]\n"
      "s_add_u32 m0, s72, 131072\n"
      "ds_read_b64_tr_b16 v[178:179], v243 offset:4096\n"
      "v_add_u32 v241, 65536, v240\n"
      "v_mfma_f32_16x16x32_bf16 v[160:163], v[192:195], a[72:75], v[160:163]\n"
      "s_add_u32 s84, s84, s73\n"
      "ds_read_b128 a[192:195], v240 offset:0\n"
      "ds_read_b128 a[208:211], v240 offset:4096\n"
      "v_mfma_f32_16x16x32_bf16 v[168:171], v[192:195], a[88:91], v[168:171]\n"
      "ds_read_b64_tr_b16 v[180:181], v244 offset:0\n"
      "ds_read_b128 a[224:227], v240 offset:32768\n"
      "buffer_load_dwordx4 v247, s[52:55], s84 offen offset:0  lds\n"
      "v_mfma_f32_16x16x32_bf16 v[132:135], v[196:199], a[8:11], v[132:135]\n"
      "ds_read_b128 a[240:243], v240 offset:36864\n"
      "ds_read_b128 v[0:3], v241 offset:0\n"
      "buffer_load_dwordx4 v248, s[52:55], s84 offen offset:1024  lds\n"
      "v_mfma_f32_16x16x32_bf16 v[140:143], v[196:199], a[24:27], v[140:143]\n"
      "ds_read_b128 v[16:19], v241 offset:4096\n"
      "ds_read_b128 a[196:199], v240 offset:1024\n"
      "buffer_load_dwordx4 v249, s[52:55], s84 offen offset:2048  lds\n"
      "v_mfma_f32_16x16x32_bf16 v[148:151], v[196:199], a[40:43], v[148:151]\n"
      "ds_read_b64_tr_b16 v[182:183], v244 offset:4096\n"
      "ds_read_b128 a[212:215], v240 offset:5120\n"
      "buffer_load_dwordx4 v250, s[52:55], s84 offen offset:3072  lds\n"
      "v_mfma_f32_16x16x32_bf16 v[156:159], v[196:199], a[56:59], v[156:159]\n"
      "ds_read_b128 a[228:231], v240 offset:33792\n"
      "ds_read_b128 a[244:247], v240 offset:37888\n"
      "v_mfma_f32_16x16x32_bf16 v[164:167], v[196:199], a[72:75], v[164:167]\n"
      "s_add_u32 m0, s72, 135168\n"
      "ds_read_b128 v[4:7], v241 offset:1024\n"
      "ds_read_b128 v[20:23], v241 offset:5120\n"
      "v_mfma_f32_16x16x32_bf16 v[172:175], v[196:199], a[88:91], v[172:175]\n"
      "s_add_u32 s84, s84, 20480\n"
      "ds_read_b128 a[200:203], v240 offset:2048\n"
      "ds_read_b128 a[216:219], v240 offset:6144\n"
      "v_mfma_f32_16x16x32_bf16 v[128:131], v[200:203], a[12:15], v[128:131]\n"
      "ds_read_b128 a[232:235], v240 offset:34816\n"
      "ds_read_b128 a[248:251], v240 offset:38912\n"
      "buffer_load_dwordx4 v247, s[52:55], s84 offen offset:0  lds\n"
      "v_mfma_f32_16x16x32_bf16 v[136:139], v[200:203], a[28:31], v[136:139]\n"
      "ds_read_b64_tr_b16 v[184:185], v243 offset:8192\n"
      "ds_read_b128 v[8:11], v241 offset:2048\n"
      "buffer_load_dwordx4 v248, s[52:55], s84 offen offset:1024  lds\n"
      "v_mfma_f32_16x16x32_bf16 v[144:147], v[200:203], a[44:47], v[144:147]\n"
      "ds_read_b128 v[24:27], v241 offset:6144\n"
      "ds_read_b128 a[204:207], v240 offset:3072\n"
      "buffer_load_dwordx4 v249, s[52:55], s84 offen offset:2048  lds\n"
      "v_mfma_f32_16x16x32_bf16 v[152:155], v[200:203], a[60:63], v[152:155]\n"
      "ds_read_b64_tr_b16 v[186:187], v243 offset:12288\n"
      "ds_read_b128 a[220:223], v240 offset:7168\n"
      "buffer_load_dwordx4 v250, s[52:55], s84 offen offset:3072  lds\n"
      "s_add_u32 s83, s83, 1\n"
      "v_mfma_f32_16x16x32_bf16 v[160:163], v[200:203], a[76:79], v[160:163]\n"
      "s_and_b32 s83, s83, 3\n"
      "ds_read_b128 a[236:239], v240 offset:35840\n"
      "ds_read_b128 a[252:255], v240 offset:39936\n"
      "v_mfma_f32_16x16x32_bf16 v[168:171], v[200:203], a[92:95], v[168:171]\n"
      "ds_read_b128 v[12:15], v241 offset:3072\n"
      "ds_read_b128 v[28:31], v241 offset:7168\n"
      "s_lshl_b32 s74, s87, 13\n"
      "v_mfma_f32_16x16x32_bf16 v[132:135], v[204:207], a[12:15], v[132:135]\n"
      "s_add_u32 s74, s74, 32768\n"
      "s_add_u32 s87, s87, 1\n"
      "ds_read_b64_tr_b16 v[188:189], v244 offset:8192\n"
      "v_mfma_f32_16x16x32_bf16 v[140:143], v[204:207], a[28:31], v[140:143]\n"
      "s_and_b32 s87, s87, 3\n"
      "v_add_u32 v240, s74, v242\n"
      "v_add_u32 v241, 65536, v240\n"
      "v_mfma_f32_16x16x32_bf16 v[148:151], v[204:207], a[44:47], v[148:151]\n"
      "ds_read_b64_tr_b16 v[190:191], v244 offset:12288\n"
      "ds_read_b128 v[32:35], v240 offset:0\n"
      "v_mfma_f32_16x16x32_bf16 v[156:159], v[204:207], a[60:63], v[156:159]\n"
      "ds_read_b128 v[48:51], v240 offset:4096\n"
      "ds_read_b128 v[64:67], v240 offset:32768\n"
      "v_mfma_f32_16x16x32_bf16 v[164:167], v[204:207], a[76:79], v[164:167]\n"
      "ds_read_b128 v[80:83], v240 offset:36864\n"
      "ds_read_b128 v[96:99], v241 offset:0\n"
      "v_mfma_f32_16x16x32_bf16 v[172:175], v[204:207], a[92:95], v[172:175]\n"
      "s_waitcnt lgkmcnt(14)\n"
      "ds_read_b128 v[112:115], v241 offset:4096\n"
      "ds_read_b128 v[36:39], v240 offset:1024\n"
      "v_mfma_f32_16x16x32_bf16 v[128:131], v[176:179], a[96:99], v[128:131]\n"
      "ds_read_b128 v[52:55], v240 offset:5120\n"
      "ds_read_b128 v[68:71], v240 offset:33792\n"
      "v_mfma_f32_16x16x32_bf16 v[136:139], v[176:179], a[112:115], v[136:139]\n"
      "ds_read_b64_tr_b16 v[192:193], v243 offset:16384\n"
      "ds_read_b128 v[84:87], v240 offset:37888\n"
      "v_mfma_f32_16x16x32_bf16 v[144:147], v[176:179], a[128:131], v[144:147]\n"
      "ds_read_b128 v[100:103], v241 offset:1024\n"
      "ds_read_b128 v[116:119], v241 offset:5120\n"
      "v_mfma_f32_16x16x32_bf16 v[152:155], v[176:179], a[144:147], v[152:155]\n"
      "ds_read_b64_tr_b16 v[194:195], v243 offset:20480\n"
      "ds_read_b128 v[40:43], v240 offset:2048\n"
      "v_mfma_f32_16x16x32_bf16 v[160:163], v[176:179], a[160:163], v[160:163]\n"
      "ds_read_b128 v[56:59], v240 offset:6144\n"
      "ds_read_b128 v[72:75], v240 offset:34816\n"
      "v_mfma_f32_16x16x32_bf16 v[168:171], v[176:179], a[176:179], v[168:171]\n"
      "ds_read_b128 v[88:91], v240 offset:38912\n"
      "v_mfma_f32_16x16x32_bf16 v[132:135], v[180:183], a[96:99], v[132:135]\n"
      "ds_read_b64_tr_b16 v[196:197], v244 offset:16384\n"
      "v_mfma_f32_16x16x32_bf16 v[140:143], v[180:183], a[112:115], v[140:143]\n"
      "ds_read_b128 v[104:107], v241 offset:2048\n"
      "v_mfma_f32_16x16x32_bf16 v[148:151], v[180:183], a[128:131], v[148:151]\n"
      "ds_read_b64_tr_b16 v[198:199], v244 offset:20480\n"
      "v_mfma_f32_16x16x32_bf16 v[156:159], v[180:183], a[144:147], v[156:159]\n"
      "ds_read_b128 v[120:123], v241 offset:6144\n"
      "v_mfma_f32_16x16x32_bf16 v[164:167], v[180:183], a[160:163], v[164:167]\n"
      "ds_read_b128 v[44:47], v240 offset:3072\n"
      "v_mfma_f32_16x16x32_bf16 v[172:175], v[180:183], a[176:179], v[172:175]\n"
      "s_waitcnt lgkmcnt(14)\n"
      "ds_read_b128 v[60:63], v240 offset:7168\n"
      "v_mfma_f32_16x16x32_bf16 v[128:131], v[184:187], a[100:103], v[128:131]\n"
      "ds_read_b64_tr_b16 v[200:201], v243 offset:24576\n"
      "v_mfma_f32_16x16x32_bf16 v[136:139], v[184:187], a[116:119], v[136:139]\n"
      "ds_read_b128 v[76:79], v240 offset:35840\n"
      "v_mfma_f32_16x16x32_bf16 v[144:147], v[184:187], a[132:135], v[144:147]\n"
      "ds_read_b64_tr_b16 v[202:203], v243 offset:28672\n"
      "v_mfma_f32_16x16x32_bf16 v[152:155], v[184:187], a[148:151], v[152:155]\n"
      "ds_read_b64_tr_b16 v[204:205], v244 offset:24576\n"
      "v_mfma_f32_16x16x32_bf16 v[160:163], v[184:187], a[164:167], v[160:163]\n"
      "ds_read_b128 v[92:95], v240 offset:39936\n"
      "v_mfma_f32_16x16x32_bf16 v[168:171], v[184:187], a[180:183], v[168:171]\n"
      "ds_read_b64_tr_b16 v[206:207], v244 offset:28672\n"
      "v_mfma_f32_16x16x32_bf16 v[132:135], v[188:191], a[100:103], v[132:135]\n"
      "ds_read_b128 v[108:111], v241 offset:3072\n"
      "v_mfma_f32_16x16x32_bf16 v[140:143], v[188:191], a[116:119], v[140:143]\n"
      "ds_read_b128 v[124:127], v241 offset:7168\n"
      "v_mfma_f32_16x16x32_bf16 v[148:151], v[188:191], a[132:135], v[148:151]\n"
      "v_mfma_f32_16x16x32_bf16 v[156:159], v[188:191], a[148:151], v[156:159]\n"
      "v_mfma_f32_16x16x32_bf16 v[164:167], v[188:191], a[164:167], v[164:167]\n"
      "v_mfma_f32_16x16x32_bf16 v[172:175], v[188:191], a[180:183], v[172:175]\n"
      "s_waitcnt vmcnt(0) lgkmcnt(0)\n"
      "s_barrier\n"
      "s_mul_i32 s84, s83, 655360\n"
      "v_mfma_f32_16x16x32_bf16 v[128:131], v[192:195], a[104:107], v[128:131]\n"
      "s_lshl_b32 s73, s82, 8\n"
      "ds_read_b64_tr_b16 v[176:177], v245 offset:32768\n"
      "v_mfma_f32_16x16x32_bf16 v[136:139], v[192:195], a[120:123], v[136:139]\n"
      "s_add_u32 m0, s72, 32768\n"
      "v_mfma_f32_16x16x32_bf16 v[144:147], v[192:195], a[136:139], v[144:147]\n"
      "s_add_u32 s84, s84, s73\n"
      "buffer_load_dwordx4 v247, s[52:55], s84 offen offset:0  lds\n"
      "v_mfma_f32_16x16x32_bf16 v[152:155], v[192:195], a[152:155], v[152:155]\n"
      "ds_read_b64_tr_b16 v[178:179], v245 offset:36864\n"
      "v_mfma_f32_16x16x32_bf16 v[160:163], v[192:195], a[168:171], v[160:163]\n"
      "buffer_load_dwordx4 v248, s[52:55], s84 offen offset:1024  lds\n"
      "v_mfma_f32_16x16x32_bf16 v[168:171], v[192:195], a[184:187], v[168:171]\n"
      "ds_read_b64_tr_b16 v[180:181], v246 offset:32768\n"
      "v_mfma_f32_16x16x32_bf16 v[132:135], v[196:199], a[104:107], v[132:135]\n"
      "buffer_load_dwordx4 v249, s[52:55], s84 offen offset:2048  lds\n"
      "v_mfma_f32_16x16x32_bf16 v[140:143], v[196:199], a[120:123], v[140:143]\n"
      "buffer_load_dwordx4 v250, s[52:55], s84 offen offset:3072  lds\n"
      "v_mfma_f32_16x16x32_bf16 v[148:151], v[196:199], a[136:139], v[148:151]\n"
      "s_add_u32 m0, s72, 36864\n"
      "ds_read_b64_tr_b16 v[182:183], v246 offset:36864\n"
      "v_mfma_f32_16x16x32_bf16 v[156:159], v[196:199], a[152:155], v[156:159]\n"
      "s_add_u32 s84, s84, 20480\n"
      "v_mfma_f32_16x16x32_bf16 v[164:167], v[196:199], a[168:171], v[164:167]\n"
      "buffer_load_dwordx4 v247, s[52:55], s84 offen offset:0  lds\n"
      "v_mfma_f32_16x16x32_bf16 v[172:175], v[196:199], a[184:187], v[172:175]\n"
      "buffer_load_dwordx4 v248, s[52:55], s84 offen offset:1024  lds\n"
      "v_mfma_f32_16x16x32_bf16 v[128:131], v[200:203], a[108:111], v[128:131]\n"
      "buffer_load_dwordx4 v249, s[52:55], s84 offen offset:2048  lds\n"
      "v_mfma_f32_16x16x32_bf16 v[136:139], v[200:203], a[124:127], v[136:139]\n"
      "ds_read_b64_tr_b16 v[184:185], v245 offset:40960\n"
      "v_mfma_f32_16x16x32_bf16 v[144:147], v[200:203], a[140:143], v[144:147]\n"
      "buffer_load_dwordx4 v250, s[52:55], s84 offen offset:3072  lds\n"
      "s_add_u32 s83, s83, 1\n"
      "v_mfma_f32_16x16x32_bf16 v[152:155], v[200:203], a[156:159], v[152:155]\n"
      "s_and_b32 s83, s83, 3\n"
      "ds_read_b64_tr_b16 v[186:187], v245 offset:45056\n"
      "v_mfma_f32_16x16x32_bf16 v[160:163], v[200:203], a[172:175], v[160:163]\n"
      "s_add_u32 s82, s82, 1\n"
      "v_mfma_f32_16x16x32_bf16 v[168:171], v[200:203], a[188:191], v[168:171]\n"
      "v_mfma_f32_16x16x32_bf16 v[132:135], v[204:207], a[108:111], v[132:135]\n"
      "s_cmp_eq_u32 s82, 5\n"
      "s_cselect_b32 s82, 0, s82\n"
      "ds_read_b64_tr_b16 v[188:189], v246 offset:40960\n"
      "v_mfma_f32_16x16x32_bf16 v[140:143], v[204:207], a[124:127], v[140:143]\n"
      "s_mul_i32 s84, s83, 655360\n"
      "v_mfma_f32_16x16x32_bf16 v[148:151], v[204:207], a[140:143], v[148:151]\n"
      "s_lshl_b32 s73, s82, 8\n"
      "ds_read_b64_tr_b16 v[190:191], v246 offset:45056\n"
      "v_mfma_f32_16x16x32_bf16 v[156:159], v[204:207], a[156:159], v[156:159]\n"
      "s_add_u32 m0, s72, 98304\n"
      "v_mfma_f32_16x16x32_bf16 v[164:167], v[204:207], a[172:175], v[164:167]\n"
      "s_add_u32 s84, s84, s73\n"
      "buffer_load_dwordx4 v247, s[52:55], s84 offen offset:0  lds\n"
      "v_mfma_f32_16x16x32_bf16 v[172:175], v[204:207], a[188:191], v[172:175]\n"
      "s_waitcnt lgkmcnt(6)\n"
      "buffer_load_dwordx4 v248, s[52:55], s84 offen offset:1024  lds\n"
      "v_mfma_f32_16x16x32_bf16 v[128:131], v[176:179], a[192:195], v[128:131]\n"
      "buffer_load_dwordx4 v249, s[52:55], s84 offen offset:2048  lds\n"
      "v_mfma_f32_16x16x32_bf16 v[136:139], v[176:179], a[208:211], v[136:139]\n"
      "ds_read_b64_tr_b16 v[192:193], v245 offset:49152\n"
      "v_mfma_f32_16x16x32_bf16 v[144:147], v[176:179], a[224:227], v[144:147]\n"
      "buffer_load_dwordx4 v250, s[52:55], s84 offen offset:3072  lds\n"
      "v_mfma_f32_16x16x32_bf16 v[152:155], v[176:179], a[240:243], v[152:155]\n"
      "s_add_u32 m0, s72, 102400\n"
      "ds_read_b64_tr_b16 v[194:195], v245 offset:53248\n"
      "v_mfma_f32_16x16x32_bf16 v[160:163], v[176:179], v[0:3], v[160:163]\n"
      "s_add_u32 s84, s84, 20480\n"
      "v_mfma_f32_16x16x32_bf16 v[168:171], v[176:179], v[16:19], v[168:171]\n"
      "s_waitcnt lgkmcnt(6)\n"
      "buffer_load_dwordx4 v247, s[52:55], s84 offen offset:0  lds\n"
      "v_mfma_f32_16x16x32_bf16 v[132:135], v[180:183], a[192:195], v[132:135]\n"
      "ds_read_b64_tr_b16 v[196:197], v246 offset:49152\n"
      "v_mfma_f32_16x16x32_bf16 v[140:143], v[180:183], a[208:211], v[140:143]\n"
      "buffer_load_dwordx4 v248, s[52:55], s84 offen offset:1024  lds\n"
      "v_mfma_f32_16x16x32_bf16 v[148:151], v[180:183], a[224:227], v[148:151]\n"
      "ds_read_b64_tr_b16 v[198:199], v246 offset:53248\n"
      "v_mfma_f32_16x16x32_bf16 v[156:159], v[180:183], a[240:243], v[156:159]\n"
      "buffer_load_dwordx4 v249, s[52:55], s84 offen offset:2048  lds\n"
      "v_mfma_f32_16x16x32_bf16 v[164:167], v[180:183], v[0:3], v[164:167]\n"
      "buffer_load_dwordx4 v250, s[52:55], s84 offen offset:3072  lds\n"
      "s_add_u32 s83, s83, 1\n"
      "v_mfma_f32_16x16x32_bf16 v[172:175], v[180:183], v[16:19], v[172:175]\n"
      "s_waitcnt lgkmcnt(6)\n"
      "s_and_b32 s83, s83, 3\n"
      "v_mfma_f32_16x16x32_bf16 v[128:131], v[184:187], a[196:199], v[128:131]\n"
      "ds_read_b64_tr_b16 v[200:201], v245 offset:57344\n"
      "v_mfma_f32_16x16x32_bf16 v[136:139], v[184:187], a[212:215], v[136:139]\n"
      "v_mfma_f32_16x16x32_bf16 v[144:147], v[184:187], a[228:231], v[144:147]\n"
      "ds_read_b64_tr_b16 v[202:203], v245 offset:61440\n"
      "v_mfma_f32_16x16x32_bf16 v[152:155], v[184:187], a[244:247], v[152:155]\n"
      "ds_read_b64_tr_b16 v[204:205], v246 offset:57344\n"
      "v_mfma_f32_16x16x32_bf16 v[160:163], v[184:187], v[4:7], v[160:163]\n"
      "v_mfma_f32_16x16x32_bf16 v[168:171], v[184:187], v[20:23], v[168:171]\n"
      "s_waitcnt lgkmcnt(7)\n"
      "ds_read_b64_tr_b16 v[206:207], v246 offset:61440\n"
      "v_mfma_f32_16x16x32_bf16 v[132:135], v[188:191], a[196:199], v[132:135]\n"
      "v_mfma_f32_16x16x32_bf16 v[140:143], v[188:191], a[212:215], v[140:143]\n"
      "v_mfma_f32_16x16x32_bf16 v[148:151], v[188:191], a[228:231], v[148:151]\n"
      "v_mfma_f32_16x16x32_bf16 v[156:159], v[188:191], a[244:247], v[156:159]\n"
      "v_mfma_f32_16x16x32_bf16 v[164:167], v[188:191], v[4:7], v[164:167]\n"
      "v_mfma_f32_16x16x32_bf16 v[172:175], v[188:191], v[20:23], v[172:175]\n"
      "s_waitcnt vmcnt(8) lgkmcnt(0)\n"
      "s_barrier\n"
      "s_mul_i32 s84, s83, 655360\n"
      "v_mfma_f32_16x16x32_bf16 v[128:131], v[192:195], a[200:203], v[128:131]\n"
      "ds_read_b64_tr_b16 v[176:177], v243 offset:32768\n"
      "v_mfma_f32_16x16x32_bf16 v[136:139], v[192:195], a[216:219], v[136:139]\n"
      "s_lshl_b32 s73, s82, 8\n"
      "v_mfma_f32_16x16x32_bf16 v[144:147], v[192:195], a[232:235], v[144:147]\n"
      "s_add_u32 m0, s72, 0\n"
      "v_mfma_f32_16x16x32_bf16 v[152:155], v[192:195], a[248:251], v[152:155]\n"
      "ds_read_b64_tr_b16 v[178:179], v243 offset:36864\n"
      "v_mfma_f32_16x16x32_bf16 v[160:163], v[192:195], v[8:11], v[160:163]\n"
      "s_add_u32 s84, s84, s73\n"
      "v_mfma_f32_16x16x32_bf16 v[168:171], v[192:195], v[24:27], v[168:171]\n"
      "ds_read_b64_tr_b16 v[180:181], v244 offset:32768\n"
      "v_mfma_f32_16x16x32_bf16 v[132:135], v[196:199], a[200:203], v[132:135]\n"
      "buffer_load_dwordx4 v247, s[52:55], s84 offen offset:0  lds\n"
      "v_mfma_f32_16x16x32_bf16 v[140:143], v[196:199], a[216:219], v[140:143]\n"
      "buffer_load_dwordx4 v248, s[52:55], s84 offen offset:1024  lds\n"
      "v_mfma_f32_16x16x32_bf16 v[148:151], v[196:199], a[232:235], v[148:151]\n"
      "ds_read_b64_tr_b16 v[182:183], v244 offset:36864\n"
      "v_mfma_f32_16x16x32_bf16 v[156:159], v[196:199], a[248:251], v[156:159]\n"
      "buffer_load_dwordx4 v249, s[52:55], s84 offen offset:2048  lds\n"
      "v_mfma_f32_16x16x32_bf16 v[164:167], v[196:199], v[8:11], v[164:167]\n"
      "buffer_load_dwordx4 v250, s[52:55], s84 offen offset:3072  lds\n"
      "v_mfma_f32_16x16x32_bf16 v[172:175], v[196:199], v[24:27], v[172:175]\n"
      "s_add_u32 m0, s72, 4096\n"
      "v_mfma_f32_16x16x32_bf16 v[128:131], v[200:203], a[204:207], v[128:131]\n"
      "v_mfma_f32_16x16x32_bf16 v[136:139], v[200:203], a[220:223], v[136:139]\n"
      "s_add_u32 s84, s84, 20480\n"
      "ds_read_b64_tr_b16 v[184:185], v243 offset:40960\n"
      "v_mfma_f32_16x16x32_bf16 v[144:147], v[200:203], a[236:239], v[144:147]\n"
      "buffer_load_dwordx4 v247, s[52:55], s84 offen offset:0  lds\n"
      "v_mfma_f32_16x16x32_bf16 v[152:155], v[200:203], a[252:255], v[152:155]\n"
      "ds_read_b64_tr_b16 v[186:187], v243 offset:45056\n"
      "v_mfma_f32_16x16x32_bf16 v[160:163], v[200:203], v[12:15], v[160:163]\n"
      "buffer_load_dwordx4 v248, s[52:55], s84 offen offset:1024  lds\n"
      "v_mfma_f32_16x16x32_bf16 v[168:171], v[200:203], v[28:31], v[168:171]\n"
      "buffer_load_dwordx4 v249, s[52:55], s84 offen offset:2048  lds\n"
      "v_mfma_f32_16x16x32_bf16 v[132:135], v[204:207], a[204:207], v[132:135]\n"
      "ds_read_b64_tr_b16 v[188:189], v244 offset:40960\n"
      "v_mfma_f32_16x16x32_bf16 v[140:143], v[204:207], a[220:223], v[140:143]\n"
      "buffer_load_dwordx4 v250, s[52:55], s84 offen offset:3072  lds\n"
      "v_mfma_f32_16x16x32_bf16 v[148:151], v[204:207], a[236:239], v[148:151]\n"
      "s_add_u32 s83, s83, 1\n"
      "ds_read_b64_tr_b16 v[190:191], v244 offset:45056\n"
      "v_mfma_f32_16x16x32_bf16 v[156:159], v[204:207], a[252:255], v[156:159]\n"
      "v_mfma_f32_16x16x32_bf16 v[164:167], v[204:207], v[12:15], v[164:167]\n"
      "s_and_b32 s83, s83, 3\n"
      "v_mfma_f32_16x16x32_bf16 v[172:175], v[204:207], v[28:31], v[172:175]\n"
      "s_waitcnt lgkmcnt(6)\n"
      "s_mul_i32 s84, s83, 655360\n"
      "v_mfma_f32_16x16x32_bf16 v[128:131], v[176:179], v[32:35], v[128:131]\n"
      "v_mfma_f32_16x16x32_bf16 v[136:139], v[176:179], v[48:51], v[136:139]\n"
      "s_lshl_b32 s73, s82, 8\n"
      "ds_read_b64_tr_b16 v[192:193], v243 offset:49152\n"
      "v_mfma_f32_16x16x32_bf16 v[144:147], v[176:179], v[64:67], v[144:147]\n"
      "s_add_u32 m0, s72, 131072\n"
      "s_mul_i32 s85, s86, 24576\n"
      "v_mfma_f32_16x16x32_bf16 v[152:155], v[176:179], v[80:83], v[152:155]\n"
      "ds_read_b64_tr_b16 v[194:195], v243 offset:53248\n"
      "v_mfma_f32_16x16x32_bf16 v[160:163], v[176:179], v[96:99], v[160:163]\n"
      "s_add_u32 s84, s84, s73\n"
      "v_mfma_f32_16x16x32_bf16 v[168:171], v[176:179], v[112:115], v[168:171]\n"
      "s_waitcnt lgkmcnt(6)\n"
      "buffer_load_dwordx4 v247, s[52:55], s84 offen offset:0  lds\n"
      "v_mfma_f32_16x16x32_bf16 v[132:135], v[180:183], v[32:35], v[132:135]\n"
      "ds_read_b64_tr_b16 v[196:197], v244 offset:49152\n"
      "v_mfma_f32_16x16x32_bf16 v[140:143], v[180:183], v[48:51], v[140:143]\n"
      "buffer_load_dwordx4 v248, s[52:55], s84 offen offset:1024  lds\n"
      "v_mfma_f32_16x16x32_bf16 v[148:151], v[180:183], v[64:67], v[148:151]\n"
      "s_mul_i32 s74, %8, 6144\n"
      "ds_read_b64_tr_b16 v[198:199], v244 offset:53248\n"
      "v_mfma_f32_16x16x32_bf16 v[156:159], v[180:183], v[80:83], v[156:159]\n"
      "buffer_load_dwordx4 v249, s[52:55], s84 offen offset:2048  lds\n"
      "v_mfma_f32_16x16x32_bf16 v[164:167], v[180:183], v[96:99], v[164:167]\n"
      "buffer_load_dwordx4 v250, s[52:55], s84 offen offset:3072  lds\n"
      "v_mfma_f32_16x16x32_bf16 v[172:175], v[180:183], v[112:115], v[172:175]\n"
      "s_waitcnt lgkmcnt(6)\n"
      "s_add_u32 m0, s72, 135168\n"
      "v_mfma_f32_16x16x32_bf16 v[128:131], v[184:187], v[36:39], v[128:131]\n"
      "ds_read_b64_tr_b16 v[200:201], v243 offset:57344\n"
      "v_mfma_f32_16x16x32_bf16 v[136:139], v[184:187], v[52:55], v[136:139]\n"
      "s_add_u32 s84, s84, 20480\n"
      "buffer_load_dwordx4 v247, s[52:55], s84 offen offset:0  lds\n"
      "v_mfma_f32_16x16x32_bf16 v[144:147], v[184:187], v[68:71], v[144:147]\n"
      "s_add_u32 s85, s85, s74\n"
      "ds_read_b64_tr_b16 v[202:203], v243 offset:61440\n"
      "v_mfma_f32_16x16x32_bf16 v[152:155], v[184:187], v[84:87], v[152:155]\n"
      "ds_read_b64_tr_b16 v[204:205], v244 offset:57344\n"
      "v_mfma_f32_16x16x32_bf16 v[160:163], v[184:187], v[100:103], v[160:163]\n"
      "buffer_load_dwordx4 v248, s[52:55], s84 offen offset:1024  lds\n"
      "v_mfma_f32_16x16x32_bf16 v[168:171], v[184:187], v[116:119], v[168:171]\n"
      "s_waitcnt lgkmcnt(7)\n"
      "ds_read_b64_tr_b16 v[206:207], v244 offset:61440\n"
      "v_mfma_f32_16x16x32_bf16 v[132:135], v[188:191], v[36:39], v[132:135]\n"
      "buffer_load_dwordx4 v249, s[52:55], s84 offen offset:2048  lds\n"
      "v_mfma_f32_16x16x32_bf16 v[140:143], v[188:191], v[52:55], v[140:143]\n"
      "s_mul_i32 s74, %13, 122880\n"
      "buffer_load_dwordx4 v250, s[52:55], s84 offen offset:3072  lds\n"
      "v_mfma_f32_16x16x32_bf16 v[148:151], v[188:191], v[68:71], v[148:151]\n"
      "s_add_u32 s83, s83, 1\n"
      "v_mfma_f32_16x16x32_bf16 v[156:159], v[188:191], v[84:87], v[156:159]\n"
      "v_mfma_f32_16x16x32_bf16 v[164:167], v[188:191], v[100:103], v[164:167]\n"
      "s_and_b32 s83, s83, 3\n"
      "v_mfma_f32_16x16x32_bf16 v[172:175], v[188:191], v[116:119], v[172:175]\n"
      "s_waitcnt vmcnt(16) lgkmcnt(0)\n"
      "s_barrier\n"
      "s_mul_i32 s84, s83, 655360\n"
      "v_mfma_f32_16x16x32_bf16 v[128:131], v[192:195], v[40:43], v[128:131]\n"
      "ds_read_b64_tr_b16 v[176:177], v245 offset:0\n"
      "v_mfma_f32_16x16x32_bf16 v[136:139], v[192:195], v[56:59], v[136:139]\n"
      "s_add_u32 s85, s85, s74\n"
      "s_lshl_b32 s73, s82, 8\n"
      "v_mfma_f32_16x16x32_bf16 v[144:147], v[192:195], v[72:75], v[144:147]\n"
      "s_add_u32 m0, s72, 32768\n"
      "v_mfma_f32_16x16x32_bf16 v[152:155], v[192:195], v[88:91], v[152:155]\n"
      "s_add_u32 s84, s84, s73\n"
      "ds_read_b64_tr_b16 v[178:179], v245 offset:4096\n"
      "v_mfma_f32_16x16x32_bf16 v[160:163], v[192:195], v[104:107], v[160:163]\n"
      "buffer_load_dwordx4 v247, s[52:55], s84 offen offset:0  lds\n"
      "v_mfma_f32_16x16x32_bf16 v[168:171], v[192:195], v[120:123], v[168:171]\n"
      "ds_read_b64_tr_b16 v[180:181], v246 offset:0\n"
      "v_mfma_f32_16x16x32_bf16 v[132:135], v[196:199], v[40:43], v[132:135]\n"
      "buffer_load_dwordx4 v248, s[52:55], s84 offen offset:1024  lds\n"
      "v_mfma_f32_16x16x32_bf16 v[140:143], v[196:199], v[56:59], v[140:143]\n"
      "s_add_u32 s86, s86, 1\n"
      "buffer_load_dwordx4 v249, s[52:55], s84 offen offset:2048  lds\n"
      "v_mfma_f32_16x16x32_bf16 v[148:151], v[196:199], v[72:75], v[148:151]\n"
      "ds_read_b64_tr_b16 v[182:183], v246 offset:4096\n"
      "v_mfma_f32_16x16x32_bf16 v[156:159], v[196:199], v[88:91], v[156:159]\n"
      "buffer_load_dwordx4 v250, s[52:55], s84 offen offset:3072  lds\n"
      "v_mfma_f32_16x16x32_bf16 v[164:167], v[196:199], v[104:107], v[164:167]\n"
      "s_add_u32 m0, s72, 36864\n"
      "s_add_u32 s84, s84, 20480\n"
      "buffer_load_dwordx4 v247, s[52:55], s84 offen offset:0  lds\n"
      "v_mfma_f32_16x16x32_bf16 v[172:175], v[196:199], v[120:123], v[172:175]\n"
      "buffer_load_dwordx4 v248, s[52:55], s84 offen offset:1024  lds\n"
      "v_mfma_f32_16x16x32_bf16 v[128:131], v[200:203], v[44:47], v[128:131]\n"
      "buffer_load_dwordx4 v249, s[52:55], s84 offen offset:2048  lds\n"
      "v_mfma_f32_16x16x32_bf16 v[136:139], v[200:203], v[60:63], v[136:139]\n"
      "s_cmp_eq_u32 s86, 5\n"
      "s_cselect_b32 s86, 0, s86\n"
      "ds_read_b64_tr_b16 v[184:185], v245 offset:8192\n"
      "v_mfma_f32_16x16x32_bf16 v[144:147], v[200:203], v[76:79], v[144:147]\n"
      "buffer_load_dwordx4 v250, s[52:55], s84 offen offset:3072  lds\n"
      "v_mfma_f32_16x16x32_bf16 v[152:155], v[200:203], v[92:95], v[152:155]\n"
      "ds_read_b64_tr_b16 v[186:187], v245 offset:12288\n"
      "v_mfma_f32_16x16x32_bf16 v[160:163], v[200:203], v[108:111], v[160:163]\n"
      "v_cvt_pk_bf16_f32 v208, v128, v129\n"
      "v_cvt_pk_bf16_f32 v209, v130, v131\n"
      "v_mfma_f32_16x16x32_bf16 v[168:171], v[200:203], v[124:127], v[168:171]\n"
      "v_cvt_pk_bf16_f32 v212, v136, v137\n"
      "v_cvt_pk_bf16_f32 v213, v138, v139\n"
      "v_mfma_f32_16x16x32_bf16 v[132:135], v[204:207], v[44:47], v[132:135]\n"
      "s_add_u32 s83, s83, 1\n"
      "s_and_b32 s83, s83, 3\n"
      "ds_read_b64_tr_b16 v[188:189], v246 offset:8192\n"
      "v_mfma_f32_16x16x32_bf16 v[140:143], v[204:207], v[60:63], v[140:143]\n"
      "s_add_u32 s82, s82, 1\n"
      "v_cvt_pk_bf16_f32 v216, v144, v145\n"
      "v_cvt_pk_bf16_f32 v217, v146, v147\n"
      "v_mfma_f32_16x16x32_bf16 v[148:151], v[204:207], v[76:79], v[148:151]\n"
      "s_cmp_eq_u32 s82, 5\n"
      "s_cselect_b32 s82, 0, s82\n"
      "ds_read_b64_tr_b16 v[190:191], v246 offset:12288\n"
      "v_mfma_f32_16x16x32_bf16 v[156:159], v[204:207], v[92:95], v[156:159]\n"
      "v_cvt_pk_bf16_f32 v220, v152, v153\n"
      "v_cvt_pk_bf16_f32 v221, v154, v155\n"
      "v_mfma_f32_16x16x32_bf16 v[164:167], v[204:207], v[108:111], v[164:167]\n"
      "v_cvt_pk_bf16_f32 v224, v160, v161\n"
      "v_cvt_pk_bf16_f32 v225, v162, v163\n"
      "v_mfma_f32_16x16x32_bf16 v[172:175], v[204:207], v[124:127], v[172:175]\n"
      "s_waitcnt lgkmcnt(6)\n"
      "v_cvt_pk_bf16_f32 v228, v168, v169\n"
      "v_cvt_pk_bf16_f32 v229, v170, v171\n"
      "v_mfma_f32_16x16x32_bf16 v[128:131], v[176:179], a[0:3], 0\n"
      "v_cvt_pk_bf16_f32 v210, v132, v133\n"
      "v_cvt_pk_bf16_f32 v211, v134, v135\n"
      "v_mfma_f32_16x16x32_bf16 v[136:139], v[176:179], a[16:19], 0\n"
      "buffer_store_dwordx4 v[208:211], v242, s[56:59], s85 offen offset:0 sc1\n"
      "v_cvt_pk_bf16_f32 v214, v140, v141\n"
      "v_cvt_pk_bf16_f32 v215, v142, v143\n"
      "v_mfma_f32_16x16x32_bf16 v[144:147], v[176:179], a[32:35], 0\n"
      "buffer_store_dwordx4 v[212:215], v242, s[56:59], s85 offen offset:1024 sc1\n"
      "v_cvt_pk_bf16_f32 v218, v148, v149\n"
      "v_cvt_pk_bf16_f32 v219, v150, v151\n"
      "v_mfma_f32_16x16x32_bf16 v[152:155], v[176:179], a[48:51], 0\n"
      "buffer_store_dwordx4 v[216:219], v242, s[56:59], s85 offen offset:2048 sc1\n"
      "v_cvt_pk_bf16_f32 v222, v156, v157\n"
      "v_cvt_pk_bf16_f32 v223, v158, v159\n"
      "v_mfma_f32_16x16x32_bf16 v[160:163], v[176:179], a[64:67], 0\n"
      "buffer_store_dwordx4 v[220:223], v242, s[56:59], s85 offen offset:3072 sc1\n"
      "v_cvt_pk_bf16_f32 v226, v164, v165\n"
      "v_cvt_pk_bf16_f32 v227, v166, v167\n"
      "v_mfma_f32_16x16x32_bf16 v[168:171], v[176:179], a[80:83], 0\n"
      "s_waitcnt lgkmcnt(4)\n"
      "s_add_u32 s85, s85, 4096\n"
      "buffer_store_dwordx4 v[224:227], v242, s[56:59], s85 offen offset:0 sc1\n"
      "v_cvt_pk_bf16_f32 v230, v172, v173\n"
      "v_cvt_pk_bf16_f32 v231, v174, v175\n"
      "v_mfma_f32_16x16x32_bf16 v[132:135], v[180:183], a[0:3], 0\n"
      "ds_read_b64_tr_b16 v[192:193], v245 offset:16384\n"
      "buffer_store_dwordx4 v[228:231], v242, s[56:59], s85 offen offset:1024 sc1\n"
      "v_mfma_f32_16x16x32_bf16 v[140:143], v[180:183], a[16:19], 0\n"
      "ds_read_b64_tr_b16 v[194:195], v245 offset:20480\n"
      "v_mfma_f32_16x16x32_bf16 v[148:151], v[180:183], a[32:35], 0\n"
      "ds_read_b64_tr_b16 v[196:197], v246 offset:16384\n"
      "v_mfma_f32_16x16x32_bf16 v[156:159], v[180:183], a[48:51], 0\n"
      "ds_read_b64_tr_b16 v[198:199], v246 offset:20480\n"
      "v_mfma_f32_16x16x32_bf16 v[164:167], v[180:183], a[64:67], 0\n"
      "v_mfma_f32_16x16x32_bf16 v[172:175], v[180:183], a[80:83], 0\n"
      "s_waitcnt lgkmcnt(6)\n"
      "v_mfma_f32_16x16x32_bf16 v[128:131], v[184:187], a[4:7], v[128:131]\n"
      "ds_read_b64_tr_b16 v[200:201], v245 offset:24576\n"
      "v_mfma_f32_16x16x32_bf16 v[136:139], v[184:187], a[20:23], v[136:139]\n"
      "v_mfma_f32_16x16x32_bf16 v[144:147], v[184:187], a[36:39], v[144:147]\n"
      "ds_read_b64_tr_b16 v[202:203], v245 offset:28672\n"
      "v_mfma_f32_16x16x32_bf16 v[152:155], v[184:187], a[52:55], v[152:155]\n"
      "ds_read_b64_tr_b16 v[204:205], v246 offset:24576\n"
      "v_mfma_f32_16x16x32_bf16 v[160:163], v[184:187], a[68:71], v[160:163]\n"
      "v_mfma_f32_16x16x32_bf16 v[168:171], v[184:187], a[84:87], v[168:171]\n"
      "s_waitcnt lgkmcnt(7)\n"
      "ds_read_b64_tr_b16 v[206:207], v246 offset:28672\n"
      "v_mfma_f32_16x16x32_bf16 v[132:135], v[188:191], a[4:7], v[132:135]\n"
      "v_mfma_f32_16x16x32_bf16 v[140:143], v[188:191], a[20:23], v[140:143]\n"
      "v_mfma_f32_16x16x32_bf16 v[148:151], v[188:191], a[36:39], v[148:151]\n"
      "v_mfma_f32_16x16x32_bf16 v[156:159], v[188:191], a[52:55], v[156:159]\n"
      "v_mfma_f32_16x16x32_bf16 v[164:167], v[188:191], a[68:71], v[164:167]\n"
      "v_mfma_f32_16x16x32_bf16 v[172:175], v[188:191], a[84:87], v[172:175]\n"
      "s_waitcnt vmcnt(22) lgkmcnt(0)\n"
      "s_barrier\n"
      "v_mfma_f32_16x16x32_bf16 v[128:131], v[192:195], a[8:11], v[128:131]\n"
      "ds_read_b64_tr_b16 v[176:177], v243 offset:0\n"
      "v_mfma_f32_16x16x32_bf16 v[136:139], v[192:195], a[24:27], v[136:139]\n"
      "v_mfma_f32_16x16x32_bf16 v[144:147], v[192:195], a[40:43], v[144:147]\n"
      "v_mfma_f32_16x16x32_bf16 v[152:155], v[192:195], a[56:59], v[152:155]\n"
      "ds_read_b64_tr_b16 v[178:179], v243 offset:4096\n"
      "v_mfma_f32_16x16x32_bf16 v[160:163], v[192:195], a[72:75], v[160:163]\n"
      "v_mfma_f32_16x16x32_bf16 v[168:171], v[192:195], a[88:91], v[168:171]\n"
      "ds_read_b64_tr_b16 v[180:181], v244 offset:0\n"
      "v_mfma_f32_16x16x32_bf16 v[132:135], v[196:199], a[8:11], v[132:135]\n"
      "v_mfma_f32_16x16x32_bf16 v[140:143], v[196:199], a[24:27], v[140:143]\n"
      "v_mfma_f32_16x16x32_bf16 v[148:151], v[196:199], a[40:43], v[148:151]\n"
      "ds_read_b64_tr_b16 v[182:183], v244 offset:4096\n"
      "v_mfma_f32_16x16x32_bf16 v[156:159], v[196:199], a[56:59], v[156:159]\n"
      "v_mfma_f32_16x16x32_bf16 v[164:167], v[196:199], a[72:75], v[164:167]\n"
      "v_mfma_f32_16x16x32_bf16 v[172:175], v[196:199], a[88:91], v[172:175]\n"
      "v_mfma_f32_16x16x32_bf16 v[128:131], v[200:203], a[12:15], v[128:131]\n"
      "v_mfma_f32_16x16x32_bf16 v[136:139], v[200:203], a[28:31], v[136:139]\n"
      "ds_read_b64_tr_b16 v[184:185], v243 offset:8192\n"
      "v_mfma_f32_16x16x32_bf16 v[144:147], v[200:203], a[44:47], v[144:147]\n"
      "v_mfma_f32_16x16x32_bf16 v[152:155], v[200:203], a[60:63], v[152:155]\n"
      "ds_read_b64_tr_b16 v[186:187], v243 offset:12288\n"
      "v_mfma_f32_16x16x32_bf16 v[160:163], v[200:203], a[76:79], v[160:163]\n"
      "v_mfma_f32_16x16x32_bf16 v[168:171], v[200:203], a[92:95], v[168:171]\n"
      "s_mul_i32 s84, s83, 655360\n"
      "v_mfma_f32_16x16x32_bf16 v[132:135], v[204:207], a[12:15], v[132:135]\n"
      "ds_read_b64_tr_b16 v[188:189], v244 offset:8192\n"
      "v_mfma_f32_16x16x32_bf16 v[140:143], v[204:207], a[28:31], v[140:143]\n"
      "s_lshl_b32 s73, s82, 8\n"
      "v_mfma_f32_16x16x32_bf16 v[148:151], v[204:207], a[44:47], v[148:151]\n"
      "ds_read_b64_tr_b16 v[190:191], v244 offset:12288\n"
      "v_mfma_f32_16x16x32_bf16 v[156:159], v[204:207], a[60:63], v[156:159]\n"
      "s_add_u32 m0, s72, 98304\n"
      "v_mfma_f32_16x16x32_bf16 v[164:167], v[204:207], a[76:79], v[164:167]\n"
      "v_mfma_f32_16x16x32_bf16 v[172:175], v[204:207], a[92:95], v[172:175]\n"
      "s_waitcnt lgkmcnt(6)\n"
      "v_mfma_f32_16x16x32_bf16 v[128:131], v[176:179], a[96:99], v[128:131]\n"
      "s_add_u32 s84, s84, s73\n"
      "v_mfma_f32_16x16x32_bf16 v[136:139], v[176:179], a[112:115], v[136:139]\n"
      "ds_read_b64_tr_b16 v[192:193], v243 offset:16384\n"
      "v_mfma_f32_16x16x32_bf16 v[144:147], v[176:179], a[128:131], v[144:147]\n"
      "buffer_load_dwordx4 v247, s[52:55], s84 offen offset:0  lds\n"
      "v_mfma_f32_16x16x32_bf16 v[152:155], v[176:179], a[144:147], v[152:155]\n"
      "ds_read_b64_tr_b16 v[194:195], v243 offset:20480\n"
      "v_mfma_f32_16x16x32_bf16 v[160:163], v[176:179], a[160:163], v[160:163]\n"
      "v_mfma_f32_16x16x32_bf16 v[168:171], v[176:179], a[176:179], v[168:171]\n"
      "s_waitcnt lgkmcnt(6)\n"
      "buffer_load_dwordx4 v248, s[52:55], s84 offen offset:1024  lds\n"
      "v_mfma_f32_16x16x32_bf16 v[132:135], v[180:183], a[96:99], v[132:135]\n"
      "ds_read_b64_tr_b16 v[196:197], v244 offset:16384\n"
      "v_mfma_f32_16x16x32_bf16 v[140:143], v[180:183], a[112:115], v[140:143]\n"
      "buffer_load_dwordx4 v249, s[52:55], s84 offen offset:2048  lds\n"
      "v_mfma_f32_16x16x32_bf16 v[148:151], v[180:183], a[128:131], v[148:151]\n"
      "ds_read_b64_tr_b16 v[198:199], v244 offset:20480\n"
      "v_mfma_f32_16x16x32_bf16 v[156:159], v[180:183], a[144:147], v[156:159]\n"
      "buffer_load_dwordx4 v250, s[52:55], s84 offen offset:3072  lds\n"
      "v_mfma_f32_16x16x32_bf16 v[164:167], v[180:183], a[160:163], v[164:167]\n"
      "v_mfma_f32_16x16x32_bf16 v[172:175], v[180:183], a[176:179], v[172:175]\n"
      "s_waitcnt lgkmcnt(6)\n"
      "v_mfma_f32_16x16x32_bf16 v[128:131], v[184:187], a[100:103], v[128:131]\n"
      "s_add_u32 m0, s72, 102400\n"
      "ds_read_b64_tr_b16 v[200:201], v243 offset:24576\n"
      "v_mfma_f32_16x16x32_bf16 v[136:139], v[184:187], a[116:119], v[136:139]\n"
      "v_mfma_f32_16x16x32_bf16 v[144:147], v[184:187], a[132:135], v[144:147]\n"
      "s_add_u32 s84, s84, 20480\n"
      "ds_read_b64_tr_b16 v[202:203], v243 offset:28672\n"
      "v_mfma_f32_16x16x32_bf16 v[152:155], v[184:187], a[148:151], v[152:155]\n"
      "ds_read_b64_tr_b16 v[204:205], v244 offset:24576\n"
      "v_mfma_f32_16x16x32_bf16 v[160:163], v[184:187], a[164:167], v[160:163]\n"
      "buffer_load_dwordx4 v247, s[52:55], s84 offen offset:0  lds\n"
      "v_mfma_f32_16x16x32_bf16 v[168:171], v[184:187], a[180:183], v[168:171]\n"
      "s_waitcnt lgkmcnt(7)\n"
      "ds_read_b64_tr_b16 v[206:207], v244 offset:28672\n"
      "v_mfma_f32_16x16x32_bf16 v[132:135], v[188:191], a[100:103], v[132:135]\n"
      "v_mfma_f32_16x16x32_bf16 v[140:143], v[188:191], a[116:119], v[140:143]\n"
      "buffer_load_dwordx4 v248, s[52:55], s84 offen offset:1024  lds\n"
      "v_mfma_f32_16x16x32_bf16 v[148:151], v[188:191], a[132:135], v[148:151]\n"
      "v_mfma_f32_16x16x32_bf16 v[156:159], v[188:191], a[148:151], v[156:159]\n"
      "buffer_load_dwordx4 v249, s[52:55], s84 offen offset:2048  lds\n"
      "v_mfma_f32_16x16x32_bf16 v[164:167], v[188:191], a[164:167], v[164:167]\n"
      "v_mfma_f32_16x16x32_bf16 v[172:175], v[188:191], a[180:183], v[172:175]\n"
      "s_waitcnt vmcnt(21) lgkmcnt(0)\n"
      "s_barrier\n"
      "buffer_load_dwordx4 v250, s[52:55], s84 offen offset:3072  lds\n"
      "v_mfma_f32_16x16x32_bf16 v[128:131], v[192:195], a[104:107], v[128:131]\n"
      "ds_read_b64_tr_b16 v[176:177], v245 offset:32768\n"
      "v_mfma_f32_16x16x32_bf16 v[136:139], v[192:195], a[120:123], v[136:139]\n"
      "v_mfma_f32_16x16x32_bf16 v[144:147], v[192:195], a[136:139], v[144:147]\n"
      "s_add_u32 s83, s83, 1\n"
      "v_mfma_f32_16x16x32_bf16 v[152:155], v[192:195], a[152:155], v[152:155]\n"
      "ds_read_b64_tr_b16 v[178:179], v245 offset:36864\n"
      "v_mfma_f32_16x16x32_bf16 v[160:163], v[192:195], a[168:171], v[160:163]\n"
      "s_and_b32 s83, s83, 3\n"
      "v_mfma_f32_16x16x32_bf16 v[168:171], v[192:195], a[184:187], v[168:171]\n"
      "ds_read_b64_tr_b16 v[180:181], v246 offset:32768\n"
      "v_mfma_f32_16x16x32_bf16 v[132:135], v[196:199], a[104:107], v[132:135]\n"
      "v_mfma_f32_16x16x32_bf16 v[140:143], v[196:199], a[120:123], v[140:143]\n"
      "s_mul_i32 s84, s83, 655360\n"
      "v_mfma_f32_16x16x32_bf16 v[148:151], v[196:199], a[136:139], v[148:151]\n"
      "ds_read_b64_tr_b16 v[182:183], v246 offset:36864\n"
      "v_mfma_f32_16x16x32_bf16 v[156:159], v[196:199], a[152:155], v[156:159]\n"
      "s_lshl_b32 s73, s82, 8\n"
      "v_mfma_f32_16x16x32_bf16 v[164:167], v[196:199], a[168:171], v[164:167]\n"
      "v_mfma_f32_16x16x32_bf16 v[172:175], v[196:199], a[184:187], v[172:175]\n"
      "s_add_u32 m0, s72, 0\n"
      "v_mfma_f32_16x16x32_bf16 v[128:131], v[200:203], a[108:111], v[128:131]\n"
      "v_mfma_f32_16x16x32_bf16 v[136:139], v[200:203], a[124:127], v[136:139]\n"
      "ds_read_b64_tr_b16 v[184:185], v245 offset:40960\n"
      "v_mfma_f32_16x16x32_bf16 v[144:147], v[200:203], a[140:143], v[144:147]\n"
      "s_add_u32 s84, s84, s73\n"
      "v_mfma_f32_16x16x32_bf16 v[152:155], v[200:203], a[156:159], v[152:155]\n"
      "ds_read_b64_tr_b16 v[186:187], v245 offset:45056\n"
      "v_mfma_f32_16x16x32_bf16 v[160:163], v[200:203], a[172:175], v[160:163]\n"
      "buffer_load_dwordx4 v247, s[52:55], s84 offen offset:0  lds\n"
      "v_mfma_f32_16x16x32_bf16 v[168:171], v[200:203], a[188:191], v[168:171]\n"
      "v_mfma_f32_16x16x32_bf16 v[132:135], v[204:207], a[108:111], v[132:135]\n"
      "ds_read_b64_tr_b16 v[188:189], v246 offset:40960\n"
      "v_mfma_f32_16x16x32_bf16 v[140:143], v[204:207], a[124:127], v[140:143]\n"
      "buffer_load_dwordx4 v248, s[52:55], s84 offen offset:1024  lds\n"
      "v_mfma_f32_16x16x32_bf16 v[148:151], v[204:207], a[140:143], v[148:151]\n"
      "ds_read_b64_tr_b16 v[190:191], v246 offset:45056\n"
      "v_mfma_f32_16x16x32_bf16 v[156:159], v[204:207], a[156:159], v[156:159]\n"
      "buffer_load_dwordx4 v249, s[52:55], s84 offen offset:2048  lds\n"
      "v_mfma_f32_16x16x32_bf16 v[164:167], v[204:207], a[172:175], v[164:167]\n"
      "v_mfma_f32_16x16x32_bf16 v[172:175], v[204:207], a[188:191], v[172:175]\n"
      "s_waitcnt lgkmcnt(6)\n"
      "buffer_load_dwordx4 v250, s[52:55], s84 offen offset:3072  lds\n"
      "v_mfma_f32_16x16x32_bf16 v[128:131], v[176:179], a[192:195], v[128:131]\n"
      "v_mfma_f32_16x16x32_bf16 v[136:139], v[176:179], a[208:211], v[136:139]\n"
      "ds_read_b64_tr_b16 v[192:193], v245 offset:49152\n"
      "v_mfma_f32_16x16x32_bf16 v[144:147], v[176:179], a[224:227], v[144:147]\n"
      "s_add_u32 m0, s72, 4096\n"
      "v_mfma_f32_16x16x32_bf16 v[152:155], v[176:179], a[240:243], v[152:155]\n"
      "ds_read_b64_tr_b16 v[194:195], v245 offset:53248\n"
      "v_mfma_f32_16x16x32_bf16 v[160:163], v[176:179], v[0:3], v[160:163]\n"
      "s_add_u32 s84, s84, 20480\n"
      "v_mfma_f32_16x16x32_bf16 v[168:171], v[176:179], v[16:19], v[168:171]\n"
      "s_waitcnt lgkmcnt(6)\n"
      "v_mfma_f32_16x16x32_bf16 v[132:135], v[180:183], a[192:195], v[132:135]\n"
      "ds_read_b64_tr_b16 v[196:197], v246 offset:49152\n"
      "v_mfma_f32_16x16x32_bf16 v[140:143], v[180:183], a[208:211], v[140:143]\n"
      "buffer_load_dwordx4 v247, s[52:55], s84 offen offset:0  lds\n"
      "v_mfma_f32_16x16x32_bf16 v[148:151], v[180:183], a[224:227], v[148:151]\n"
      "ds_read_b64_tr_b16 v[198:199], v246 offset:53248\n"
      "v_mfma_f32_16x16x32_bf16 v[156:159], v[180:183], a[240:243], v[156:159]\n"
      "buffer_load_dwordx4 v248, s[52:55], s84 offen offset:1024  lds\n"
      "v_mfma_f32_16x16x32_bf16 v[164:167], v[180:183], v[0:3], v[164:167]\n"
      "v_mfma_f32_16x16x32_bf16 v[172:175], v[180:183], v[16:19], v[172:175]\n"
      "s_waitcnt lgkmcnt(6)\n"
      "buffer_load_dwordx4 v249, s[52:55], s84 offen offset:2048  lds\n"
      "v_mfma_f32_16x16x32_bf16 v[128:131], v[184:187], a[196:199], v[128:131]\n"
      "ds_read_b64_tr_b16 v[200:201], v245 offset:57344\n"
      "v_mfma_f32_16x16x32_bf16 v[136:139], v[184:187], a[212:215], v[136:139]\n"
      "buffer_load_dwordx4 v250, s[52:55], s84 offen offset:3072  lds\n"
      "v_mfma_f32_16x16x32_bf16 v[144:147], v[184:187], a[228:231], v[144:147]\n"
      "ds_read_b64_tr_b16 v[202:203], v245 offset:61440\n"
      "v_mfma_f32_16x16x32_bf16 v[152:155], v[184:187], a[244:247], v[152:155]\n"
      "ds_read_b64_tr_b16 v[204:205], v246 offset:57344\n"
      "v_mfma_f32_16x16x32_bf16 v[160:163], v[184:187], v[4:7], v[160:163]\n"
      "s_add_u32 s83, s83, 1\n"
      "v_mfma_f32_16x16x32_bf16 v[168:171], v[184:187], v[20:23], v[168:171]\n"
      "s_waitcnt lgkmcnt(7)\n"
      "ds_read_b64_tr_b16 v[206:207], v246 offset:61440\n"
      "v_mfma_f32_16x16x32_bf16 v[132:135], v[188:191], a[196:199], v[132:135]\n"
      "s_and_b32 s83, s83, 3\n"
      "v_mfma_f32_16x16x32_bf16 v[140:143], v[188:191], a[212:215], v[140:143]\n"
      "v_mfma_f32_16x16x32_bf16 v[148:151], v[188:191], a[228:231], v[148:151]\n"
      "v_mfma_f32_16x16x32_bf16 v[156:159], v[188:191], a[244:247], v[156:159]\n"
      "v_mfma_f32_16x16x32_bf16 v[164:167], v[188:191], v[4:7], v[164:167]\n"
      "v_mfma_f32_16x16x32_bf16 v[172:175], v[188:191], v[20:23], v[172:175]\n"
      "s_waitcnt vmcnt(22) lgkmcnt(0)\n"
      "s_barrier\n"
      "v_mfma_f32_16x16x32_bf16 v[128:131], v[192:195], a[200:203], v[128:131]\n"
      "s_mul_i32 s84, s83, 655360\n"
      "ds_read_b64_tr_b16 v[176:177], v243 offset:32768\n"
      "v_mfma_f32_16x16x32_bf16 v[136:139], v[192:195], a[216:219], v[136:139]\n"
      "v_mfma_f32_16x16x32_bf16 v[144:147], v[192:195], a[232:235], v[144:147]\n"
      "v_mfma_f32_16x16x32_bf16 v[152:155], v[192:195], a[248:251], v[152:155]\n"
      "s_lshl_b32 s73, s82, 8\n"
      "ds_read_b64_tr_b16 v[178:179], v243 offset:36864\n"
      "v_mfma_f32_16x16x32_bf16 v[160:163], v[192:195], v[8:11], v[160:163]\n"
      "v_mfma_f32_16x16x32_bf16 v[168:171], v[192:195], v[24:27], v[168:171]\n"
      "ds_read_b64_tr_b16 v[180:181], v244 offset:32768\n"
      "v_mfma_f32_16x16x32_bf16 v[132:135], v[196:199], a[200:203], v[132:135]\n"
      "s_add_u32 m0, s72, 131072\n"
      "v_mfma_f32_16x16x32_bf16 v[140:143], v[196:199], a[216:219], v[140:143]\n"
      "v_mfma_f32_16x16x32_bf16 v[148:151], v[196:199], a[232:235], v[148:151]\n"
      "ds_read_b64_tr_b16 v[182:183], v244 offset:36864\n"
      "v_mfma_f32_16x16x32_bf16 v[156:159], v[196:199], a[248:251], v[156:159]\n"
      "s_add_u32 s84, s84, s73\n"
      "v_mfma_f32_16x16x32_bf16 v[164:167], v[196:199], v[8:11], v[164:167]\n"
      "v_mfma_f32_16x16x32_bf16 v[172:175], v[196:199], v[24:27], v[172:175]\n"
      "v_mfma_f32_16x16x32_bf16 v[128:131], v[200:203], a[204:207], v[128:131]\n"
      "buffer_load_dwordx4 v247, s[52:55], s84 offen offset:0  lds\n"
      "v_mfma_f32_16x16x32_bf16 v[136:139], v[200:203], a[220:223], v[136:139]\n"
      "ds_read_b64_tr_b16 v[184:185], v243 offset:40960\n"
      "v_mfma_f32_16x16x32_bf16 v[144:147], v[200:203], a[236:239], v[144:147]\n"
      "buffer_load_dwordx4 v248, s[52:55], s84 offen offset:1024  lds\n"
      "v_mfma_f32_16x16x32_bf16 v[152:155], v[200:203], a[252:255], v[152:155]\n"
      "ds_read_b64_tr_b16 v[186:187], v243 offset:45056\n"
      "v_mfma_f32_16x16x32_bf16 v[160:163], v[200:203], v[12:15], v[160:163]\n"
      "v_mfma_f32_16x16x32_bf16 v[168:171], v[200:203], v[28:31], v[168:171]\n"
      "buffer_load_dwordx4 v249, s[52:55], s84 offen offset:2048  lds\n"
      "v_mfma_f32_16x16x32_bf16 v[132:135], v[204:207], a[204:207], v[132:135]\n"
      "ds_read_b64_tr_b16 v[188:189], v244 offset:40960\n"
      "v_mfma_f32_16x16x32_bf16 v[140:143], v[204:207], a[220:223], v[140:143]\n"
      "v_mfma_f32_16x16x32_bf16 v[148:151], v[204:207], a[236:239], v[148:151]\n"
      "ds_read_b64_tr_b16 v[190:191], v244 offset:45056\n"
      "v_mfma_f32_16x16x32_bf16 v[156:159], v[204:207], a[252:255], v[156:159]\n"
      "buffer_load_dwordx4 v250, s[52:55], s84 offen offset:3072  lds\n"
      "v_mfma_f32_16x16x32_bf16 v[164:167], v[204:207], v[12:15], v[164:167]\n"
      "v_mfma_f32_16x16x32_bf16 v[172:175], v[204:207], v[28:31], v[172:175]\n"
      "s_waitcnt lgkmcnt(6)\n"
      "v_mfma_f32_16x16x32_bf16 v[128:131], v[176:179], v[32:35], v[128:131]\n"
      "s_add_u32 m0, s72, 135168\n"
      "v_mfma_f32_16x16x32_bf16 v[136:139], v[176:179], v[48:51], v[136:139]\n"
      "ds_read_b64_tr_b16 v[192:193], v243 offset:49152\n"
      "v_mfma_f32_16x16x32_bf16 v[144:147], v[176:179], v[64:67], v[144:147]\n"
      "s_mul_i32 s85, s86, 24576\n"
      "v_mfma_f32_16x16x32_bf16 v[152:155], v[176:179], v[80:83], v[152:155]\n"
      "s_add_u32 s84, s84, 20480\n"
      "ds_read_b64_tr_b16 v[194:195], v243 offset:53248\n"
      "v_mfma_f32_16x16x32_bf16 v[160:163], v[176:179], v[96:99], v[160:163]\n"
      "v_mfma_f32_16x16x32_bf16 v[168:171], v[176:179], v[112:115], v[168:171]\n"
      "s_waitcnt lgkmcnt(6)\n"
      "buffer_load_dwordx4 v247, s[52:55], s84 offen offset:0  lds\n"
      "v_mfma_f32_16x16x32_bf16 v[132:135], v[180:183], v[32:35], v[132:135]\n"
      "ds_read_b64_tr_b16 v[196:197], v244 offset:49152\n"
      "v_mfma_f32_16x16x32_bf16 v[140:143], v[180:183], v[48:51], v[140:143]\n"
      "v_mfma_f32_16x16x32_bf16 v[148:151], v[180:183], v[64:67], v[148:151]\n"
      "s_mul_i32 s74, %8, 6144\n"
      "ds_read_b64_tr_b16 v[198:199], v244 offset:53248\n"
      "v_mfma_f32_16x16x32_bf16 v[156:159], v[180:183], v[80:83], v[156:159]\n"
      "buffer_load_dwordx4 v248, s[52:55], s84 offen offset:1024  lds\n"
      "v_mfma_f32_16x16x32_bf16 v[164:167], v[180:183], v[96:99], v[164:167]\n"
      "v_mfma_f32_16x16x32_bf16 v[172:175], v[180:183], v[112:115], v[172:175]\n"
      "s_waitcnt lgkmcnt(6)\n"
      "buffer_load_dwordx4 v249, s[52:55], s84 offen offset:2048  lds\n"
      "v_mfma_f32_16x16x32_bf16 v[128:131], v[184:187], v[36:39], v[128:131]\n"
      "ds_read_b64_tr_b16 v[200:201], v243 offset:57344\n"
      "v_mfma_f32_16x16x32_bf16 v[136:139], v[184:187], v[52:55], v[136:139]\n"
      "v_mfma_f32_16x16x32_bf16 v[144:147], v[184:187], v[68:71], v[144:147]\n"
      "s_add_u32 s85, s85, s74\n"
      "ds_read_b64_tr_b16 v[202:203], v243 offset:61440\n"
      "v_mfma_f32_16x16x32_bf16 v[152:155], v[184:187], v[84:87], v[152:155]\n"
      "ds_read_b64_tr_b16 v[204:205], v244 offset:57344\n"
      "v_mfma_f32_16x16x32_bf16 v[160:163], v[184:187], v[100:103], v[160:163]\n"
      "buffer_load_dwordx4 v250, s[52:55], s84 offen offset:3072  lds\n"
      "v_mfma_f32_16x16x32_bf16 v[168:171], v[184:187], v[116:119], v[168:171]\n"
      "s_waitcnt lgkmcnt(7)\n"
      "ds_read_b64_tr_b16 v[206:207], v244 offset:61440\n"
      "v_mfma_f32_16x16x32_bf16 v[132:135], v[188:191], v[36:39], v[132:135]\n"
      "s_add_u32 s83, s83, 1\n"
      "v_mfma_f32_16x16x32_bf16 v[140:143], v[188:191], v[52:55], v[140:143]\n"
      "s_mul_i32 s74, %13, 122880\n"
      "v_mfma_f32_16x16x32_bf16 v[148:151], v[188:191], v[68:71], v[148:151]\n"
      "v_mfma_f32_16x16x32_bf16 v[156:159], v[188:191], v[84:87], v[156:159]\n"
      "s_and_b32 s83, s83, 3\n"
      "v_mfma_f32_16x16x32_bf16 v[164:167], v[188:191], v[100:103], v[164:167]\n"
      "v_mfma_f32_16x16x32_bf16 v[172:175], v[188:191], v[116:119], v[172:175]\n"
      "s_waitcnt vmcnt(16) lgkmcnt(0)\n"
      "s_barrier\n"
      "s_mul_i32 s84, s83, 655360\n"
      "v_mfma_f32_16x16x32_bf16 v[128:131], v[192:195], v[40:43], v[128:131]\n"
      "ds_read_b64_tr_b16 v[176:177], v245 offset:0\n"
      "v_mfma_f32_16x16x32_bf16 v[136:139], v[192:195], v[56:59], v[136:139]\n"
      "s_lshl_b32 s73, s82, 8\n"
      "s_add_u32 s85, s85, s74\n"
      "v_mfma_f32_16x16x32_bf16 v[144:147], v[192:195], v[72:75], v[144:147]\n"
      "s_add_u32 m0, s72, 32768\n"
      "v_mfma_f32_16x16x32_bf16 v[152:155], v[192:195], v[88:91], v[152:155]\n"
      "s_add_u32 s84, s84, s73\n"
      "ds_read_b64_tr_b16 v[178:179], v245 offset:4096\n"
      "v_mfma_f32_16x16x32_bf16 v[160:163], v[192:195], v[104:107], v[160:163]\n"
      "buffer_load_dwordx4 v247, s[52:55], s84 offen offset:0  lds\n"
      "v_mfma_f32_16x16x32_bf16 v[168:171], v[192:195], v[120:123], v[168:171]\n"
      "ds_read_b64_tr_b16 v[180:181], v246 offset:0\n"
      "v_mfma_f32_16x16x32_bf16 v[132:135], v[196:199], v[40:43], v[132:135]\n"
      "buffer_load_dwordx4 v248, s[52:55], s84 offen offset:1024  lds\n"
      "v_mfma_f32_16x16x32_bf16 v[140:143], v[196:199], v[56:59], v[140:143]\n"
      "s_add_u32 s86, s86, 1\n"
      "buffer_load_dwordx4 v249, s[52:55], s84 offen offset:2048  lds\n"
      "v_mfma_f32_16x16x32_bf16 v[148:151], v[196:199], v[72:75], v[148:151]\n"
      "ds_read_b64_tr_b16 v[182:183], v246 offset:4096\n"
      "v_mfma_f32_16x16x32_bf16 v[156:159], v[196:199], v[88:91], v[156:159]\n"
      "buffer_load_dwordx4 v250, s[52:55], s84 offen offset:3072  lds\n"
      "v_mfma_f32_16x16x32_bf16 v[164:167], v[196:199], v[104:107], v[164:167]\n"
      "s_add_u32 m0, s72, 36864\n"
      "s_add_u32 s84, s84, 20480\n"
      "buffer_load_dwordx4 v247, s[52:55], s84 offen offset:0  lds\n"
      "v_mfma_f32_16x16x32_bf16 v[172:175], v[196:199], v[120:123], v[172:175]\n"
      "buffer_load_dwordx4 v248, s[52:55], s84 offen offset:1024  lds\n"
      "v_mfma_f32_16x16x32_bf16 v[128:131], v[200:203], v[44:47], v[128:131]\n"
      "buffer_load_dwordx4 v249, s[52:55], s84 offen offset:2048  lds\n"
      "v_mfma_f32_16x16x32_bf16 v[136:139], v[200:203], v[60:63], v[136:139]\n"
      "s_cmp_eq_u32 s86, 5\n"
      "s_cselect_b32 s86, 0, s86\n"
      "ds_read_b64_tr_b16 v[184:185], v245 offset:8192\n"
      "v_mfma_f32_16x16x32_bf16 v[144:147], v[200:203], v[76:79], v[144:147]\n"
      "buffer_load_dwordx4 v250, s[52:55], s84 offen offset:3072  lds\n"
      "v_mfma_f32_16x16x32_bf16 v[152:155], v[200:203], v[92:95], v[152:155]\n"
      "ds_read_b64_tr_b16 v[186:187], v245 offset:12288\n"
      "v_mfma_f32_16x16x32_bf16 v[160:163], v[200:203], v[108:111], v[160:163]\n"
      "v_cvt_pk_bf16_f32 v208, v128, v129\n"
      "v_cvt_pk_bf16_f32 v209, v130, v131\n"
      "v_mfma_f32_16x16x32_bf16 v[168:171], v[200:203], v[124:127], v[168:171]\n"
      "v_cvt_pk_bf16_f32 v212, v136, v137\n"
      "v_cvt_pk_bf16_f32 v213, v138, v139\n"
      "v_mfma_f32_16x16x32_bf16 v[132:135], v[204:207], v[44:47], v[132:135]\n"
      "s_add_u32 s83, s83, 1\n"
      "s_and_b32 s83, s83, 3\n"
      "ds_read_b64_tr_b16 v[188:189], v246 offset:8192\n"
      "v_mfma_f32_16x16x32_bf16 v[140:143], v[204:207], v[60:63], v[140:143]\n"
      "s_add_u32 s82, s82, 1\n"
      "v_cvt_pk_bf16_f32 v216, v144, v145\n"
      "v_cvt_pk_bf16_f32 v217, v146, v147\n"
      "v_mfma_f32_16x16x32_bf16 v[148:151], v[204:207], v[76:79], v[148:151]\n"
      "s_cmp_eq_u32 s82, 5\n"
      "s_cselect_b32 s82, 0, s82\n"
      "ds_read_b64_tr_b16 v[190:191], v246 offset:12288\n"
      "v_mfma_f32_16x16x32_bf16 v[156:159], v[204:207], v[92:95], v[156:159]\n"
      "v_cvt_pk_bf16_f32 v220, v152, v153\n"
      "v_cvt_pk_bf16_f32 v221, v154, v155\n"
      "v_mfma_f32_16x16x32_bf16 v[164:167], v[204:207], v[108:111], v[164:167]\n"
      "v_cvt_pk_bf16_f32 v224, v160, v161\n"
      "v_cvt_pk_bf16_f32 v225, v162, v163\n"
      "v_mfma_f32_16x16x32_bf16 v[172:175], v[204:207], v[124:127], v[172:175]\n"
      "s_waitcnt lgkmcnt(6)\n"
      "v_cvt_pk_bf16_f32 v228, v168, v169\n"
      "v_cvt_pk_bf16_f32 v229, v170, v171\n"
      "v_mfma_f32_16x16x32_bf16 v[128:131], v[176:179], a[0:3], 0\n"
      "v_cvt_pk_bf16_f32 v210, v132, v133\n"
      "v_cvt_pk_bf16_f32 v211, v134, v135\n"
      "v_mfma_f32_16x16x32_bf16 v[136:139], v[176:179], a[16:19], 0\n"
      "buffer_store_dwordx4 v[208:211], v242, s[56:59], s85 offen offset:0 sc1\n"
      "v_cvt_pk_bf16_f32 v214, v140, v141\n"
      "v_cvt_pk_bf16_f32 v215, v142, v143\n"
      "v_mfma_f32_16x16x32_bf16 v[144:147], v[176:179], a[32:35], 0\n"
      "buffer_store_dwordx4 v[212:215], v242, s[56:59], s85 offen offset:1024 sc1\n"
      "v_cvt_pk_bf16_f32 v218, v148, v149\n"
      "v_cvt_pk_bf16_f32 v219, v150, v151\n"
      "v_mfma_f32_16x16x32_bf16 v[152:155], v[176:179], a[48:51], 0\n"
      "buffer_store_dwordx4 v[216:219], v242, s[56:59], s85 offen offset:2048 sc1\n"
      "v_cvt_pk_bf16_f32 v222, v156, v157\n"
      "v_cvt_pk_bf16_f32 v223, v158, v159\n"
      "v_mfma_f32_16x16x32_bf16 v[160:163], v[176:179], a[64:67], 0\n"
      "buffer_store_dwordx4 v[220:223], v242, s[56:59], s85 offen offset:3072 sc1\n"
      "v_cvt_pk_bf16_f32 v226, v164, v165\n"
      "v_cvt_pk_bf16_f32 v227, v166, v167\n"
      "v_mfma_f32_16x16x32_bf16 v[168:171], v[176:179], a[80:83], 0\n"
      "s_waitcnt lgkmcnt(4)\n"
      "s_add_u32 s85, s85, 4096\n"
      "buffer_store_dwordx4 v[224:227], v242, s[56:59], s85 offen offset:0 sc1\n"
      "v_cvt_pk_bf16_f32 v230, v172, v173\n"
      "v_cvt_pk_bf16_f32 v231, v174, v175\n"
      "v_mfma_f32_16x16x32_bf16 v[132:135], v[180:183], a[0:3], 0\n"
      "ds_read_b64_tr_b16 v[192:193], v245 offset:16384\n"
      "buffer_store_dwordx4 v[228:231], v242, s[56:59], s85 offen offset:1024 sc1\n"
      "v_mfma_f32_16x16x32_bf16 v[140:143], v[180:183], a[16:19], 0\n"
      "ds_read_b64_tr_b16 v[194:195], v245 offset:20480\n"
      "v_mfma_f32_16x16x32_bf16 v[148:151], v[180:183], a[32:35], 0\n"
      "ds_read_b64_tr_b16 v[196:197], v246 offset:16384\n"
      "v_mfma_f32_16x16x32_bf16 v[156:159], v[180:183], a[48:51], 0\n"
      "ds_read_b64_tr_b16 v[198:199], v246 offset:20480\n"
      "v_mfma_f32_16x16x32_bf16 v[164:167], v[180:183], a[64:67], 0\n"
      "v_mfma_f32_16x16x32_bf16 v[172:175], v[180:183], a[80:83], 0\n"
      "s_waitcnt lgkmcnt(6)\n"
      "v_mfma_f32_16x16x32_bf16 v[128:131], v[184:187], a[4:7], v[128:131]\n"
      "ds_read_b64_tr_b16 v[200:201], v245 offset:24576\n"
      "v_mfma_f32_16x16x32_bf16 v[136:139], v[184:187], a[20:23], v[136:139]\n"
      "v_mfma_f32_16x16x32_bf16 v[144:147], v[184:187], a[36:39], v[144:147]\n"
      "ds_read_b64_tr_b16 v[202:203], v245 offset:28672\n"
      "v_mfma_f32_16x16x32_bf16 v[152:155], v[184:187], a[52:55], v[152:155]\n"
      "ds_read_b64_tr_b16 v[204:205], v246 offset:24576\n"
      "v_mfma_f32_16x16x32_bf16 v[160:163], v[184:187], a[68:71], v[160:163]\n"
      "v_mfma_f32_16x16x32_bf16 v[168:171], v[184:187], a[84:87], v[168:171]\n"
      "s_waitcnt lgkmcnt(7)\n"
      "ds_read_b64_tr_b16 v[206:207], v246 offset:28672\n"
      "v_mfma_f32_16x16x32_bf16 v[132:135], v[188:191], a[4:7], v[132:135]\n"
      "v_mfma_f32_16x16x32_bf16 v[140:143], v[188:191], a[20:23], v[140:143]\n"
      "v_mfma_f32_16x16x32_bf16 v[148:151], v[188:191], a[36:39], v[148:151]\n"
      "v_mfma_f32_16x16x32_bf16 v[156:159], v[188:191], a[52:55], v[156:159]\n"
      "v_mfma_f32_16x16x32_bf16 v[164:167], v[188:191], a[68:71], v[164:167]\n"
      "v_mfma_f32_16x16x32_bf16 v[172:175], v[188:191], a[84:87], v[172:175]\n"
      "s_waitcnt vmcnt(22) lgkmcnt(0)\n"
      "s_barrier\n"
      "v_mfma_f32_16x16x32_bf16 v[128:131], v[192:195], a[8:11], v[128:131]\n"
      "ds_read_b64_tr_b16 v[176:177], v243 offset:0\n"
      "v_mfma_f32_16x16x32_bf16 v[136:139], v[192:195], a[24:27], v[136:139]\n"
      "v_mfma_f32_16x16x32_bf16 v[144:147], v[192:195], a[40:43], v[144:147]\n"
      "v_mfma_f32_16x16x32_bf16 v[152:155], v[192:195], a[56:59], v[152:155]\n"
      "ds_read_b64_tr_b16 v[178:179], v243 offset:4096\n"
      "v_mfma_f32_16x16x32_bf16 v[160:163], v[192:195], a[72:75], v[160:163]\n"
      "v_mfma_f32_16x16x32_bf16 v[168:171], v[192:195], a[88:91], v[168:171]\n"
      "ds_read_b64_tr_b16 v[180:181], v244 offset:0\n"
      "v_mfma_f32_16x16x32_bf16 v[132:135], v[196:199], a[8:11], v[132:135]\n"
      "v_mfma_f32_16x16x32_bf16 v[140:143], v[196:199], a[24:27], v[140:143]\n"
      "v_mfma_f32_16x16x32_bf16 v[148:151], v[196:199], a[40:43], v[148:151]\n"
      "ds_read_b64_tr_b16 v[182:183], v244 offset:4096\n"
      "v_mfma_f32_16x16x32_bf16 v[156:159], v[196:199], a[56:59], v[156:159]\n"
      "v_mfma_f32_16x16x32_bf16 v[164:167], v[196:199], a[72:75], v[164:167]\n"
      "v_mfma_f32_16x16x32_bf16 v[172:175], v[196:199], a[88:91], v[172:175]\n"
      "v_mfma_f32_16x16x32_bf16 v[128:131], v[200:203], a[12:15], v[128:131]\n"
      "v_mfma_f32_16x16x32_bf16 v[136:139], v[200:203], a[28:31], v[136:139]\n"
      "ds_read_b64_tr_b16 v[184:185], v243 offset:8192\n"
      "v_mfma_f32_16x16x32_bf16 v[144:147], v[200:203], a[44:47], v[144:147]\n"
      "v_mfma_f32_16x16x32_bf16 v[152:155], v[200:203], a[60:63], v[152:155]\n"
      "ds_read_b64_tr_b16 v[186:187], v243 offset:12288\n"
      "v_mfma_f32_16x16x32_bf16 v[160:163], v[200:203], a[76:79], v[160:163]\n"
      "v_mfma_f32_16x16x32_bf16 v[168:171], v[200:203], a[92:95], v[168:171]\n"
      "s_mul_i32 s84, s83, 655360\n"
      "v_mfma_f32_16x16x32_bf16 v[132:135], v[204:207], a[12:15], v[132:135]\n"
      "ds_read_b64_tr_b16 v[188:189], v244 offset:8192\n"
      "v_mfma_f32_16x16x32_bf16 v[140:143], v[204:207], a[28:31], v[140:143]\n"
      "s_lshl_b32 s73, s82, 8\n"
      "v_mfma_f32_16x16x32_bf16 v[148:151], v[204:207], a[44:47], v[148:151]\n"
      "ds_read_b64_tr_b16 v[190:191], v244 offset:12288\n"
      "v_mfma_f32_16x16x32_bf16 v[156:159], v[204:207], a[60:63], v[156:159]\n"
      "s_add_u32 m0, s72, 98304\n"
      "v_mfma_f32_16x16x32_bf16 v[164:167], v[204:207], a[76:79], v[164:167]\n"
      "v_mfma_f32_16x16x32_bf16 v[172:175], v[204:207], a[92:95], v[172:175]\n"
      "s_waitcnt lgkmcnt(6)\n"
      "v_mfma_f32_16x16x32_bf16 v[128:131], v[176:179], a[96:99], v[128:131]\n"
      "s_add_u32 s84, s84, s73\n"
      "v_mfma_f32_16x16x32_bf16 v[136:139], v[176:179], a[112:115], v[136:139]\n"
      "ds_read_b64_tr_b16 v[192:193], v243 offset:16384\n"
      "v_mfma_f32_16x16x32_bf16 v[144:147], v[176:179], a[128:131], v[144:147]\n"
      "buffer_load_dwordx4 v247, s[52:55], s84 offen offset:0  lds\n"
      "v_mfma_f32_16x16x32_bf16 v[152:155], v[176:179], a[144:147], v[152:155]\n"
      "ds_read_b64_tr_b16 v[194:195], v243 offset:20480\n"
      "v_mfma_f32_16x16x32_bf16 v[160:163], v[176:179], a[160:163], v[160:163]\n"
      "v_mfma_f32_16x16x32_bf16 v[168:171], v[176:179], a[176:179], v[168:171]\n"
      "s_waitcnt lgkmcnt(6)\n"
      "buffer_load_dwordx4 v248, s[52:55], s84 offen offset:1024  lds\n"
      "v_mfma_f32_16x16x32_bf16 v[132:135], v[180:183], a[96:99], v[132:135]\n"
      "ds_read_b64_tr_b16 v[196:197], v244 offset:16384\n"
      "v_mfma_f32_16x16x32_bf16 v[140:143], v[180:183], a[112:115], v[140:143]\n"
      "buffer_load_dwordx4 v249, s[52:55], s84 offen offset:2048  lds\n"
      "v_mfma_f32_16x16x32_bf16 v[148:151], v[180:183], a[128:131], v[148:151]\n"
      "ds_read_b64_tr_b16 v[198:199], v244 offset:20480\n"
      "v_mfma_f32_16x16x32_bf16 v[156:159], v[180:183], a[144:147], v[156:159]\n"
      "buffer_load_dwordx4 v250, s[52:55], s84 offen offset:3072  lds\n"
      "v_mfma_f32_16x16x32_bf16 v[164:167], v[180:183], a[160:163], v[164:167]\n"
      "v_mfma_f32_16x16x32_bf16 v[172:175], v[180:183], a[176:179], v[172:175]\n"
      "s_waitcnt lgkmcnt(6)\n"
      "v_mfma_f32_16x16x32_bf16 v[128:131], v[184:187], a[100:103], v[128:131]\n"
      "s_add_u32 m0, s72, 102400\n"
      "ds_read_b64_tr_b16 v[200:201], v243 offset:24576\n"
      "v_mfma_f32_16x16x32_bf16 v[136:139], v[184:187], a[116:119], v[136:139]\n"
      "v_mfma_f32_16x16x32_bf16 v[144:147], v[184:187], a[132:135], v[144:147]\n"
      "s_add_u32 s84, s84, 20480\n"
      "ds_read_b64_tr_b16 v[202:203], v243 offset:28672\n"
      "v_mfma_f32_16x16x32_bf16 v[152:155], v[184:187], a[148:151], v[152:155]\n"
      "ds_read_b64_tr_b16 v[204:205], v244 offset:24576\n"
      "v_mfma_f32_16x16x32_bf16 v[160:163], v[184:187], a[164:167], v[160:163]\n"
      "buffer_load_dwordx4 v247, s[52:55], s84 offen offset:0  lds\n"
      "v_mfma_f32_16x16x32_bf16 v[168:171], v[184:187], a[180:183], v[168:171]\n"
      "s_waitcnt lgkmcnt(7)\n"
      "ds_read_b64_tr_b16 v[206:207], v244 offset:28672\n"
      "v_mfma_f32_16x16x32_bf16 v[132:135], v[188:191], a[100:103], v[132:135]\n"
      "v_mov_b32 v241, 2\n"
      "v_mfma_f32_16x16x32_bf16 v[140:143], v[188:191], a[116:119], v[140:143]\n"
      "buffer_load_dwordx4 v248, s[52:55], s84 offen offset:1024  lds\n"
      "v_mfma_f32_16x16x32_bf16 v[148:151], v[188:191], a[132:135], v[148:151]\n"
      "v_mfma_f32_16x16x32_bf16 v[156:159], v[188:191], a[148:151], v[156:159]\n"
      "buffer_load_dwordx4 v249, s[52:55], s84 offen offset:2048  lds\n"
      "v_mfma_f32_16x16x32_bf16 v[164:167], v[188:191], a[164:167], v[164:167]\n"
      "v_mfma_f32_16x16x32_bf16 v[172:175], v[188:191], a[180:183], v[172:175]\n"
      "s_waitcnt vmcnt(7) lgkmcnt(0)\n"
      "s_barrier\n"
      "buffer_load_dwordx4 v250, s[52:55], s84 offen offset:3072  lds\n"
      "v_mfma_f32_16x16x32_bf16 v[128:131], v[192:195], a[104:107], v[128:131]\n"
      "ds_read_b64_tr_b16 v[176:177], v245 offset:32768\n"
      "v_mfma_f32_16x16x32_bf16 v[136:139], v[192:195], a[120:123], v[136:139]\n"
      "buffer_store_dword v241, v254, s[64:67], 0 offen offset:0 sc1\n"
      "v_mfma_f32_16x16x32_bf16 v[144:147], v[192:195], a[136:139], v[144:147]\n"
      "s_add_u32 s83, s83, 1\n"
      "v_mfma_f32_16x16x32_bf16 v[152:155], v[192:195], a[152:155], v[152:155]\n"
      "ds_read_b64_tr_b16 v[178:179], v245 offset:36864\n"
      "v_mfma_f32_16x16x32_bf16 v[160:163], v[192:195], a[168:171], v[160:163]\n"
      "s_and_b32 s83, s83, 3\n"
      "v_mfma_f32_16x16x32_bf16 v[168:171], v[192:195], a[184:187], v[168:171]\n"
      "ds_read_b64_tr_b16 v[180:181], v246 offset:32768\n"
      "v_mfma_f32_16x16x32_bf16 v[132:135], v[196:199], a[104:107], v[132:135]\n"
      "v_mfma_f32_16x16x32_bf16 v[140:143], v[196:199], a[120:123], v[140:143]\n"
      "s_mul_i32 s84, s83, 655360\n"
      "v_mfma_f32_16x16x32_bf16 v[148:151], v[196:199], a[136:139], v[148:151]\n"
      "ds_read_b64_tr_b16 v[182:183], v246 offset:36864\n"
      "v_mfma_f32_16x16x32_bf16 v[156:159], v[196:199], a[152:155], v[156:159]\n"
      "s_lshl_b32 s73, s82, 8\n"
      "v_mfma_f32_16x16x32_bf16 v[164:167], v[196:199], a[168:171], v[164:167]\n"
      "v_mfma_f32_16x16x32_bf16 v[172:175], v[196:199], a[184:187], v[172:175]\n"
      "s_add_u32 m0, s72, 0\n"
      "v_mfma_f32_16x16x32_bf16 v[128:131], v[200:203], a[108:111], v[128:131]\n"
      "v_mfma_f32_16x16x32_bf16 v[136:139], v[200:203], a[124:127], v[136:139]\n"
      "ds_read_b64_tr_b16 v[184:185], v245 offset:40960\n"
      "v_mfma_f32_16x16x32_bf16 v[144:147], v[200:203], a[140:143], v[144:147]\n"
      "s_add_u32 s84, s84, s73\n"
      "v_mfma_f32_16x16x32_bf16 v[152:155], v[200:203], a[156:159], v[152:155]\n"
      "ds_read_b64_tr_b16 v[186:187], v245 offset:45056\n"
      "v_mfma_f32_16x16x32_bf16 v[160:163], v[200:203], a[172:175], v[160:163]\n"
      "buffer_load_dwordx4 v247, s[52:55], s84 offen offset:0  lds\n"
      "v_mfma_f32_16x16x32_bf16 v[168:171], v[200:203], a[188:191], v[168:171]\n"
      "v_mfma_f32_16x16x32_bf16 v[132:135], v[204:207], a[108:111], v[132:135]\n"
      "ds_read_b64_tr_b16 v[188:189], v246 offset:40960\n"
      "v_mfma_f32_16x16x32_bf16 v[140:143], v[204:207], a[124:127], v[140:143]\n"
      "buffer_load_dwordx4 v248, s[52:55], s84 offen offset:1024  lds\n"
      "v_mfma_f32_16x16x32_bf16 v[148:151], v[204:207], a[140:143], v[148:151]\n"
      "ds_read_b64_tr_b16 v[190:191], v246 offset:45056\n"
      "v_mfma_f32_16x16x32_bf16 v[156:159], v[204:207], a[156:159], v[156:159]\n"
      "buffer_load_dwordx4 v249, s[52:55], s84 offen offset:2048  lds\n"
      "v_mfma_f32_16x16x32_bf16 v[164:167], v[204:207], a[172:175], v[164:167]\n"
      "v_mfma_f32_16x16x32_bf16 v[172:175], v[204:207], a[188:191], v[172:175]\n"
      "s_waitcnt lgkmcnt(6)\n"
      "buffer_load_dwordx4 v250, s[52:55], s84 offen offset:3072  lds\n"
      "v_mfma_f32_16x16x32_bf16 v[128:131], v[176:179], a[192:195], v[128:131]\n"
      "v_mfma_f32_16x16x32_bf16 v[136:139], v[176:179], a[208:211], v[136:139]\n"
      "ds_read_b64_tr_b16 v[192:193], v245 offset:49152\n"
      "v_mfma_f32_16x16x32_bf16 v[144:147], v[176:179], a[224:227], v[144:147]\n"
      "s_add_u32 m0, s72, 4096\n"
      "v_mfma_f32_16x16x32_bf16 v[152:155], v[176:179], a[240:243], v[152:155]\n"
      "ds_read_b64_tr_b16 v[194:195], v245 offset:53248\n"
      "v_mfma_f32_16x16x32_bf16 v[160:163], v[176:179], v[0:3], v[160:163]\n"
      "s_add_u32 s84, s84, 20480\n"
      "v_mfma_f32_16x16x32_bf16 v[168:171], v[176:179], v[16:19], v[168:171]\n"
      "s_waitcnt lgkmcnt(6)\n"
      "v_mfma_f32_16x16x32_bf16 v[132:135], v[180:183], a[192:195], v[132:135]\n"
      "ds_read_b64_tr_b16 v[196:197], v246 offset:49152\n"
      "v_mfma_f32_16x16x32_bf16 v[140:143], v[180:183], a[208:211], v[140:143]\n"
      "buffer_load_dwordx4 v247, s[52:55], s84 offen offset:0  lds\n"
      "v_mfma_f32_16x16x32_bf16 v[148:151], v[180:183], a[224:227], v[148:151]\n"
      "ds_read_b64_tr_b16 v[198:199], v246 offset:53248\n"
      "v_mfma_f32_16x16x32_bf16 v[156:159], v[180:183], a[240:243], v[156:159]\n"
      "buffer_load_dwordx4 v248, s[52:55], s84 offen offset:1024  lds\n"
      "v_mfma_f32_16x16x32_bf16 v[164:167], v[180:183], v[0:3], v[164:167]\n"
      "v_mfma_f32_16x16x32_bf16 v[172:175], v[180:183], v[16:19], v[172:175]\n"
      "s_waitcnt lgkmcnt(6)\n"
      "buffer_load_dwordx4 v249, s[52:55], s84 offen offset:2048  lds\n"
      "v_mfma_f32_16x16x32_bf16 v[128:131], v[184:187], a[196:199], v[128:131]\n"
      "ds_read_b64_tr_b16 v[200:201], v245 offset:57344\n"
      "v_mfma_f32_16x16x32_bf16 v[136:139], v[184:187], a[212:215], v[136:139]\n"
      "buffer_load_dwordx4 v250, s[52:55], s84 offen offset:3072  lds\n"
      "v_mfma_f32_16x16x32_bf16 v[144:147], v[184:187], a[228:231], v[144:147]\n"
      "ds_read_b64_tr_b16 v[202:203], v245 offset:61440\n"
      "v_mfma_f32_16x16x32_bf16 v[152:155], v[184:187], a[244:247], v[152:155]\n"
      "ds_read_b64_tr_b16 v[204:205], v246 offset:57344\n"
      "v_mfma_f32_16x16x32_bf16 v[160:163], v[184:187], v[4:7], v[160:163]\n"
      "s_add_u32 s83, s83, 1\n"
      "v_mfma_f32_16x16x32_bf16 v[168:171], v[184:187], v[20:23], v[168:171]\n"
      "s_waitcnt lgkmcnt(7)\n"
      "ds_read_b64_tr_b16 v[206:207], v246 offset:61440\n"
      "v_mfma_f32_16x16x32_bf16 v[132:135], v[188:191], a[196:199], v[132:135]\n"
      "s_and_b32 s83, s83, 3\n"
      "v_mfma_f32_16x16x32_bf16 v[140:143], v[188:191], a[212:215], v[140:143]\n"
      "v_mfma_f32_16x16x32_bf16 v[148:151], v[188:191], a[228:231], v[148:151]\n"
      "v_mfma_f32_16x16x32_bf16 v[156:159], v[188:191], a[244:247], v[156:159]\n"
      "v_mfma_f32_16x16x32_bf16 v[164:167], v[188:191], v[4:7], v[164:167]\n"
      "v_mfma_f32_16x16x32_bf16 v[172:175], v[188:191], v[20:23], v[172:175]\n"
      "s_waitcnt lgkmcnt(0)\n"
      "s_barrier\n"
      "v_mfma_f32_16x16x32_bf16 v[128:131], v[192:195], a[200:203], v[128:131]\n"
      "s_mul_i32 s84, s83, 655360\n"
      "ds_read_b64_tr_b16 v[176:177], v243 offset:32768\n"
      "v_mfma_f32_16x16x32_bf16 v[136:139], v[192:195], a[216:219], v[136:139]\n"
      "v_mfma_f32_16x16x32_bf16 v[144:147], v[192:195], a[232:235], v[144:147]\n"
      "v_mfma_f32_16x16x32_bf16 v[152:155], v[192:195], a[248:251], v[152:155]\n"
      "s_lshl_b32 s73, s82, 8\n"
      "ds_read_b64_tr_b16 v[178:179], v243 offset:36864\n"
      "v_mfma_f32_16x16x32_bf16 v[160:163], v[192:195], v[8:11], v[160:163]\n"
      "v_mfma_f32_16x16x32_bf16 v[168:171], v[192:195], v[24:27], v[168:171]\n"
      "ds_read_b64_tr_b16 v[180:181], v244 offset:32768\n"
      "v_mfma_f32_16x16x32_bf16 v[132:135], v[196:199], a[200:203], v[132:135]\n"
      "s_add_u32 m0, s72, 131072\n"
      "v_mfma_f32_16x16x32_bf16 v[140:143], v[196:199], a[216:219], v[140:143]\n"
      "v_mfma_f32_16x16x32_bf16 v[148:151], v[196:199], a[232:235], v[148:151]\n"
      "ds_read_b64_tr_b16 v[182:183], v244 offset:36864\n"
      "v_mfma_f32_16x16x32_bf16 v[156:159], v[196:199], a[248:251], v[156:159]\n"
      "s_add_u32 s84, s84, s73\n"
      "v_mfma_f32_16x16x32_bf16 v[164:167], v[196:199], v[8:11], v[164:167]\n"
      "v_mfma_f32_16x16x32_bf16 v[172:175], v[196:199], v[24:27], v[172:175]\n"
      "v_mfma_f32_16x16x32_bf16 v[128:131], v[200:203], a[204:207], v[128:131]\n"
      "buffer_load_dwordx4 v247, s[52:55], s84 offen offset:0  lds\n"
      "v_mfma_f32_16x16x32_bf16 v[136:139], v[200:203], a[220:223], v[136:139]\n"
      "ds_read_b64_tr_b16 v[184:185], v243 offset:40960\n"
      "v_mfma_f32_16x16x32_bf16 v[144:147], v[200:203], a[236:239], v[144:147]\n"
      "buffer_load_dwordx4 v248, s[52:55], s84 offen offset:1024  lds\n"
      "v_mfma_f32_16x16x32_bf16 v[152:155], v[200:203], a[252:255], v[152:155]\n"
      "ds_read_b64_tr_b16 v[186:187], v243 offset:45056\n"
      "v_mfma_f32_16x16x32_bf16 v[160:163], v[200:203], v[12:15], v[160:163]\n"
      "v_mfma_f32_16x16x32_bf16 v[168:171], v[200:203], v[28:31], v[168:171]\n"
      "buffer_load_dwordx4 v249, s[52:55], s84 offen offset:2048  lds\n"
      "v_mfma_f32_16x16x32_bf16 v[132:135], v[204:207], a[204:207], v[132:135]\n"
      "ds_read_b64_tr_b16 v[188:189], v244 offset:40960\n"
      "v_mfma_f32_16x16x32_bf16 v[140:143], v[204:207], a[220:223], v[140:143]\n"
      "v_mfma_f32_16x16x32_bf16 v[148:151], v[204:207], a[236:239], v[148:151]\n"
      "ds_read_b64_tr_b16 v[190:191], v244 offset:45056\n"
      "v_mfma_f32_16x16x32_bf16 v[156:159], v[204:207], a[252:255], v[156:159]\n"
      "buffer_load_dwordx4 v250, s[52:55], s84 offen offset:3072  lds\n"
      "v_mfma_f32_16x16x32_bf16 v[164:167], v[204:207], v[12:15], v[164:167]\n"
      "v_mfma_f32_16x16x32_bf16 v[172:175], v[204:207], v[28:31], v[172:175]\n"
      "s_waitcnt lgkmcnt(6)\n"
      "v_mfma_f32_16x16x32_bf16 v[128:131], v[176:179], v[32:35], v[128:131]\n"
      "s_add_u32 m0, s72, 135168\n"
      "v_mfma_f32_16x16x32_bf16 v[136:139], v[176:179], v[48:51], v[136:139]\n"
      "ds_read_b64_tr_b16 v[192:193], v243 offset:49152\n"
      "v_mfma_f32_16x16x32_bf16 v[144:147], v[176:179], v[64:67], v[144:147]\n"
      "s_mul_i32 s85, s86, 24576\n"
      "v_mfma_f32_16x16x32_bf16 v[152:155], v[176:179], v[80:83], v[152:155]\n"
      "s_add_u32 s84, s84, 20480\n"
      "ds_read_b64_tr_b16 v[194:195], v243 offset:53248\n"
      "v_mfma_f32_16x16x32_bf16 v[160:163], v[176:179], v[96:99], v[160:163]\n"
      "v_mfma_f32_16x16x32_bf16 v[168:171], v[176:179], v[112:115], v[168:171]\n"
      "s_waitcnt lgkmcnt(6)\n"
      "buffer_load_dwordx4 v247, s[52:55], s84 offen offset:0  lds\n"
      "v_mfma_f32_16x16x32_bf16 v[132:135], v[180:183], v[32:35], v[132:135]\n"
      "ds_read_b64_tr_b16 v[196:197], v244 offset:49152\n"
      "v_mfma_f32_16x16x32_bf16 v[140:143], v[180:183], v[48:51], v[140:143]\n"
      "v_mfma_f32_16x16x32_bf16 v[148:151], v[180:183], v[64:67], v[148:151]\n"
      "s_mul_i32 s74, %8, 6144\n"
      "ds_read_b64_tr_b16 v[198:199], v244 offset:53248\n"
      "v_mfma_f32_16x16x32_bf16 v[156:159], v[180:183], v[80:83], v[156:159]\n"
      "buffer_load_dwordx4 v248, s[52:55], s84 offen offset:1024  lds\n"
      "v_mfma_f32_16x16x32_bf16 v[164:167], v[180:183], v[96:99], v[164:167]\n"
      "v_mfma_f32_16x16x32_bf16 v[172:175], v[180:183], v[112:115], v[172:175]\n"
      "s_waitcnt lgkmcnt(6)\n"
      "buffer_load_dwordx4 v249, s[52:55], s84 offen offset:2048  lds\n"
      "v_mfma_f32_16x16x32_bf16 v[128:131], v[184:187], v[36:39], v[128:131]\n"
      "ds_read_b64_tr_b16 v[200:201], v243 offset:57344\n"
      "v_mfma_f32_16x16x32_bf16 v[136:139], v[184:187], v[52:55], v[136:139]\n"
      "v_mfma_f32_16x16x32_bf16 v[144:147], v[184:187], v[68:71], v[144:147]\n"
      "s_add_u32 s85, s85, s74\n"
      "ds_read_b64_tr_b16 v[202:203], v243 offset:61440\n"
      "v_mfma_f32_16x16x32_bf16 v[152:155], v[184:187], v[84:87], v[152:155]\n"
      "ds_read_b64_tr_b16 v[204:205], v244 offset:57344\n"
      "v_mfma_f32_16x16x32_bf16 v[160:163], v[184:187], v[100:103], v[160:163]\n"
      "buffer_load_dwordx4 v250, s[52:55], s84 offen offset:3072  lds\n"
      "v_mfma_f32_16x16x32_bf16 v[168:171], v[184:187], v[116:119], v[168:171]\n"
      "s_waitcnt lgkmcnt(7)\n"
      "ds_read_b64_tr_b16 v[206:207], v244 offset:61440\n"
      "v_mfma_f32_16x16x32_bf16 v[132:135], v[188:191], v[36:39], v[132:135]\n"
      "s_add_u32 s83, s83, 1\n"
      "v_mfma_f32_16x16x32_bf16 v[140:143], v[188:191], v[52:55], v[140:143]\n"
      "s_mul_i32 s74, %13, 122880\n"
      "v_mfma_f32_16x16x32_bf16 v[148:151], v[188:191], v[68:71], v[148:151]\n"
      "v_mfma_f32_16x16x32_bf16 v[156:159], v[188:191], v[84:87], v[156:159]\n"
      "s_and_b32 s83, s83, 3\n"
      "v_mfma_f32_16x16x32_bf16 v[164:167], v[188:191], v[100:103], v[164:167]\n"
      "v_mfma_f32_16x16x32_bf16 v[172:175], v[188:191], v[116:119], v[172:175]\n"
      "s_waitcnt vmcnt(17) lgkmcnt(0)\n"
      "s_barrier\n"
      "s_mul_i32 s84, s83, 655360\n"
      "v_mfma_f32_16x16x32_bf16 v[128:131], v[192:195], v[40:43], v[128:131]\n"
      "ds_read_b64_tr_b16 v[176:177], v245 offset:0\n"
      "v_mfma_f32_16x16x32_bf16 v[136:139], v[192:195], v[56:59], v[136:139]\n"
      "s_lshl_b32 s73, s82, 8\n"
      "s_add_u32 s85, s85, s74\n"
      "v_mfma_f32_16x16x32_bf16 v[144:147], v[192:195], v[72:75], v[144:147]\n"
      "s_add_u32 m0, s72, 32768\n"
      "v_mfma_f32_16x16x32_bf16 v[152:155], v[192:195], v[88:91], v[152:155]\n"
      "s_add_u32 s84, s84, s73\n"
      "ds_read_b64_tr_b16 v[178:179], v245 offset:4096\n"
      "v_mfma_f32_16x16x32_bf16 v[160:163], v[192:195], v[104:107], v[160:163]\n"
      "buffer_load_dwordx4 v247, s[52:55], s84 offen offset:0  lds\n"
      "v_mfma_f32_16x16x32_bf16 v[168:171], v[192:195], v[120:123], v[168:171]\n"
      "ds_read_b64_tr_b16 v[180:181], v246 offset:0\n"
      "v_mfma_f32_16x16x32_bf16 v[132:135], v[196:199], v[40:43], v[132:135]\n"
      "buffer_load_dwordx4 v248, s[52:55], s84 offen offset:1024  lds\n"
      "v_mfma_f32_16x16x32_bf16 v[140:143], v[196:199], v[56:59], v[140:143]\n"
      "s_add_u32 s86, s86, 1\n"
      "buffer_load_dwordx4 v249, s[52:55], s84 offen offset:2048  lds\n"
      "v_mfma_f32_16x16x32_bf16 v[148:151], v[196:199], v[72:75], v[148:151]\n"
      "ds_read_b64_tr_b16 v[182:183], v246 offset:4096\n"
      "v_mfma_f32_16x16x32_bf16 v[156:159], v[196:199], v[88:91], v[156:159]\n"
      "buffer_load_dwordx4 v250, s[52:55], s84 offen offset:3072  lds\n"
      "v_mfma_f32_16x16x32_bf16 v[164:167], v[196:199], v[104:107], v[164:167]\n"
      "s_add_u32 m0, s72, 36864\n"
      "s_add_u32 s84, s84, 20480\n"
      "buffer_load_dwordx4 v247, s[52:55], s84 offen offset:0  lds\n"
      "v_mfma_f32_16x16x32_bf16 v[172:175], v[196:199], v[120:123], v[172:175]\n"
      "buffer_load_dwordx4 v248, s[52:55], s84 offen offset:1024  lds\n"
      "v_mfma_f32_16x16x32_bf16 v[128:131], v[200:203], v[44:47], v[128:131]\n"
      "buffer_load_dwordx4 v249, s[52:55], s84 offen offset:2048  lds\n"
      "v_mfma_f32_16x16x32_bf16 v[136:139], v[200:203], v[60:63], v[136:139]\n"
      "s_cmp_eq_u32 s86, 5\n"
      "s_cselect_b32 s86, 0, s86\n"
      "ds_read_b64_tr_b16 v[184:185], v245 offset:8192\n"
      "v_mfma_f32_16x16x32_bf16 v[144:147], v[200:203], v[76:79], v[144:147]\n"
      "buffer_load_dwordx4 v250, s[52:55], s84 offen offset:3072  lds\n"
      "v_mfma_f32_16x16x32_bf16 v[152:155], v[200:203], v[92:95], v[152:155]\n"
      "ds_read_b64_tr_b16 v[186:187], v245 offset:12288\n"
      "v_mfma_f32_16x16x32_bf16 v[160:163], v[200:203], v[108:111], v[160:163]\n"
      "v_cvt_pk_bf16_f32 v208, v128, v129\n"
      "v_cvt_pk_bf16_f32 v209, v130, v131\n"
      "v_mfma_f32_16x16x32_bf16 v[168:171], v[200:203], v[124:127], v[168:171]\n"
      "s_add_u32 s90, %8, 0\n"
      "v_cvt_pk_bf16_f32 v212, v136, v137\n"
      "v_cvt_pk_bf16_f32 v213, v138, v139\n"
      "v_mfma_f32_16x16x32_bf16 v[132:135], v[204:207], v[44:47], v[132:135]\n"
      "s_add_u32 s83, s83, 1\n"
      "s_and_b32 s83, s83, 3\n"
      "ds_read_b64_tr_b16 v[188:189], v246 offset:8192\n"
      "v_mfma_f32_16x16x32_bf16 v[140:143], v[204:207], v[60:63], v[140:143]\n"
      "s_add_u32 s82, s82, 1\n"
      "buffer_load_dword v240, v253, s[64:67], 0 offen offset:0 sc1\n"
      "v_cvt_pk_bf16_f32 v216, v144, v145\n"
      "v_cvt_pk_bf16_f32 v217, v146, v147\n"
      "v_mfma_f32_16x16x32_bf16 v[148:151], v[204:207], v[76:79], v[148:151]\n"
      "s_cmp_eq_u32 s82, 5\n"
      "s_cselect_b32 s82, 0, s82\n"
      "ds_read_b64_tr_b16 v[190:191], v246 offset:12288\n"
      "v_mfma_f32_16x16x32_bf16 v[156:159], v[204:207], v[92:95], v[156:159]\n"
      "s_min_u32 s90, s90, 14\n"
      "v_cvt_pk_bf16_f32 v220, v152, v153\n"
      "v_cvt_pk_bf16_f32 v221, v154, v155\n"
      "v_mfma_f32_16x16x32_bf16 v[164:167], v[204:207], v[108:111], v[164:167]\n"
      "s_mul_i32 s88, s90, 43\n"
      "v_cvt_pk_bf16_f32 v224, v160, v161\n"
      "v_cvt_pk_bf16_f32 v225, v162, v163\n"
      "v_mfma_f32_16x16x32_bf16 v[172:175], v[204:207], v[124:127], v[172:175]\n"
      "s_waitcnt lgkmcnt(6)\n"
      "v_cvt_pk_bf16_f32 v228, v168, v169\n"
      "v_cvt_pk_bf16_f32 v229, v170, v171\n"
      "v_mfma_f32_16x16x32_bf16 v[128:131], v[176:179], a[0:3], 0\n"
      "v_cvt_pk_bf16_f32 v210, v132, v133\n"
      "v_cvt_pk_bf16_f32 v211, v134, v135\n"
      "v_mfma_f32_16x16x32_bf16 v[136:139], v[176:179], a[16:19], 0\n"
      "s_lshr_b32 s88, s88, 7\n"
      "buffer_store_dwordx4 v[208:211], v242, s[56:59], s85 offen offset:0 sc1\n"
      "v_cvt_pk_bf16_f32 v214, v140, v141\n"
      "v_cvt_pk_bf16_f32 v215, v142, v143\n"
      "v_mfma_f32_16x16x32_bf16 v[144:147], v[176:179], a[32:35], 0\n"
      "buffer_store_dwordx4 v[212:215], v242, s[56:59], s85 offen offset:1024 sc1\n"
      "v_cvt_pk_bf16_f32 v218, v148, v149\n"
      "v_cvt_pk_bf16_f32 v219, v150, v151\n"
      "v_mfma_f32_16x16x32_bf16 v[152:155], v[176:179], a[48:51], 0\n"
      "buffer_store_dwordx4 v[216:219], v242, s[56:59], s85 offen offset:2048 sc1\n"
      "v_cvt_pk_bf16_f32 v222, v156, v157\n"
      "v_cvt_pk_bf16_f32 v223, v158, v159\n"
      "v_mfma_f32_16x16x32_bf16 v[160:163], v[176:179], a[64:67], 0\n"
      "s_mul_i32 s79, s88, 3\n"
      "buffer_store_dwordx4 v[220:223], v242, s[56:59], s85 offen offset:3072 sc1\n"
      "v_cvt_pk_bf16_f32 v226, v164, v165\n"
      "v_cvt_pk_bf16_f32 v227, v166, v167\n"
      "v_mfma_f32_16x16x32_bf16 v[168:171], v[176:179], a[80:83], 0\n"
      "s_waitcnt lgkmcnt(4)\n"
      "s_add_u32 s85, s85, 4096\n"
      "buffer_store_dwordx4 v[224:227], v242, s[56:59], s85 offen offset:0 sc1\n"
      "v_cvt_pk_bf16_f32 v230, v172, v173\n"
      "v_cvt_pk_bf16_f32 v231, v174, v175\n"
      "v_mfma_f32_16x16x32_bf16 v[132:135], v[180:183], a[0:3], 0\n"
      "ds_read_b64_tr_b16 v[192:193], v245 offset:16384\n"
      "buffer_store_dwordx4 v[228:231], v242, s[56:59], s85 offen offset:1024 sc1\n"
      "v_mfma_f32_16x16x32_bf16 v[140:143], v[180:183], a[16:19], 0\n"
      "s_sub_u32 s90, s90, s79\n"
      "ds_read_b64_tr_b16 v[194:195], v245 offset:20480\n"
      "v_mfma_f32_16x16x32_bf16 v[148:151], v[180:183], a[32:35], 0\n"
      "ds_read_b64_tr_b16 v[196:197], v246 offset:16384\n"
      "v_mfma_f32_16x16x32_bf16 v[156:159], v[180:183], a[48:51], 0\n"
      "ds_read_b64_tr_b16 v[198:199], v246 offset:20480\n"
      "v_mfma_f32_16x16x32_bf16 v[164:167], v[180:183], a[64:67], 0\n"
      "s_mul_i32 s79, %13, 3\n"
      "v_mfma_f32_16x16x32_bf16 v[172:175], v[180:183], a[80:83], 0\n"
      "s_waitcnt lgkmcnt(6)\n"
      "v_mfma_f32_16x16x32_bf16 v[128:131], v[184:187], a[4:7], v[128:131]\n"
      "s_add_u32 s90, s90, s79\n"
      "ds_read_b64_tr_b16 v[200:201], v245 offset:24576\n"
      "v_mfma_f32_16x16x32_bf16 v[136:139], v[184:187], a[20:23], v[136:139]\n"
      "v_mfma_f32_16x16x32_bf16 v[144:147], v[184:187], a[36:39], v[144:147]\n"
      "ds_read_b64_tr_b16 v[202:203], v245 offset:28672\n"
      "v_mfma_f32_16x16x32_bf16 v[152:155], v[184:187], a[52:55], v[152:155]\n"
      "s_add_u32 s88, s88, %11\n"
      "ds_read_b64_tr_b16 v[204:205], v246 offset:24576\n"
      "v_mfma_f32_16x16x32_bf16 v[160:163], v[184:187], a[68:71], v[160:163]\n"
      "v_mfma_f32_16x16x32_bf16 v[168:171], v[184:187], a[84:87], v[168:171]\n"
      "s_waitcnt lgkmcnt(7)\n"
      "ds_read_b64_tr_b16 v[206:207], v246 offset:28672\n"
      "v_mfma_f32_16x16x32_bf16 v[132:135], v[188:191], a[4:7], v[132:135]\n"
      "s_sub_u32 s79, s88, 5\n"
      "v_mfma_f32_16x16x32_bf16 v[140:143], v[188:191], a[20:23], v[140:143]\n"
      "v_mfma_f32_16x16x32_bf16 v[148:151], v[188:191], a[36:39], v[148:151]\n"
      "v_mfma_f32_16x16x32_bf16 v[156:159], v[188:191], a[52:55], v[156:159]\n"
      "s_min_u32 s88, s88, s79\n"
      "v_mfma_f32_16x16x32_bf16 v[164:167], v[188:191], a[68:71], v[164:167]\n"
      "v_mfma_f32_16x16x32_bf16 v[172:175], v[188:191], a[84:87], v[172:175]\n"
      "s_waitcnt vmcnt(23) lgkmcnt(0)\n"
      "s_barrier\n"
      "v_mfma_f32_16x16x32_bf16 v[128:131], v[192:195], a[8:11], v[128:131]\n"
      "s_mul_i32 s79, s90, 40960\n"
      "ds_read_b64_tr_b16 v[176:177], v243 offset:0\n"
      "v_mfma_f32_16x16x32_bf16 v[136:139], v[192:195], a[24:27], v[136:139]\n"
      "v_mfma_f32_16x16x32_bf16 v[144:147], v[192:195], a[40:43], v[144:147]\n"
      "s_lshl_b32 s90, s90, 10\n"
      "v_mfma_f32_16x16x32_bf16 v[152:155], v[192:195], a[56:59], v[152:155]\n"
      "ds_read_b64_tr_b16 v[178:179], v243 offset:4096\n"
      "v_mfma_f32_16x16x32_bf16 v[160:163], v[192:195], a[72:75], v[160:163]\n"
      "v_mfma_f32_16x16x32_bf16 v[168:171], v[192:195], a[88:91], v[168:171]\n"
      "s_mul_i32 s78, s88, 24576\n"
      "ds_read_b64_tr_b16 v[180:181], v244 offset:0\n"
      "v_mfma_f32_16x16x32_bf16 v[132:135], v[196:199], a[8:11], v[132:135]\n"
      "v_mfma_f32_16x16x32_bf16 v[140:143], v[196:199], a[24:27], v[140:143]\n"
      "v_mfma_f32_16x16x32_bf16 v[148:151], v[196:199], a[40:43], v[148:151]\n"
      "s_add_u32 s90, s90, s78\n"
      "ds_read_b64_tr_b16 v[182:183], v244 offset:4096\n"
      "v_mfma_f32_16x16x32_bf16 v[156:159], v[196:199], a[56:59], v[156:159]\n"
      "v_mfma_f32_16x16x32_bf16 v[164:167], v[196:199], a[72:75], v[164:167]\n"
      "v_mfma_f32_16x16x32_bf16 v[172:175], v[196:199], a[88:91], v[172:175]\n"
      "s_lshl_b32 s88, s88, 8\n"
      "v_mfma_f32_16x16x32_bf16 v[128:131], v[200:203], a[12:15], v[128:131]\n"
      "v_mfma_f32_16x16x32_bf16 v[136:139], v[200:203], a[28:31], v[136:139]\n"
      "ds_read_b64_tr_b16 v[184:185], v243 offset:8192\n"
      "v_mfma_f32_16x16x32_bf16 v[144:147], v[200:203], a[44:47], v[144:147]\n"
      "s_add_u32 s79, s79, s88\n"
      "v_mfma_f32_16x16x32_bf16 v[152:155], v[200:203], a[60:63], v[152:155]\n"
      "ds_read_b64_tr_b16 v[186:187], v243 offset:12288\n"
      "v_mfma_f32_16x16x32_bf16 v[160:163], v[200:203], a[76:79], v[160:163]\n"
      "s_lshr_b32 s88, %13, 1\n"
      "v_mfma_f32_16x16x32_bf16 v[168:171], v[200:203], a[92:95], v[168:171]\n"
      "v_mfma_f32_16x16x32_bf16 v[132:135], v[204:207], a[12:15], v[132:135]\n"
      "s_waitcnt vmcnt(6)\n"
      "ds_read_b64_tr_b16 v[188:189], v244 offset:8192\n"
      "v_cmp_gt_u32 vcc, 2, v240\n"
      "v_mfma_f32_16x16x32_bf16 v[140:143], v[204:207], a[28:31], v[140:143]\n"
      "s_mul_i32 s88, s88, 245696\n"
      "v_mfma_f32_16x16x32_bf16 v[148:151], v[204:207], a[44:47], v[148:151]\n"
      "ds_read_b64_tr_b16 v[190:191], v244 offset:12288\n"
      "v_mfma_f32_16x16x32_bf16 v[156:159], v[204:207], a[60:63], v[156:159]\n"
      "v_mfma_f32_16x16x32_bf16 v[164:167], v[204:207], a[76:79], v[164:167]\n"
      "s_sub_u32 s79, s79, s88\n"
      "s_cbranch_vccnz CP2_%=\n"
      "BP2_%=:\n"
      "v_mfma_f32_16x16x32_bf16 v[172:175], v[204:207], a[92:95], v[172:175]\n"
      "s_waitcnt lgkmcnt(6)\n"
      "v_mfma_f32_16x16x32_bf16 v[128:131], v[176:179], a[96:99], v[128:131]\n"
      "buffer_load_dwordx4 v[208:211], v242, s[56:59], s90 offen offset:0 sc1\n"
      "v_mfma_f32_16x16x32_bf16 v[136:139], v[176:179], a[112:115], v[136:139]\n"
      "s_add_u32 s90, s90, 122880\n"
      "ds_read_b64_tr_b16 v[192:193], v243 offset:16384\n"
      "buffer_load_dwordx4 v[212:215], v242, s[56:59], s90 offen offset:0 sc1\n"
      "v_mfma_f32_16x16x32_bf16 v[144:147], v[176:179], a[128:131], v[144:147]\n"
      "s_add_u32 s90, s90, 122880\n"
      "buffer_load_dwordx4 v[216:219], v242, s[56:59], s90 offen offset:0 sc1\n"
      "v_mfma_f32_16x16x32_bf16 v[152:155], v[176:179], a[144:147], v[152:155]\n"
      "s_add_u32 s90, s90, 122880\n"
      "ds_read_b64_tr_b16 v[194:195], v243 offset:20480\n"
      "v_mfma_f32_16x16x32_bf16 v[160:163], v[176:179], a[160:163], v[160:163]\n"
      "buffer_load_dwordx4 v[220:223], v242, s[56:59], s90 offen offset:0 sc1\n"
      "v_mfma_f32_16x16x32_bf16 v[168:171], v[176:179], a[176:179], v[168:171]\n"
      "s_waitcnt lgkmcnt(6)\n"
      "s_add_u32 s90, s90, 122880\n"
      "buffer_load_dwordx4 v[224:227], v242, s[56:59], s90 offen offset:0 sc1\n"
      "v_mfma_f32_16x16x32_bf16 v[132:135], v[180:183], a[96:99], v[132:135]\n"
      "s_add_u32 s90, s90, 122880\n"
      "ds_read_b64_tr_b16 v[196:197], v244 offset:16384\n"
      "v_mfma_f32_16x16x32_bf16 v[140:143], v[180:183], a[112:115], v[140:143]\n"
      "buffer_load_dwordx4 v[228:231], v242, s[56:59], s90 offen offset:0 sc1\n"
      "s_add_u32 s90, s90, 122880\n"
      "v_mfma_f32_16x16x32_bf16 v[148:151], v[180:183], a[128:131], v[148:151]\n"
      "ds_read_b64_tr_b16 v[198:199], v244 offset:20480\n"
      "buffer_load_dwordx4 v[232:235], v242, s[56:59], s90 offen offset:0 sc1\n"
      "v_mfma_f32_16x16x32_bf16 v[156:159], v[180:183], a[144:147], v[156:159]\n"
      "s_add_u32 s90, s90, 122880\n"
      "buffer_load_dwordx4 v[236:239], v242, s[56:59], s90 offen offset:0 sc1\n"
      "v_mfma_f32_16x16x32_bf16 v[164:167], v[180:183], a[160:163], v[164:167]\n"
      "v_mfma_f32_16x16x32_bf16 v[172:175], v[180:183], a[176:179], v[172:175]\n"
      "s_waitcnt lgkmcnt(6)\n"
      "v_mfma_f32_16x16x32_bf16 v[128:131], v[184:187], a[100:103], v[128:131]\n"
      "ds_read_b64_tr_b16 v[200:201], v243 offset:24576\n"
      "v_mfma_f32_16x16x32_bf16 v[136:139], v[184:187], a[116:119], v[136:139]\n"
      "v_mfma_f32_16x16x32_bf16 v[144:147], v[184:187], a[132:135], v[144:147]\n"
      "ds_read_b64_tr_b16 v[202:203], v243 offset:28672\n"
      "v_mfma_f32_16x16x32_bf16 v[152:155], v[184:187], a[148:151], v[152:155]\n"
      "ds_read_b64_tr_b16 v[204:205], v244 offset:24576\n"
      "v_mfma_f32_16x16x32_bf16 v[160:163], v[184:187], a[164:167], v[160:163]\n"
      "v_mfma_f32_16x16x32_bf16 v[168:171], v[184:187], a[180:183], v[168:171]\n"
      "s_waitcnt lgkmcnt(7)\n"
      "ds_read_b64_tr_b16 v[206:207], v244 offset:28672\n"
      "v_mfma_f32_16x16x32_bf16 v[132:135], v[188:191], a[100:103], v[132:135]\n"
      "v_mov_b32 v241, 3\n"
      "v_mfma_f32_16x16x32_bf16 v[140:143], v[188:191], a[116:119], v[140:143]\n"
      "v_mfma_f32_16x16x32_bf16 v[148:151], v[188:191], a[132:135], v[148:151]\n"
      "v_mfma_f32_16x16x32_bf16 v[156:159], v[188:191], a[148:151], v[156:159]\n"
      "v_mfma_f32_16x16x32_bf16 v[164:167], v[188:191], a[164:167], v[164:167]\n"
      "v_mfma_f32_16x16x32_bf16 v[172:175], v[188:191], a[180:183], v[172:175]\n"
      "s_waitcnt vmcnt(8) lgkmcnt(0)\n"
      "s_barrier\n"
      "buffer_store_dword v241, v254, s[64:67], 0 offen offset:0 sc1\n"
      "v_mfma_f32_16x16x32_bf16 v[128:131], v[192:195], a[104:107], v[128:131]\n"
      "ds_read_b64_tr_b16 v[176:177], v245 offset:32768\n"
      "v_mfma_f32_16x16x32_bf16 v[136:139], v[192:195], a[120:123], v[136:139]\n"
      "v_mfma_f32_16x16x32_bf16 v[144:147], v[192:195], a[136:139], v[144:147]\n"
      "v_mfma_f32_16x16x32_bf16 v[152:155], v[192:195], a[152:155], v[152:155]\n"
      "ds_read_b64_tr_b16 v[178:179], v245 offset:36864\n"
      "v_mfma_f32_16x16x32_bf16 v[160:163], v[192:195], a[168:171], v[160:163]\n"
      "v_mfma_f32_16x16x32_bf16 v[168:171], v[192:195], a[184:187], v[168:171]\n"
      "ds_read_b64_tr_b16 v[180:181], v246 offset:32768\n"
      "v_mfma_f32_16x16x32_bf16 v[132:135], v[196:199], a[104:107], v[132:135]\n"
      "v_mfma_f32_16x16x32_bf16 v[140:143], v[196:199], a[120:123], v[140:143]\n"
      "v_mfma_f32_16x16x32_bf16 v[148:151], v[196:199], a[136:139], v[148:151]\n"
      "ds_read_b64_tr_b16 v[182:183], v246 offset:36864\n"
      "v_mfma_f32_16x16x32_bf16 v[156:159], v[196:199], a[152:155], v[156:159]\n"
      "v_mfma_f32_16x16x32_bf16 v[164:167], v[196:199], a[168:171], v[164:167]\n"
      "v_mfma_f32_16x16x32_bf16 v[172:175], v[196:199], a[184:187], v[172:175]\n"
      "v_mfma_f32_16x16x32_bf16 v[128:131], v[200:203], a[108:111], v[128:131]\n"
      "v_mfma_f32_16x16x32_bf16 v[136:139], v[200:203], a[124:127], v[136:139]\n"
      "s_mul_i32 s84, s83, 655360\n"
      "ds_read_b64_tr_b16 v[184:185], v245 offset:40960\n"
      "v_mfma_f32_16x16x32_bf16 v[144:147], v[200:203], a[140:143], v[144:147]\n"
      "v_mfma_f32_16x16x32_bf16 v[152:155], v[200:203], a[156:159], v[152:155]\n"
      "s_lshl_b32 s73, s82, 8\n"
      "ds_read_b64_tr_b16 v[186:187], v245 offset:45056\n"
      "v_mfma_f32_16x16x32_bf16 v[160:163], v[200:203], a[172:175], v[160:163]\n"
      "s_add_u32 m0, s72, 98304\n"
      "s_add_u32 s84, s84, s73\n"
      "buffer_load_dwordx4 v247, s[52:55], s84 offen offset:0  lds\n"
      "v_mfma_f32_16x16x32_bf16 v[168:171], v[200:203], a[188:191], v[168:171]\n"
      "buffer_load_dwordx4 v248, s[52:55], s84 offen offset:1024  lds\n"
      "v_mfma_f32_16x16x32_bf16 v[132:135], v[204:207], a[108:111], v[132:135]\n"
      "ds_read_b64_tr_b16 v[188:189], v246 offset:40960\n"
      "v_mfma_f32_16x16x32_bf16 v[140:143], v[204:207], a[124:127], v[140:143]\n"
      "buffer_load_dwordx4 v249, s[52:55], s84 offen offset:2048  lds\n"
      "v_mfma_f32_16x16x32_bf16 v[148:151], v[204:207], a[140:143], v[148:151]\n"
      "ds_read_b64_tr_b16 v[190:191], v246 offset:45056\n"
      "v_mfma_f32_16x16x32_bf16 v[156:159], v[204:207], a[156:159], v[156:159]\n"
      "buffer_load_dwordx4 v250, s[52:55], s84 offen offset:3072  lds\n"
      "v_mfma_f32_16x16x32_bf16 v[164:167], v[204:207], a[172:175], v[164:167]\n"
      "s_add_u32 m0, s72, 102400\n"
      "s_add_u32 s84, s84, 20480\n"
      "buffer_load_dwordx4 v247, s[52:55], s84 offen offset:0  lds\n"
      "v_mfma_f32_16x16x32_bf16 v[172:175], v[204:207], a[188:191], v[172:175]\n"
      "s_waitcnt vmcnt(13) lgkmcnt(6)\n"
      "buffer_load_dwordx4 v248, s[52:55], s84 offen offset:1024  lds\n"
      "v_lshlrev_b32 v240, 16, v208\n"
      "v_mfma_f32_16x16x32_bf16 v[128:131], v[176:179], a[192:195], v[128:131]\n"
      "s_waitcnt vmcnt(13)\n"
      "v_and_b32 v208, 0xffff0000, v208\n"
      "v_lshlrev_b32 v241, 16, v212\n"
      "v_mfma_f32_16x16x32_bf16 v[136:139], v[176:179], a[208:211], v[136:139]\n"
      "ds_read_b64_tr_b16 v[192:193], v245 offset:49152\n"
      "v_and_b32 v212, 0xffff0000, v212\n"
      "v_mfma_f32_16x16x32_bf16 v[144:147], v[176:179], a[224:227], v[144:147]\n"
      "v_add_f32 v240, v240, v241\n"
      "v_add_f32 v208, v208, v212\n"
      "v_mfma_f32_16x16x32_bf16 v[152:155], v[176:179], a[240:243], v[152:155]\n"
      "s_waitcnt vmcnt(12)\n"
      "ds_read_b64_tr_b16 v[194:195], v245 offset:53248\n"
      "v_lshlrev_b32 v241, 16, v216\n"
      "v_mfma_f32_16x16x32_bf16 v[160:163], v[176:179], v[0:3], v[160:163]\n"
      "v_and_b32 v216, 0xffff0000, v216\n"
      "v_add_f32 v240, v240, v241\n"
      "v_mfma_f32_16x16x32_bf16 v[168:171], v[176:179], v[16:19], v[168:171]\n"
      "s_waitcnt lgkmcnt(6)\n"
      "buffer_load_dwordx4 v249, s[52:55], s84 offen offset:2048  lds\n"
      "v_add_f32 v208, v208, v216\n"
      "v_mfma_f32_16x16x32_bf16 v[132:135], v[180:183], a[192:195], v[132:135]\n"
      "s_waitcnt vmcnt(12)\n"
      "ds_read_b64_tr_b16 v[196:197], v246 offset:49152\n"
      "v_lshlrev_b32 v241, 16, v220\n"
      "v_mfma_f32_16x16x32_bf16 v[140:143], v[180:183], a[208:211], v[140:143]\n"
      "v_and_b32 v220, 0xffff0000, v220\n"
      "v_add_f32 v240, v240, v241\n"
      "v_mfma_f32_16x16x32_bf16 v[148:151], v[180:183], a[224:227], v[148:151]\n"
      "ds_read_b64_tr_b16 v[198:199], v246 offset:53248\n"
      "v_add_f32 v208, v208, v220\n"
      "v_mfma_f32_16x16x32_bf16 v[156:159], v[180:183], a[240:243], v[156:159]\n"
      "s_waitcnt vmcnt(11)\n"
      "v_lshlrev_b32 v241, 16, v224\n"
      "v_and_b32 v224, 0xffff0000, v224\n"
      "v_mfma_f32_16x16x32_bf16 v[164:167], v[180:183], v[0:3], v[164:167]\n"
      "buffer_load_dwordx4 v250, s[52:55], s84 offen offset:3072  lds\n"
      "v_add_f32 v240, v240, v241\n"
      "v_mfma_f32_16x16x32_bf16 v[172:175], v[180:183], v[16:19], v[172:175]\n"
      "s_waitcnt vmcnt(11) lgkmcnt(6)\n"
      "v_add_f32 v208, v208, v224\n"
      "v_lshlrev_b32 v241, 16, v228\n"
      "v_mfma_f32_16x16x32_bf16 v[128:131], v[184:187], a[196:199], v[128:131]\n"
      "s_add_u32 s83, s83, 1\n"
      "ds_read_b64_tr_b16 v[200:201], v245 offset:57344\n"
      "v_and_b32 v228, 0xffff0000, v228\n"
      "v_mfma_f32_16x16x32_bf16 v[136:139], v[184:187], a[212:215], v[136:139]\n"
      "v_add_f32 v240, v240, v241\n"
      "v_add_f32 v208, v208, v228\n"
      "v_mfma_f32_16x16x32_bf16 v[144:147], v[184:187], a[228:231], v[144:147]\n"
      "s_waitcnt vmcnt(10)\n"
      "s_and_b32 s83, s83, 3\n"
      "ds_read_b64_tr_b16 v[202:203], v245 offset:61440\n"
      "v_lshlrev_b32 v241, 16, v232\n"
      "v_mfma_f32_16x16x32_bf16 v[152:155], v[184:187], a[244:247], v[152:155]\n"
      "s_mul_i32 s84, s83, 655360\n"
      "ds_read_b64_tr_b16 v[204:205], v246 offset:57344\n"
      "v_and_b32 v232, 0xffff0000, v232\n"
      "v_mfma_f32_16x16x32_bf16 v[160:163], v[184:187], v[4:7], v[160:163]\n"
      "v_add_f32 v240, v240, v241\n"
      "v_add_f32 v208, v208, v232\n"
      "v_mfma_f32_16x16x32_bf16 v[168:171], v[184:187], v[20:23], v[168:171]\n"
      "s_waitcnt vmcnt(9) lgkmcnt(7)\n"
      "s_lshl_b32 s73, s82, 8\n"
      "ds_read_b64_tr_b16 v[206:207], v246 offset:61440\n"
      "v_lshlrev_b32 v241, 16, v236\n"
      "v_mfma_f32_16x16x32_bf16 v[132:135], v[188:191], a[196:199], v[132:135]\n"
      "s_add_u32 m0, s72, 0\n"
      "v_and_b32 v236, 0xffff0000, v236\n"
      "v_add_f32 v240, v240, v241\n"
      "v_mfma_f32_16x16x32_bf16 v[140:143], v[188:191], a[212:215], v[140:143]\n"
      "s_add_u32 s84, s84, s73\n"
      "buffer_load_dwordx4 v247, s[52:55], s84 offen offset:0  lds\n"
      "v_add_f32 v208, v208, v236\n"
      "v_mfma_f32_16x16x32_bf16 v[148:151], v[188:191], a[228:231], v[148:151]\n"
      "v_cvt_pk_bf16_f32 v208, v240, v208\n"
      "v_lshlrev_b32 v240, 16, v210\n"
      "v_mfma_f32_16x16x32_bf16 v[156:159], v[188:191], a[244:247], v[156:159]\n"
      "buffer_load_dwordx4 v248, s[52:55], s84 offen offset:1024  lds\n"
      "v_and_b32 v210, 0xffff0000, v210\n"
      "v_mfma_f32_16x16x32_bf16 v[164:167], v[188:191], v[4:7], v[164:167]\n"
      "v_lshlrev_b32 v241, 16, v214\n"
      "v_and_b32 v214, 0xffff0000, v214\n"
      "v_mfma_f32_16x16x32_bf16 v[172:175], v[188:191], v[20:23], v[172:175]\n"
      "s_waitcnt lgkmcnt(0)\n"
      "s_barrier\n"
      "v_add_f32 v240, v240, v241\n"
      "v_mfma_f32_16x16x32_bf16 v[128:131], v[192:195], a[200:203], v[128:131]\n"
      "ds_read_b64_tr_b16 v[176:177], v243 offset:32768\n"
      "v_add_f32 v210, v210, v214\n"
      "v_mfma_f32_16x16x32_bf16 v[136:139], v[192:195], a[216:219], v[136:139]\n"
      "buffer_load_dwordx4 v249, s[52:55], s84 offen offset:2048  lds\n"
      "v_lshlrev_b32 v241, 16, v218\n"
      "v_and_b32 v218, 0xffff0000, v218\n"
      "v_mfma_f32_16x16x32_bf16 v[144:147], v[192:195], a[232:235], v[144:147]\n"
      "v_add_f32 v240, v240, v241\n"
      "v_add_f32 v210, v210, v218\n"
      "v_mfma_f32_16x16x32_bf16 v[152:155], v[192:195], a[248:251], v[152:155]\n"
      "ds_read_b64_tr_b16 v[178:179], v243 offset:36864\n"
      "v_lshlrev_b32 v241, 16, v222\n"
      "v_mfma_f32_16x16x32_bf16 v[160:163], v[192:195], v[8:11], v[160:163]\n"
      "buffer_load_dwordx4 v250, s[52:55], s84 offen offset:3072  lds\n"
      "v_and_b32 v222, 0xffff0000, v222\n"
      "v_add_f32 v240, v240, v241\n"
      "v_mfma_f32_16x16x32_bf16 v[168:171], v[192:195], v[24:27], v[168:171]\n"
      "s_add_u32 m0, s72, 4096\n"
      "ds_read_b64_tr_b16 v[180:181], v244 offset:32768\n"
      "v_add_f32 v210, v210, v222\n"
      "v_mfma_f32_16x16x32_bf16 v[132:135], v[196:199], a[200:203], v[132:135]\n"
      "s_add_u32 s84, s84, 20480\n"
      "buffer_load_dwordx4 v247, s[52:55], s84 offen offset:0  lds\n"
      "v_lshlrev_b32 v241, 16, v226\n"
      "v_mfma_f32_16x16x32_bf16 v[140:143], v[196:199], a[216:219], v[140:143]\n"
      "v_and_b32 v226, 0xffff0000, v226\n"
      "v_add_f32 v240, v240, v241\n"
      "v_mfma_f32_16x16x32_bf16 v[148:151], v[196:199], a[232:235], v[148:151]\n"
      "ds_read_b64_tr_b16 v[182:183], v244 offset:36864\n"
      "v_add_f32 v210, v210, v226\n"
      "v_mfma_f32_16x16x32_bf16 v[156:159], v[196:199], a[248:251], v[156:159]\n"
      "v_lshlrev_b32 v241, 16, v230\n"
      "v_and_b32 v230, 0xffff0000, v230\n"
      "v_mfma_f32_16x16x32_bf16 v[164:167], v[196:199], v[8:11], v[164:167]\n"
      "buffer_load_dwordx4 v248, s[52:55], s84 offen offset:1024  lds\n"
      "v_add_f32 v240, v240, v241\n"
      "v_mfma_f32_16x16x32_bf16 v[172:175], v[196:199], v[24:27], v[172:175]\n"
      "buffer_load_dwordx4 v249, s[52:55], s84 offen offset:2048  lds\n"
      "v_add_f32 v210, v210, v230\n"
      "v_mfma_f32_16x16x32_bf16 v[128:131], v[200:203], a[204:207], v[128:131]\n"
      "v_lshlrev_b32 v241, 16, v234\n"
      "v_and_b32 v234, 0xffff0000, v234\n"
      "v_mfma_f32_16x16x32_bf16 v[136:139], v[200:203], a[220:223], v[136:139]\n"
      "ds_read_b64_tr_b16 v[184:185], v243 offset:40960\n"
      "v_add_f32 v240, v240, v241\n"
      "v_mfma_f32_16x16x32_bf16 v[144:147], v[200:203], a[236:239], v[144:147]\n"
      "buffer_load_dwordx4 v250, s[52:55], s84 offen offset:3072  lds\n"
      "v_add_f32 v210, v210, v234\n"
      "v_lshlrev_b32 v241, 16, v238\n"
      "v_mfma_f32_16x16x32_bf16 v[152:155], v[200:203], a[252:255], v[152:155]\n"
      "s_add_u32 s83, s83, 1\n"
      "ds_read_b64_tr_b16 v[186:187], v243 offset:45056\n"
      "v_and_b32 v238, 0xffff0000, v238\n"
      "v_mfma_f32_16x16x32_bf16 v[160:163], v[200:203], v[12:15], v[160:163]\n"
      "v_add_f32 v240, v240, v241\n"
      "v_add_f32 v210, v210, v238\n"
      "v_mfma_f32_16x16x32_bf16 v[168:171], v[200:203], v[28:31], v[168:171]\n"
      "s_and_b32 s83, s83, 3\n"
      "v_cvt_pk_bf16_f32 v210, v240, v210\n"
      "v_mfma_f32_16x16x32_bf16 v[132:135], v[204:207], a[204:207], v[132:135]\n"
      "s_mul_i32 s84, s83, 655360\n"
      "ds_read_b64_tr_b16 v[188:189], v244 offset:40960\n"
      "v_lshlrev_b32 v240, 16, v209\n"
      "v_mfma_f32_16x16x32_bf16 v[140:143], v[204:207], a[220:223], v[140:143]\n"
      "v_and_b32 v209, 0xffff0000, v209\n"
      "v_lshlrev_b32 v241, 16, v213\n"
      "v_mfma_f32_16x16x32_bf16 v[148:151], v[204:207], a[236:239], v[148:151]\n"
      "s_lshl_b32 s73, s82, 8\n"
      "ds_read_b64_tr_b16 v[190:191], v244 offset:45056\n"
      "v_and_b32 v213, 0xffff0000, v213\n"
      "v_mfma_f32_16x16x32_bf16 v[156:159], v[204:207], a[252:255], v[156:159]\n"
      "s_add_u32 m0, s72, 131072\n"
      "v_add_f32 v240, v240, v241\n"
      "v_add_f32 v209, v209, v213\n"
      "v_mfma_f32_16x16x32_bf16 v[164:167], v[204:207], v[12:15], v[164:167]\n"
      "s_add_u32 s84, s84, s73\n"
      "buffer_load_dwordx4 v247, s[52:55], s84 offen offset:0  lds\n"
      "v_lshlrev_b32 v241, 16, v217\n"
      "v_mfma_f32_16x16x32_bf16 v[172:175], v[204:207], v[28:31], v[172:175]\n"
      "s_waitcnt lgkmcnt(6)\n"
      "v_and_b32 v217, 0xffff0000, v217\n"
      "v_add_f32 v240, v240, v241\n"
      "v_mfma_f32_16x16x32_bf16 v[128:131], v[176:179], v[32:35], v[128:131]\n"
      "buffer_load_dwordx4 v248, s[52:55], s84 offen offset:1024  lds\n"
      "v_add_f32 v209, v209, v217\n"
      "v_mfma_f32_16x16x32_bf16 v[136:139], v[176:179], v[48:51], v[136:139]\n"
      "ds_read_b64_tr_b16 v[192:193], v243 offset:49152\n"
      "v_lshlrev_b32 v241, 16, v221\n"
      "v_mfma_f32_16x16x32_bf16 v[144:147], v[176:179], v[64:67], v[144:147]\n"
      "s_mul_i32 s85, s86, 24576\n"
      "v_and_b32 v221, 0xffff0000, v221\n"
      "v_add_f32 v240, v240, v241\n"
      "v_mfma_f32_16x16x32_bf16 v[152:155], v[176:179], v[80:83], v[152:155]\n"
      "ds_read_b64_tr_b16 v[194:195], v243 offset:53248\n"
      "v_add_f32 v209, v209, v221\n"
      "v_mfma_f32_16x16x32_bf16 v[160:163], v[176:179], v[96:99], v[160:163]\n"
      "v_lshlrev_b32 v241, 16, v225\n"
      "v_and_b32 v225, 0xffff0000, v225\n"
      "v_mfma_f32_16x16x32_bf16 v[168:171], v[176:179], v[112:115], v[168:171]\n"
      "s_waitcnt lgkmcnt(6)\n"
      "buffer_load_dwordx4 v249, s[52:55], s84 offen offset:2048  lds\n"
      "v_add_f32 v240, v240, v241\n"
      "v_add_f32 v209, v209, v225\n"
      "v_mfma_f32_16x16x32_bf16 v[132:135], v[180:183], v[32:35], v[132:135]\n"
      "ds_read_b64_tr_b16 v[196:197], v244 offset:49152\n"
      "v_lshlrev_b32 v241, 16, v229\n"
      "v_mfma_f32_16x16x32_bf16 v[140:143], v[180:183], v[48:51], v[140:143]\n"
      "buffer_load_dwordx4 v250, s[52:55], s84 offen offset:3072  lds\n"
      "v_and_b32 v229, 0xffff0000, v229\n"
      "v_mfma_f32_16x16x32_bf16 v[148:151], v[180:183], v[64:67], v[148:151]\n"
      "s_mul_i32 s74, %8, 6144\n"
      "ds_read_b64_tr_b16 v[198:199], v244 offset:53248\n"
      "v_add_f32 v240, v240, v241\n"
      "v_mfma_f32_16x16x32_bf16 v[156:159], v[180:183], v[80:83], v[156:159]\n"
      "s_add_u32 m0, s72, 135168\n"
      "v_add_f32 v209, v209, v229\n"
      "v_lshlrev_b32 v241, 16, v233\n"
      "v_mfma_f32_16x16x32_bf16 v[164:167], v[180:183], v[96:99], v[164:167]\n"
      "s_add_u32 s84, s84, 20480\n"
      "v_and_b32 v233, 0xffff0000, v233\n"
      "v_add_f32 v240, v240, v241\n"
      "v_mfma_f32_16x16x32_bf16 v[172:175], v[180:183], v[112:115], v[172:175]\n"
      "s_waitcnt lgkmcnt(6)\n"
      "buffer_load_dwordx4 v247, s[52:55], s84 offen offset:0  lds\n"
      "v_add_f32 v209, v209, v233\n"
      "v_mfma_f32_16x16x32_bf16 v[128:131], v[184:187], v[36:39], v[128:131]\n"
      "ds_read_b64_tr_b16 v[200:201], v243 offset:57344\n"
      "v_lshlrev_b32 v241, 16, v237\n"
      "v_mfma_f32_16x16x32_bf16 v[136:139], v[184:187], v[52:55], v[136:139]\n"
      "buffer_load_dwordx4 v248, s[52:55], s84 offen offset:1024  lds\n"
      "v_and_b32 v237, 0xffff0000, v237\n"
      "v_add_f32 v240, v240, v241\n"
      "v_mfma_f32_16x16x32_bf16 v[144:147], v[184:187], v[68:71], v[144:147]\n"
      "s_add_u32 s85, s85, s74\n"
      "ds_read_b64_tr_b16 v[202:203], v243 offset:61440\n"
      "v_add_f32 v209, v209, v237\n"
      "v_mfma_f32_16x16x32_bf16 v[152:155], v[184:187], v[84:87], v[152:155]\n"
      "ds_read_b64_tr_b16 v[204:205], v244 offset:57344\n"
      "v_cvt_pk_bf16_f32 v209, v240, v209\n"
      "v_mfma_f32_16x16x32_bf16 v[160:163], v[184:187], v[100:103], v[160:163]\n"
      "buffer_load_dwordx4 v249, s[52:55], s84 offen offset:2048  lds\n"
      "v_lshlrev_b32 v240, 16, v211\n"
      "v_and_b32 v211, 0xffff0000, v211\n"
      "v_mfma_f32_16x16x32_bf16 v[168:171], v[184:187], v[116:119], v[168:171]\n"
      "s_waitcnt lgkmcnt(7)\n"
      "ds_read_b64_tr_b16 v[206:207], v244 offset:61440\n"
      "v_lshlrev_b32 v241, 16, v215\n"
      "v_mfma_f32_16x16x32_bf16 v[132:135], v[188:191], v[36:39], v[132:135]\n"
      "v_and_b32 v215, 0xffff0000, v215\n"
      "v_add_f32 v240, v240, v241\n"
      "v_mfma_f32_16x16x32_bf16 v[140:143], v[188:191], v[52:55], v[140:143]\n"
      "s_mul_i32 s74, %13, 122880\n"
      "v_add_f32 v211, v211, v215\n"
      "v_lshlrev_b32 v241, 16, v219\n"
      "v_mfma_f32_16x16x32_bf16 v[148:151], v[188:191], v[68:71], v[148:151]\n"
      "buffer_load_dwordx4 v250, s[52:55], s84 offen offset:3072  lds\n"
      "s_add_u32 s83, s83, 1\n"
      "v_and_b32 v219, 0xffff0000, v219\n"
      "v_mfma_f32_16x16x32_bf16 v[156:159], v[188:191], v[84:87], v[156:159]\n"
      "v_add_f32 v240, v240, v241\n"
      "v_add_f32 v211, v211, v219\n"
      "v_mfma_f32_16x16x32_bf16 v[164:167], v[188:191], v[100:103], v[164:167]\n"
      "s_and_b32 s83, s83, 3\n"
      "v_lshlrev_b32 v241, 16, v223\n"
      "v_and_b32 v223, 0xffff0000, v223\n"
      "v_mfma_f32_16x16x32_bf16 v[172:175], v[188:191], v[116:119], v[172:175]\n"
      "s_waitcnt vmcnt(16) lgkmcnt(0)\n"
      "s_barrier\n"
      "v_add_f32 v240, v240, v241\n"
      "v_mfma_f32_16x16x32_bf16 v[128:131], v[192:195], v[40:43], v[128:131]\n"
      "s_mul_i32 s84, s83, 655360\n"
      "ds_read_b64_tr_b16 v[176:177], v245 offset:0\n"
      "v_add_f32 v211, v211, v223\n"
      "v_mfma_f32_16x16x32_bf16 v[136:139], v[192:195], v[56:59], v[136:139]\n"
      "s_lshl_b32 s73, s82, 8\n"
      "v_lshlrev_b32 v241, 16, v227\n"
      "v_and_b32 v227, 0xffff0000, v227\n"
      "v_mfma_f32_16x16x32_bf16 v[144:147], v[192:195], v[72:75], v[144:147]\n"
      "s_add_u32 m0, s72, 32768\n"
      "v_add_f32 v240, v240, v241\n"
      "v_add_f32 v211, v211, v227\n"
      "v_mfma_f32_16x16x32_bf16 v[152:155], v[192:195], v[88:91], v[152:155]\n"
      "s_add_u32 s84, s84, s73\n"
      "ds_read_b64_tr_b16 v[178:179], v245 offset:4096\n"
      "v_lshlrev_b32 v241, 16, v231\n"
      "v_mfma_f32_16x16x32_bf16 v[160:163], v[192:195], v[104:107], v[160:163]\n"
      "s_add_u32 s85, s85, s74\n"
      "buffer_load_dwordx4 v247, s[52:55], s84 offen offset:0  lds\n"
      "v_and_b32 v231, 0xffff0000, v231\n"
      "v_add_f32 v240, v240, v241\n"
      "v_mfma_f32_16x16x32_bf16 v[168:171], v[192:195], v[120:123], v[168:171]\n"
      "ds_read_b64_tr_b16 v[180:181], v246 offset:0\n"
      "v_add_f32 v211, v211, v231\n"
      "v_mfma_f32_16x16x32_bf16 v[132:135], v[196:199], v[40:43], v[132:135]\n"
      "buffer_load_dwordx4 v248, s[52:55], s84 offen offset:1024  lds\n"
      "v_lshlrev_b32 v241, 16, v235\n"
      "v_and_b32 v235, 0xffff0000, v235\n"
      "v_mfma_f32_16x16x32_bf16 v[140:143], v[196:199], v[56:59], v[140:143]\n"
      "s_add_u32 s86, s86, 1\n"
      "buffer_load_dwordx4 v249, s[52:55], s84 offen offset:2048  lds\n"
      "v_add_f32 v240, v240, v241\n"
      "v_add_f32 v211, v211, v235\n"
      "v_mfma_f32_16x16x32_bf16 v[148:151], v[196:199], v[72:75], v[148:151]\n"
      "ds_read_b64_tr_b16 v[182:183], v246 offset:4096\n"
      "v_lshlrev_b32 v241, 16, v239\n"
      "v_mfma_f32_16x16x32_bf16 v[156:159], v[196:199], v[88:91], v[156:159]\n"
      "buffer_load_dwordx4 v250, s[52:55], s84 offen offset:3072  lds\n"
      "v_and_b32 v239, 0xffff0000, v239\n"
      "v_add_f32 v240, v240, v241\n"
      "v_mfma_f32_16x16x32_bf16 v[164:167], v[196:199], v[104:107], v[164:167]\n"
      "s_add_u32 m0, s72, 36864\n"
      "v_add_f32 v211, v211, v239\n"
      "v_cvt_pk_bf16_f32 v211, v240, v211\n"
      "v_mfma_f32_16x16x32_bf16 v[172:175], v[196:199], v[120:123], v[172:175]\n"
      "v_permlane16_swap_b32 v208, v210\n"
      "v_mfma_f32_16x16x32_bf16 v[128:131], v[200:203], v[44:47], v[128:131]\n"
      "s_add_u32 s84, s84, 20480\n"
      "v_permlane16_swap_b32 v209, v211\n"
      "v_mfma_f32_16x16x32_bf16 v[136:139], v[200:203], v[60:63], v[136:139]\n"
      "ds_read_b64_tr_b16 v[184:185], v245 offset:8192\n"
      "buffer_load_dwordx4 v247, s[52:55], s84 offen offset:0  lds\n"
      "v_mfma_f32_16x16x32_bf16 v[144:147], v[200:203], v[76:79], v[144:147]\n"
      "buffer_store_dwordx4 v[208:211], v252, s[60:63], s79 offen offset:0 \n"
      "buffer_load_dwordx4 v248, s[52:55], s84 offen offset:1024  lds\n"
      "v_mfma_f32_16x16x32_bf16 v[152:155], v[200:203], v[92:95], v[152:155]\n"
      "s_cmp_eq_u32 s86, 5\n"
      "s_cselect_b32 s86, 0, s86\n"
      "ds_read_b64_tr_b16 v[186:187], v245 offset:12288\n"
      "v_mfma_f32_16x16x32_bf16 v[160:163], v[200:203], v[108:111], v[160:163]\n"
      "buffer_load_dwordx4 v249, s[52:55], s84 offen offset:2048  lds\n"
      "v_cvt_pk_bf16_f32 v208, v128, v129\n"
      "v_cvt_pk_bf16_f32 v209, v130, v131\n"
      "v_mfma_f32_16x16x32_bf16 v[168:171], v[200:203], v[124:127], v[168:171]\n"
      "s_add_u32 s90, %8, 4\n"
      "buffer_load_dwordx4 v250, s[52:55], s84 offen offset:3072  lds\n"
      "v_cvt_pk_bf16_f32 v212, v136, v137\n"
      "v_cvt_pk_bf16_f32 v213, v138, v139\n"
      "v_mfma_f32_16x16x32_bf16 v[132:135], v[204:207], v[44:47], v[132:135]\n"
      "s_add_u32 s83, s83, 1\n"
      "s_and_b32 s83, s83, 3\n"
      "ds_read_b64_tr_b16 v[188:189], v246 offset:8192\n"
      "v_mfma_f32_16x16x32_bf16 v[140:143], v[204:207], v[60:63], v[140:143]\n"
      "s_add_u32 s82, s82, 1\n"
      "buffer_load_dword v240, v253, s[64:67], 0 offen offset:0 sc1\n"
      "v_cvt_pk_bf16_f32 v216, v144, v145\n"
      "v_cvt_pk_bf16_f32 v217, v146, v147\n"
      "v_mfma_f32_16x16x32_bf16 v[148:151], v[204:207], v[76:79], v[148:151]\n"
      "s_cmp_eq_u32 s82, 5\n"
      "s_cselect_b32 s82, 0, s82\n"
      "ds_read_b64_tr_b16 v[190:191], v246 offset:12288\n"
      "v_mfma_f32_16x16x32_bf16 v[156:159], v[204:207], v[92:95], v[156:159]\n"
      "s_min_u32 s90, s90, 14\n"
      "v_cvt_pk_bf16_f32 v220, v152, v153\n"
      "v_cvt_pk_bf16_f32 v221, v154, v155\n"
      "v_mfma_f32_16x16x32_bf16 v[164:167], v[204:207], v[108:111], v[164:167]\n"
      "s_mul_i32 s88, s90, 43\n"
      "v_cvt_pk_bf16_f32 v224, v160, v161\n"
      "v_cvt_pk_bf16_f32 v225, v162, v163\n"
      "v_mfma_f32_16x16x32_bf16 v[172:175], v[204:207], v[124:127], v[172:175]\n"
      "s_waitcnt lgkmcnt(6)\n"
      "v_cvt_pk_bf16_f32 v228, v168, v169\n"
      "v_cvt_pk_bf16_f32 v229, v170, v171\n"
      "v_mfma_f32_16x16x32_bf16 v[128:131], v[176:179], a[0:3], 0\n"
      "v_cvt_pk_bf16_f32 v210, v132, v133\n"
      "v_cvt_pk_bf16_f32 v211, v134, v135\n"
      "v_mfma_f32_16x16x32_bf16 v[136:139], v[176:179], a[16:19], 0\n"
      "s_lshr_b32 s88, s88, 7\n"
      "buffer_store_dwordx4 v[208:211], v242, s[56:59], s85 offen offset:0 sc1\n"
      "v_cvt_pk_bf16_f32 v214, v140, v141\n"
      "v_cvt_pk_bf16_f32 v215, v142, v143\n"
      "v_mfma_f32_16x16x32_bf16 v[144:147], v[176:179], a[32:35], 0\n"
      "buffer_store_dwordx4 v[212:215], v242, s[56:59], s85 offen offset:1024 sc1\n"
      "v_cvt_pk_bf16_f32 v218, v148, v149\n"
      "v_cvt_pk_bf16_f32 v219, v150, v151\n"
      "v_mfma_f32_16x16x32_bf16 v[152:155], v[176:179], a[48:51], 0\n"
      "buffer_store_dwordx4 v[216:219], v242, s[56:59], s85 offen offset:2048 sc1\n"
      "v_cvt_pk_bf16_f32 v222, v156, v157\n"
      "v_cvt_pk_bf16_f32 v223, v158, v159\n"
      "v_mfma_f32_16x16x32_bf16 v[160:163], v[176:179], a[64:67], 0\n"
      "s_mul_i32 s79, s88, 3\n"
      "buffer_store_dwordx4 v[220:223], v242, s[56:59], s85 offen offset:3072 sc1\n"
      "v_cvt_pk_bf16_f32 v226, v164, v165\n"
      "v_cvt_pk_bf16_f32 v227, v166, v167\n"
      "v_mfma_f32_16x16x32_bf16 v[168:171], v[176:179], a[80:83], 0\n"
      "s_waitcnt lgkmcnt(4)\n"
      "s_add_u32 s85, s85, 4096\n"
      "buffer_store_dwordx4 v[224:227], v242, s[56:59], s85 offen offset:0 sc1\n"
      "v_cvt_pk_bf16_f32 v230, v172, v173\n"
      "v_cvt_pk_bf16_f32 v231, v174, v175\n"
      "v_mfma_f32_16x16x32_bf16 v[132:135], v[180:183], a[0:3], 0\n"
      "ds_read_b64_tr_b16 v[192:193], v245 offset:16384\n"
      "buffer_store_dwordx4 v[228:231], v242, s[56:59], s85 offen offset:1024 sc1\n"
      "v_mfma_f32_16x16x32_bf16 v[140:143], v[180:183], a[16:19], 0\n"
      "s_sub_u32 s90, s90, s79\n"
      "ds_read_b64_tr_b16 v[194:195], v245 offset:20480\n"
      "v_mfma_f32_16x16x32_bf16 v[148:151], v[180:183], a[32:35], 0\n"
      "ds_read_b64_tr_b16 v[196:197], v246 offset:16384\n"
      "v_mfma_f32_16x16x32_bf16 v[156:159], v[180:183], a[48:51], 0\n"
      "ds_read_b64_tr_b16 v[198:199], v246 offset:20480\n"
      "v_mfma_f32_16x16x32_bf16 v[164:167], v[180:183], a[64:67], 0\n"
      "s_mul_i32 s79, %13, 3\n"
      "v_mfma_f32_16x16x32_bf16 v[172:175], v[180:183], a[80:83], 0\n"
      "s_waitcnt lgkmcnt(6)\n"
      "v_mfma_f32_16x16x32_bf16 v[128:131], v[184:187], a[4:7], v[128:131]\n"
      "s_add_u32 s90, s90, s79\n"
      "ds_read_b64_tr_b16 v[200:201], v245 offset:24576\n"
      "v_mfma_f32_16x16x32_bf16 v[136:139], v[184:187], a[20:23], v[136:139]\n"
      "v_mfma_f32_16x16x32_bf16 v[144:147], v[184:187], a[36:39], v[144:147]\n"
      "ds_read_b64_tr_b16 v[202:203], v245 offset:28672\n"
      "v_mfma_f32_16x16x32_bf16 v[152:155], v[184:187], a[52:55], v[152:155]\n"
      "s_add_u32 s88, s88, %11\n"
      "ds_read_b64_tr_b16 v[204:205], v246 offset:24576\n"
      "v_mfma_f32_16x16x32_bf16 v[160:163], v[184:187], a[68:71], v[160:163]\n"
      "v_mfma_f32_16x16x32_bf16 v[168:171], v[184:187], a[84:87], v[168:171]\n"
      "s_waitcnt lgkmcnt(7)\n"
      "ds_read_b64_tr_b16 v[206:207], v246 offset:28672\n"
      "v_mfma_f32_16x16x32_bf16 v[132:135], v[188:191], a[4:7], v[132:135]\n"
      "s_sub_u32 s79, s88, 5\n"
      "v_mfma_f32_16x16x32_bf16 v[140:143], v[188:191], a[20:23], v[140:143]\n"
      "v_mfma_f32_16x16x32_bf16 v[148:151], v[188:191], a[36:39], v[148:151]\n"
      "v_mfma_f32_16x16x32_bf16 v[156:159], v[188:191], a[52:55], v[156:159]\n"
      "s_min_u32 s88, s88, s79\n"
      "v_mfma_f32_16x16x32_bf16 v[164:167], v[188:191], a[68:71], v[164:167]\n"
      "v_mfma_f32_16x16x32_bf16 v[172:175], v[188:191], a[84:87], v[172:175]\n"
      "s_waitcnt vmcnt(24) lgkmcnt(0)\n"
      "s_barrier\n"
      "v_mfma_f32_16x16x32_bf16 v[128:131], v[192:195], a[8:11], v[128:131]\n"
      "s_mul_i32 s79, s90, 40960\n"
      "ds_read_b64_tr_b16 v[176:177], v243 offset:0\n"
      "v_mfma_f32_16x16x32_bf16 v[136:139], v[192:195], a[24:27], v[136:139]\n"
      "v_mfma_f32_16x16x32_bf16 v[144:147], v[192:195], a[40:43], v[144:147]\n"
      "s_lshl_b32 s90, s90, 10\n"
      "v_mfma_f32_16x16x32_bf16 v[152:155], v[192:195], a[56:59], v[152:155]\n"
      "ds_read_b64_tr_b16 v[178:179], v243 offset:4096\n"
      "v_mfma_f32_16x16x32_bf16 v[160:163], v[192:195], a[72:75], v[160:163]\n"
      "v_mfma_f32_16x16x32_bf16 v[168:171], v[192:195], a[88:91], v[168:171]\n"
      "s_mul_i32 s78, s88, 24576\n"
      "ds_read_b64_tr_b16 v[180:181], v244 offset:0\n"
      "v_mfma_f32_16x16x32_bf16 v[132:135], v[196:199], a[8:11], v[132:135]\n"
      "v_mfma_f32_16x16x32_bf16 v[140:143], v[196:199], a[24:27], v[140:143]\n"
      "v_mfma_f32_16x16x32_bf16 v[148:151], v[196:199], a[40:43], v[148:151]\n"
      "s_add_u32 s90, s90, s78\n"
      "ds_read_b64_tr_b16 v[182:183], v244 offset:4096\n"
      "v_mfma_f32_16x16x32_bf16 v[156:159], v[196:199], a[56:59], v[156:159]\n"
      "v_mfma_f32_16x16x32_bf16 v[164:167], v[196:199], a[72:75], v[164:167]\n"
      "v_mfma_f32_16x16x32_bf16 v[172:175], v[196:199], a[88:91], v[172:175]\n"
      "s_lshl_b32 s88, s88, 8\n"
      "v_mfma_f32_16x16x32_bf16 v[128:131], v[200:203], a[12:15], v[128:131]\n"
      "v_mfma_f32_16x16x32_bf16 v[136:139], v[200:203], a[28:31], v[136:139]\n"
      "ds_read_b64_tr_b16 v[184:185], v243 offset:8192\n"
      "v_mfma_f32_16x16x32_bf16 v[144:147], v[200:203], a[44:47], v[144:147]\n"
      "s_add_u32 s79, s79, s88\n"
      "v_mfma_f32_16x16x32_bf16 v[152:155], v[200:203], a[60:63], v[152:155]\n"
      "ds_read_b64_tr_b16 v[186:187], v243 offset:12288\n"
      "v_mfma_f32_16x16x32_bf16 v[160:163], v[200:203], a[76:79], v[160:163]\n"
      "s_lshr_b32 s88, %13, 1\n"
      "v_mfma_f32_16x16x32_bf16 v[168:171], v[200:203], a[92:95], v[168:171]\n"
      "v_mfma_f32_16x16x32_bf16 v[132:135], v[204:207], a[12:15], v[132:135]\n"
      "s_waitcnt vmcnt(6)\n"
      "ds_read_b64_tr_b16 v[188:189], v244 offset:8192\n"
      "v_cmp_gt_u32 vcc, 3, v240\n"
      "v_mfma_f32_16x16x32_bf16 v[140:143], v[204:207], a[28:31], v[140:143]\n"
      "s_mul_i32 s88, s88, 245696\n"
      "v_mfma_f32_16x16x32_bf16 v[148:151], v[204:207], a[44:47], v[148:151]\n"
      "ds_read_b64_tr_b16 v[190:191], v244 offset:12288\n"
      "v_mfma_f32_16x16x32_bf16 v[156:159], v[204:207], a[60:63], v[156:159]\n"
      "v_mfma_f32_16x16x32_bf16 v[164:167], v[204:207], a[76:79], v[164:167]\n"
      "s_sub_u32 s79, s79, s88\n"
      "s_cbranch_vccnz CP3_%=\n"
      "BP3_%=:\n"
      "v_mfma_f32_16x16x32_bf16 v[172:175], v[204:207], a[92:95], v[172:175]\n"
      "s_waitcnt lgkmcnt(6)\n"
      "v_mfma_f32_16x16x32_bf16 v[128:131], v[176:179], a[96:99], v[128:131]\n"
      "buffer_load_dwordx4 v[208:211], v242, s[56:59], s90 offen offset:0 sc1\n"
      "v_mfma_f32_16x16x32_bf16 v[136:139], v[176:179], a[112:115], v[136:139]\n"
      "s_add_u32 s90, s90, 122880\n"
      "ds_read_b64_tr_b16 v[192:193], v243 offset:16384\n"
      "buffer_load_dwordx4 v[212:215], v242, s[56:59], s90 offen offset:0 sc1\n"
      "v_mfma_f32_16x16x32_bf16 v[144:147], v[176:179], a[128:131], v[144:147]\n"
      "s_add_u32 s90, s90, 122880\n"
      "buffer_load_dwordx4 v[216:219], v242, s[56:59], s90 offen offset:0 sc1\n"
      "v_mfma_f32_16x16x32_bf16 v[152:155], v[176:179], a[144:147], v[152:155]\n"
      "s_add_u32 s90, s90, 122880\n"
      "ds_read_b64_tr_b16 v[194:195], v243 offset:20480\n"
      "v_mfma_f32_16x16x32_bf16 v[160:163], v[176:179], a[160:163], v[160:163]\n"
      "buffer_load_dwordx4 v[220:223], v242, s[56:59], s90 offen offset:0 sc1\n"
      "v_mfma_f32_16x16x32_bf16 v[168:171], v[176:179], a[176:179], v[168:171]\n"
      "s_waitcnt lgkmcnt(6)\n"
      "s_add_u32 s90, s90, 122880\n"
      "buffer_load_dwordx4 v[224:227], v242, s[56:59], s90 offen offset:0 sc1\n"
      "v_mfma_f32_16x16x32_bf16 v[132:135], v[180:183], a[96:99], v[132:135]\n"
      "s_add_u32 s90, s90, 122880\n"
      "ds_read_b64_tr_b16 v[196:197], v244 offset:16384\n"
      "v_mfma_f32_16x16x32_bf16 v[140:143], v[180:183], a[112:115], v[140:143]\n"
      "buffer_load_dwordx4 v[228:231], v242, s[56:59], s90 offen offset:0 sc1\n"
      "s_add_u32 s90, s90, 122880\n"
      "v_mfma_f32_16x16x32_bf16 v[148:151], v[180:183], a[128:131], v[148:151]\n"
      "ds_read_b64_tr_b16 v[198:199], v244 offset:20480\n"
      "buffer_load_dwordx4 v[232:235], v242, s[56:59], s90 offen offset:0 sc1\n"
      "v_mfma_f32_16x16x32_bf16 v[156:159], v[180:183], a[144:147], v[156:159]\n"
      "s_add_u32 s90, s90, 122880\n"
      "buffer_load_dwordx4 v[236:239], v242, s[56:59], s90 offen offset:0 sc1\n"
      "v_mfma_f32_16x16x32_bf16 v[164:167], v[180:183], a[160:163], v[164:167]\n"
      "v_mfma_f32_16x16x32_bf16 v[172:175], v[180:183], a[176:179], v[172:175]\n"
      "s_waitcnt lgkmcnt(6)\n"
      "v_mfma_f32_16x16x32_bf16 v[128:131], v[184:187], a[100:103], v[128:131]\n"
      "ds_read_b64_tr_b16 v[200:201], v243 offset:24576\n"
      "v_mfma_f32_16x16x32_bf16 v[136:139], v[184:187], a[116:119], v[136:139]\n"
      "v_mfma_f32_16x16x32_bf16 v[144:147], v[184:187], a[132:135], v[144:147]\n"
      "ds_read_b64_tr_b16 v[202:203], v243 offset:28672\n"
      "v_mfma_f32_16x16x32_bf16 v[152:155], v[184:187], a[148:151], v[152:155]\n"
      "ds_read_b64_tr_b16 v[204:205], v244 offset:24576\n"
      "v_mfma_f32_16x16x32_bf16 v[160:163], v[184:187], a[164:167], v[160:163]\n"
      "v_mfma_f32_16x16x32_bf16 v[168:171], v[184:187], a[180:183], v[168:171]\n"
      "s_waitcnt lgkmcnt(7)\n"
      "ds_read_b64_tr_b16 v[206:207], v244 offset:28672\n"
      "v_mfma_f32_16x16x32_bf16 v[132:135], v[188:191], a[100:103], v[132:135]\n"
      "v_mov_b32 v241, 4\n"
      "v_mfma_f32_16x16x32_bf16 v[140:143], v[188:191], a[116:119], v[140:143]\n"
      "v_mfma_f32_16x16x32_bf16 v[148:151], v[188:191], a[132:135], v[148:151]\n"
      "v_mfma_f32_16x16x32_bf16 v[156:159], v[188:191], a[148:151], v[156:159]\n"
      "v_mfma_f32_16x16x32_bf16 v[164:167], v[188:191], a[164:167], v[164:167]\n"
      "v_mfma_f32_16x16x32_bf16 v[172:175], v[188:191], a[180:183], v[172:175]\n"
      "s_waitcnt vmcnt(8) lgkmcnt(0)\n"
      "s_barrier\n"
      "buffer_store_dword v241, v254, s[64:67], 0 offen offset:0 sc1\n"
      "v_mfma_f32_16x16x32_bf16 v[128:131], v[192:195], a[104:107], v[128:131]\n"
      "ds_read_b64_tr_b16 v[176:177], v245 offset:32768\n"
      "v_mfma_f32_16x16x32_bf16 v[136:139], v[192:195], a[120:123], v[136:139]\n"
      "v_mfma_f32_16x16x32_bf16 v[144:147], v[192:195], a[136:139], v[144:147]\n"
      "v_mfma_f32_16x16x32_bf16 v[152:155], v[192:195], a[152:155], v[152:155]\n"
      "ds_read_b64_tr_b16 v[178:179], v245 offset:36864\n"
      "v_mfma_f32_16x16x32_bf16 v[160:163], v[192:195], a[168:171], v[160:163]\n"
      "v_mfma_f32_16x16x32_bf16 v[168:171], v[192:195], a[184:187], v[168:171]\n"
      "ds_read_b64_tr_b16 v[180:181], v246 offset:32768\n"
      "v_mfma_f32_16x16x32_bf16 v[132:135], v[196:199], a[104:107], v[132:135]\n"
      "v_mfma_f32_16x16x32_bf16 v[140:143], v[196:199], a[120:123], v[140:143]\n"
      "v_mfma_f32_16x16x32_bf16 v[148:151], v[196:199], a[136:139], v[148:151]\n"
      "ds_read_b64_tr_b16 v[182:183], v246 offset:36864\n"
      "v_mfma_f32_16x16x32_bf16 v[156:159], v[196:199], a[152:155], v[156:159]\n"
      "v_mfma_f32_16x16x32_bf16 v[164:167], v[196:199], a[168:171], v[164:167]\n"
      "v_mfma_f32_16x16x32_bf16 v[172:175], v[196:199], a[184:187], v[172:175]\n"
      "v_mfma_f32_16x16x32_bf16 v[128:131], v[200:203], a[108:111], v[128:131]\n"
      "v_mfma_f32_16x16x32_bf16 v[136:139], v[200:203], a[124:127], v[136:139]\n"
      "ds_read_b64_tr_b16 v[184:185], v245 offset:40960\n"
      "v_mfma_f32_16x16x32_bf16 v[144:147], v[200:203], a[140:143], v[144:147]\n"
      "v_mfma_f32_16x16x32_bf16 v[152:155], v[200:203], a[156:159], v[152:155]\n"
      "ds_read_b64_tr_b16 v[186:187], v245 offset:45056\n"
      "v_mfma_f32_16x16x32_bf16 v[160:163], v[200:203], a[172:175], v[160:163]\n"
      "v_mfma_f32_16x16x32_bf16 v[168:171], v[200:203], a[188:191], v[168:171]\n"
      "v_mfma_f32_16x16x32_bf16 v[132:135], v[204:207], a[108:111], v[132:135]\n"
      "ds_read_b64_tr_b16 v[188:189], v246 offset:40960\n"
      "v_mfma_f32_16x16x32_bf16 v[140:143], v[204:207], a[124:127], v[140:143]\n"
      "v_mfma_f32_16x16x32_bf16 v[148:151], v[204:207], a[140:143], v[148:151]\n"
      "ds_read_b64_tr_b16 v[190:191], v246 offset:45056\n"
      "v_mfma_f32_16x16x32_bf16 v[156:159], v[204:207], a[156:159], v[156:159]\n"
      "v_mfma_f32_16x16x32_bf16 v[164:167], v[204:207], a[172:175], v[164:167]\n"
      "v_mfma_f32_16x16x32_bf16 v[172:175], v[204:207], a[188:191], v[172:175]\n"
      "s_waitcnt vmcnt(8) lgkmcnt(6)\n"
      "v_lshlrev_b32 v240, 16, v208\n"
      "v_mfma_f32_16x16x32_bf16 v[128:131], v[176:179], a[192:195], v[128:131]\n"
      "s_waitcnt vmcnt(7)\n"
      "v_and_b32 v208, 0xffff0000, v208\n"
      "v_lshlrev_b32 v241, 16, v212\n"
      "v_mfma_f32_16x16x32_bf16 v[136:139], v[176:179], a[208:211], v[136:139]\n"
      "ds_read_b64_tr_b16 v[192:193], v245 offset:49152\n"
      "v_and_b32 v212, 0xffff0000, v212\n"
      "v_mfma_f32_16x16x32_bf16 v[144:147], v[176:179], a[224:227], v[144:147]\n"
      "v_add_f32 v240, v240, v241\n"
      "v_add_f32 v208, v208, v212\n"
      "v_mfma_f32_16x16x32_bf16 v[152:155], v[176:179], a[240:243], v[152:155]\n"
      "s_waitcnt vmcnt(6)\n"
      "ds_read_b64_tr_b16 v[194:195], v245 offset:53248\n"
      "v_lshlrev_b32 v241, 16, v216\n"
      "v_mfma_f32_16x16x32_bf16 v[160:163], v[176:179], v[0:3], v[160:163]\n"
      "v_and_b32 v216, 0xffff0000, v216\n"
      "v_add_f32 v240, v240, v241\n"
      "v_mfma_f32_16x16x32_bf16 v[168:171], v[176:179], v[16:19], v[168:171]\n"
      "s_waitcnt lgkmcnt(6)\n"
      "v_add_f32 v208, v208, v216\n"
      "v_mfma_f32_16x16x32_bf16 v[132:135], v[180:183], a[192:195], v[132:135]\n"
      "s_waitcnt vmcnt(5)\n"
      "ds_read_b64_tr_b16 v[196:197], v246 offset:49152\n"
      "v_lshlrev_b32 v241, 16, v220\n"
      "v_mfma_f32_16x16x32_bf16 v[140:143], v[180:183], a[208:211], v[140:143]\n"
      "v_and_b32 v220, 0xffff0000, v220\n"
      "v_add_f32 v240, v240, v241\n"
      "v_mfma_f32_16x16x32_bf16 v[148:151], v[180:183], a[224:227], v[148:151]\n"
      "ds_read_b64_tr_b16 v[198:199], v246 offset:53248\n"
      "v_add_f32 v208, v208, v220\n"
      "v_mfma_f32_16x16x32_bf16 v[156:159], v[180:183], a[240:243], v[156:159]\n"
      "s_waitcnt vmcnt(4)\n"
      "v_lshlrev_b32 v241, 16, v224\n"
      "v_and_b32 v224, 0xffff0000, v224\n"
      "v_mfma_f32_16x16x32_bf16 v[164:167], v[180:183], v[0:3], v[164:167]\n"
      "v_add_f32 v240, v240, v241\n"
      "v_mfma_f32_16x16x32_bf16 v[172:175], v[180:183], v[16:19], v[172:175]\n"
      "s_waitcnt vmcnt(3) lgkmcnt(6)\n"
      "v_add_f32 v208, v208, v224\n"
      "v_lshlrev_b32 v241, 16, v228\n"
      "v_mfma_f32_16x16x32_bf16 v[128:131], v[184:187], a[196:199], v[128:131]\n"
      "ds_read_b64_tr_b16 v[200:201], v245 offset:57344\n"
      "v_and_b32 v228, 0xffff0000, v228\n"
      "v_mfma_f32_16x16x32_bf16 v[136:139], v[184:187], a[212:215], v[136:139]\n"
      "v_add_f32 v240, v240, v241\n"
      "v_add_f32 v208, v208, v228\n"
      "v_mfma_f32_16x16x32_bf16 v[144:147], v[184:187], a[228:231], v[144:147]\n"
      "s_waitcnt vmcnt(2)\n"
      "ds_read_b64_tr_b16 v[202:203], v245 offset:61440\n"
      "v_lshlrev_b32 v241, 16, v232\n"
      "v_mfma_f32_16x16x32_bf16 v[152:155], v[184:187], a[244:247], v[152:155]\n"
      "ds_read_b64_tr_b16 v[204:205], v246 offset:57344\n"
      "v_and_b32 v232, 0xffff0000, v232\n"
      "v_mfma_f32_16x16x32_bf16 v[160:163], v[184:187], v[4:7], v[160:163]\n"
      "v_add_f32 v240, v240, v241\n"
      "v_add_f32 v208, v208, v232\n"
      "v_mfma_f32_16x16x32_bf16 v[168:171], v[184:187], v[20:23], v[168:171]\n"
      "s_waitcnt vmcnt(1) lgkmcnt(7)\n"
      "ds_read_b64_tr_b16 v[206:207], v246 offset:61440\n"
      "v_lshlrev_b32 v241, 16, v236\n"
      "v_mfma_f32_16x16x32_bf16 v[132:135], v[188:191], a[196:199], v[132:135]\n"
      "v_and_b32 v236, 0xffff0000, v236\n"
      "v_add_f32 v240, v240, v241\n"
      "v_mfma_f32_16x16x32_bf16 v[140:143], v[188:191], a[212:215], v[140:143]\n"
      "v_add_f32 v208, v208, v236\n"
      "v_mfma_f32_16x16x32_bf16 v[148:151], v[188:191], a[228:231], v[148:151]\n"
      "v_cvt_pk_bf16_f32 v208, v240, v208\n"
      "v_lshlrev_b32 v240, 16, v210\n"
      "v_mfma_f32_16x16x32_bf16 v[156:159], v[188:191], a[244:247], v[156:159]\n"
      "v_and_b32 v210, 0xffff0000, v210\n"
      "v_mfma_f32_16x16x32_bf16 v[164:167], v[188:191], v[4:7], v[164:167]\n"
      "v_lshlrev_b32 v241, 16, v214\n"
      "v_and_b32 v214, 0xffff0000, v214\n"
      "v_mfma_f32_16x16x32_bf16 v[172:175], v[188:191], v[20:23], v[172:175]\n"
      "s_waitcnt lgkmcnt(0)\n"
      "s_barrier\n"
      "v_add_f32 v240, v240, v241\n"
      "v_mfma_f32_16x16x32_bf16 v[128:131], v[192:195], a[200:203], v[128:131]\n"
      "ds_read_b64_tr_b16 v[176:177], v243 offset:32768\n"
      "v_add_f32 v210, v210, v214\n"
      "v_mfma_f32_16x16x32_bf16 v[136:139], v[192:195], a[216:219], v[136:139]\n"
      "v_lshlrev_b32 v241, 16, v218\n"
      "v_and_b32 v218, 0xffff0000, v218\n"
      "v_mfma_f32_16x16x32_bf16 v[144:147], v[192:195], a[232:235], v[144:147]\n"
      "v_add_f32 v240, v240, v241\n"
      "v_add_f32 v210, v210, v218\n"
      "v_mfma_f32_16x16x32_bf16 v[152:155], v[192:195], a[248:251], v[152:155]\n"
      "ds_read_b64_tr_b16 v[178:179], v243 offset:36864\n"
      "v_lshlrev_b32 v241, 16, v222\n"
      "v_mfma_f32_16x16x32_bf16 v[160:163], v[192:195], v[8:11], v[160:163]\n"
      "v_and_b32 v222, 0xffff0000, v222\n"
      "v_add_f32 v240, v240, v241\n"
      "v_mfma_f32_16x16x32_bf16 v[168:171], v[192:195], v[24:27], v[168:171]\n"
      "ds_read_b64_tr_b16 v[180:181], v244 offset:32768\n"
      "v_add_f32 v210, v210, v222\n"
      "v_mfma_f32_16x16x32_bf16 v[132:135], v[196:199], a[200:203], v[132:135]\n"
      "v_lshlrev_b32 v241, 16, v226\n"
      "v_mfma_f32_16x16x32_bf16 v[140:143], v[196:199], a[216:219], v[140:143]\n"
      "v_and_b32 v226, 0xffff0000, v226\n"
      "v_add_f32 v240, v240, v241\n"
      "v_mfma_f32_16x16x32_bf16 v[148:151], v[196:199], a[232:235], v[148:151]\n"
      "ds_read_b64_tr_b16 v[182:183], v244 offset:36864\n"
      "v_add_f32 v210, v210, v226\n"
      "v_mfma_f32_16x16x32_bf16 v[156:159], v[196:199], a[248:251], v[156:159]\n"
      "v_lshlrev_b32 v241, 16, v230\n"
      "v_and_b32 v230, 0xffff0000, v230\n"
      "v_mfma_f32_16x16x32_bf16 v[164:167], v[196:199], v[8:11], v[164:167]\n"
      "v_add_f32 v240, v240, v241\n"
      "v_mfma_f32_16x16x32_bf16 v[172:175], v[196:199], v[24:27], v[172:175]\n"
      "v_add_f32 v210, v210, v230\n"
      "v_mfma_f32_16x16x32_bf16 v[128:131], v[200:203], a[204:207], v[128:131]\n"
      "v_lshlrev_b32 v241, 16, v234\n"
      "v_and_b32 v234, 0xffff0000, v234\n"
      "v_mfma_f32_16x16x32_bf16 v[136:139], v[200:203], a[220:223], v[136:139]\n"
      "ds_read_b64_tr_b16 v[184:185], v243 offset:40960\n"
      "v_add_f32 v240, v240, v241\n"
      "v_mfma_f32_16x16x32_bf16 v[144:147], v[200:203], a[236:239], v[144:147]\n"
      "v_add_f32 v210, v210, v234\n"
      "v_lshlrev_b32 v241, 16, v238\n"
      "v_mfma_f32_16x16x32_bf16 v[152:155], v[200:203], a[252:255], v[152:155]\n"
      "ds_read_b64_tr_b16 v[186:187], v243 offset:45056\n"
      "v_and_b32 v238, 0xffff0000, v238\n"
      "v_mfma_f32_16x16x32_bf16 v[160:163], v[200:203], v[12:15], v[160:163]\n"
      "v_add_f32 v240, v240, v241\n"
      "v_add_f32 v210, v210, v238\n"
      "v_mfma_f32_16x16x32_bf16 v[168:171], v[200:203], v[28:31], v[168:171]\n"
      "v_cvt_pk_bf16_f32 v210, v240, v210\n"
      "v_mfma_f32_16x16x32_bf16 v[132:135], v[204:207], a[204:207], v[132:135]\n"
      "ds_read_b64_tr_b16 v[188:189], v244 offset:40960\n"
      "v_lshlrev_b32 v240, 16, v209\n"
      "v_mfma_f32_16x16x32_bf16 v[140:143], v[204:207], a[220:223], v[140:143]\n"
      "v_and_b32 v209, 0xffff0000, v209\n"
      "v_lshlrev_b32 v241, 16, v213\n"
      "v_mfma_f32_16x16x32_bf16 v[148:151], v[204:207], a[236:239], v[148:151]\n"
      "ds_read_b64_tr_b16 v[190:191], v244 offset:45056\n"
      "v_and_b32 v213, 0xffff0000, v213\n"
      "v_mfma_f32_16x16x32_bf16 v[156:159], v[204:207], a[252:255], v[156:159]\n"
      "v_add_f32 v240, v240, v241\n"
      "v_add_f32 v209, v209, v213\n"
      "v_mfma_f32_16x16x32_bf16 v[164:167], v[204:207], v[12:15], v[164:167]\n"
      "v_lshlrev_b32 v241, 16, v217\n"
      "v_mfma_f32_16x16x32_bf16 v[172:175], v[204:207], v[28:31], v[172:175]\n"
      "s_waitcnt lgkmcnt(6)\n"
      "v_and_b32 v217, 0xffff0000, v217\n"
      "v_add_f32 v240, v240, v241\n"
      "v_mfma_f32_16x16x32_bf16 v[128:131], v[176:179], v[32:35], v[128:131]\n"
      "v_add_f32 v209, v209, v217\n"
      "v_mfma_f32_16x16x32_bf16 v[136:139], v[176:179], v[48:51], v[136:139]\n"
      "ds_read_b64_tr_b16 v[192:193], v243 offset:49152\n"
      "v_lshlrev_b32 v241, 16, v221\n"
      "v_mfma_f32_16x16x32_bf16 v[144:147], v[176:179], v[64:67], v[144:147]\n"
      "s_mul_i32 s85, s86, 24576\n"
      "v_and_b32 v221, 0xffff0000, v221\n"
      "v_add_f32 v240, v240, v241\n"
      "v_mfma_f32_16x16x32_bf16 v[152:155], v[176:179], v[80:83], v[152:155]\n"
      "ds_read_b64_tr_b16 v[194:195], v243 offset:53248\n"
      "v_add_f32 v209, v209, v221\n"
      "v_mfma_f32_16x16x32_bf16 v[160:163], v[176:179], v[96:99], v[160:163]\n"
      "v_lshlrev_b32 v241, 16, v225\n"
      "v_and_b32 v225, 0xffff0000, v225\n"
      "v_mfma_f32_16x16x32_bf16 v[168:171], v[176:179], v[112:115], v[168:171]\n"
      "s_waitcnt lgkmcnt(6)\n"
      "v_add_f32 v240, v240, v241\n"
      "v_add_f32 v209, v209, v225\n"
      "v_mfma_f32_16x16x32_bf16 v[132:135], v[180:183], v[32:35], v[132:135]\n"
      "ds_read_b64_tr_b16 v[196:197], v244 offset:49152\n"
      "v_lshlrev_b32 v241, 16, v229\n"
      "v_mfma_f32_16x16x32_bf16 v[140:143], v[180:183], v[48:51], v[140:143]\n"
      "v_and_b32 v229, 0xffff0000, v229\n"
      "v_mfma_f32_16x16x32_bf16 v[148:151], v[180:183], v[64:67], v[148:151]\n"
      "s_mul_i32 s74, %8, 6144\n"
      "ds_read_b64_tr_b16 v[198:199], v244 offset:53248\n"
      "v_add_f32 v240, v240, v241\n"
      "v_mfma_f32_16x16x32_bf16 v[156:159], v[180:183], v[80:83], v[156:159]\n"
      "v_add_f32 v209, v209, v229\n"
      "v_lshlrev_b32 v241, 16, v233\n"
      "v_mfma_f32_16x16x32_bf16 v[164:167], v[180:183], v[96:99], v[164:167]\n"
      "v_and_b32 v233, 0xffff0000, v233\n"
      "v_add_f32 v240, v240, v241\n"
      "v_mfma_f32_16x16x32_bf16 v[172:175], v[180:183], v[112:115], v[172:175]\n"
      "s_waitcnt lgkmcnt(6)\n"
      "v_add_f32 v209, v209, v233\n"
      "v_mfma_f32_16x16x32_bf16 v[128:131], v[184:187], v[36:39], v[128:131]\n"
      "ds_read_b64_tr_b16 v[200:201], v243 offset:57344\n"
      "v_lshlrev_b32 v241, 16, v237\n"
      "v_mfma_f32_16x16x32_bf16 v[136:139], v[184:187], v[52:55], v[136:139]\n"
      "v_and_b32 v237, 0xffff0000, v237\n"
      "v_add_f32 v240, v240, v241\n"
      "v_mfma_f32_16x16x32_bf16 v[144:147], v[184:187], v[68:71], v[144:147]\n"
      "s_add_u32 s85, s85, s74\n"
      "ds_read_b64_tr_b16 v[202:203], v243 offset:61440\n"
      "v_add_f32 v209, v209, v237\n"
      "v_mfma_f32_16x16x32_bf16 v[152:155], v[184:187], v[84:87], v[152:155]\n"
      "ds_read_b64_tr_b16 v[204:205], v244 offset:57344\n"
      "v_cvt_pk_bf16_f32 v209, v240, v209\n"
      "v_mfma_f32_16x16x32_bf16 v[160:163], v[184:187], v[100:103], v[160:163]\n"
      "v_lshlrev_b32 v240, 16, v211\n"
      "v_and_b32 v211, 0xffff0000, v211\n"
      "v_mfma_f32_16x16x32_bf16 v[168:171], v[184:187], v[116:119], v[168:171]\n"
      "s_waitcnt lgkmcnt(7)\n"
      "ds_read_b64_tr_b16 v[206:207], v244 offset:61440\n"
      "v_lshlrev_b32 v241, 16, v215\n"
      "v_mfma_f32_16x16x32_bf16 v[132:135], v[188:191], v[36:39], v[132:135]\n"
      "v_and_b32 v215, 0xffff0000, v215\n"
      "v_add_f32 v240, v240, v241\n"
      "v_mfma_f32_16x16x32_bf16 v[140:143], v[188:191], v[52:55], v[140:143]\n"
      "s_mul_i32 s74, %13, 122880\n"
      "v_add_f32 v211, v211, v215\n"
      "v_lshlrev_b32 v241, 16, v219\n"
      "v_mfma_f32_16x16x32_bf16 v[148:151], v[188:191], v[68:71], v[148:151]\n"
      "v_and_b32 v219, 0xffff0000, v219\n"
      "v_mfma_f32_16x16x32_bf16 v[156:159], v[188:191], v[84:87], v[156:159]\n"
      "v_add_f32 v240, v240, v241\n"
      "v_mfma_f32_16x16x32_bf16 v[164:167], v[188:191], v[100:103], v[164:167]\n"
      "v_add_f32 v211, v211, v219\n"
      "v_lshlrev_b32 v241, 16, v223\n"
      "v_mfma_f32_16x16x32_bf16 v[172:175], v[188:191], v[116:119], v[172:175]\n"
      "s_waitcnt lgkmcnt(6)\n"
      "v_and_b32 v223, 0xffff0000, v223\n"
      "v_mfma_f32_16x16x32_bf16 v[128:131], v[192:195], v[40:43], v[128:131]\n"
      "v_add_f32 v240, v240, v241\n"
      "v_add_f32 v211, v211, v223\n"
      "v_mfma_f32_16x16x32_bf16 v[136:139], v[192:195], v[56:59], v[136:139]\n"
      "s_add_u32 s85, s85, s74\n"
      "v_lshlrev_b32 v241, 16, v227\n"
      "v_mfma_f32_16x16x32_bf16 v[144:147], v[192:195], v[72:75], v[144:147]\n"
      "v_and_b32 v227, 0xffff0000, v227\n"
      "v_add_f32 v240, v240, v241\n"
      "v_mfma_f32_16x16x32_bf16 v[152:155], v[192:195], v[88:91], v[152:155]\n"
      "v_add_f32 v211, v211, v227\n"
      "v_mfma_f32_16x16x32_bf16 v[160:163], v[192:195], v[104:107], v[160:163]\n"
      "v_lshlrev_b32 v241, 16, v231\n"
      "v_and_b32 v231, 0xffff0000, v231\n"
      "v_mfma_f32_16x16x32_bf16 v[168:171], v[192:195], v[120:123], v[168:171]\n"
      "s_waitcnt lgkmcnt(4)\n"
      "v_add_f32 v240, v240, v241\n"
      "v_mfma_f32_16x16x32_bf16 v[132:135], v[196:199], v[40:43], v[132:135]\n"
      "v_add_f32 v211, v211, v231\n"
      "v_mfma_f32_16x16x32_bf16 v[140:143], v[196:199], v[56:59], v[140:143]\n"
      "s_add_u32 s86, s86, 1\n"
      "v_lshlrev_b32 v241, 16, v235\n"
      "v_and_b32 v235, 0xffff0000, v235\n"
      "v_mfma_f32_16x16x32_bf16 v[148:151], v[196:199], v[72:75], v[148:151]\n"
      "v_add_f32 v240, v240, v241\n"
      "v_mfma_f32_16x16x32_bf16 v[156:159], v[196:199], v[88:91], v[156:159]\n"
      "v_add_f32 v211, v211, v235\n"
      "v_lshlrev_b32 v241, 16, v239\n"
      "v_mfma_f32_16x16x32_bf16 v[164:167], v[196:199], v[104:107], v[164:167]\n"
      "v_and_b32 v239, 0xffff0000, v239\n"
      "v_add_f32 v240, v240, v241\n"
      "v_mfma_f32_16x16x32_bf16 v[172:175], v[196:199], v[120:123], v[172:175]\n"
      "s_waitcnt lgkmcnt(2)\n"
      "v_add_f32 v211, v211, v239\n"
      "v_cvt_pk_bf16_f32 v211, v240, v211\n"
      "v_mfma_f32_16x16x32_bf16 v[128:131], v[200:203], v[44:47], v[128:131]\n"
      "v_permlane16_swap_b32 v208, v210\n"
      "v_mfma_f32_16x16x32_bf16 v[136:139], v[200:203], v[60:63], v[136:139]\n"
      "v_permlane16_swap_b32 v209, v211\n"
      "v_mfma_f32_16x16x32_bf16 v[144:147], v[200:203], v[76:79], v[144:147]\n"
      "s_cmp_eq_u32 s86, 5\n"
      "s_cselect_b32 s86, 0, s86\n"
      "buffer_store_dwordx4 v[208:211], v252, s[60:63], s79 offen offset:0 \n"
      "v_mfma_f32_16x16x32_bf16 v[152:155], v[200:203], v[92:95], v[152:155]\n"
      "v_mfma_f32_16x16x32_bf16 v[160:163], v[200:203], v[108:111], v[160:163]\n"
      "v_cvt_pk_bf16_f32 v208, v128, v129\n"
      "v_cvt_pk_bf16_f32 v209, v130, v131\n"
      "v_mfma_f32_16x16x32_bf16 v[168:171], v[200:203], v[124:127], v[168:171]\n"
      "s_waitcnt lgkmcnt(0)\n"
      "v_cvt_pk_bf16_f32 v212, v136, v137\n"
      "v_cvt_pk_bf16_f32 v213, v138, v139\n"
      "v_mfma_f32_16x16x32_bf16 v[132:135], v[204:207], v[44:47], v[132:135]\n"
      "v_cvt_pk_bf16_f32 v216, v144, v145\n"
      "v_cvt_pk_bf16_f32 v217, v146, v147\n"
      "v_mfma_f32_16x16x32_bf16 v[140:143], v[204:207], v[60:63], v[140:143]\n"
      "v_cvt_pk_bf16_f32 v220, v152, v153\n"
      "v_cvt_pk_bf16_f32 v221, v154, v155\n"
      "v_mfma_f32_16x16x32_bf16 v[148:151], v[204:207], v[76:79], v[148:151]\n"
      "v_cvt_pk_bf16_f32 v224, v160, v161\n"
      "v_cvt_pk_bf16_f32 v225, v162, v163\n"
      "v_mfma_f32_16x16x32_bf16 v[156:159], v[204:207], v[92:95], v[156:159]\n"
      "v_cvt_pk_bf16_f32 v228, v168, v169\n"
      "v_cvt_pk_bf16_f32 v229, v170, v171\n"
      "v_mfma_f32_16x16x32_bf16 v[164:167], v[204:207], v[108:111], v[164:167]\n"
      "v_cvt_pk_bf16_f32 v210, v132, v133\n"
      "v_cvt_pk_bf16_f32 v211, v134, v135\n"
      "v_mfma_f32_16x16x32_bf16 v[172:175], v[204:207], v[124:127], v[172:175]\n"
      "buffer_load_dword v240, v253, s[64:67], 0 offen offset:0 sc1\n"
      "v_cvt_pk_bf16_f32 v214, v140, v141\n"
      "v_cvt_pk_bf16_f32 v215, v142, v143\n"
      "v_cvt_pk_bf16_f32 v218, v148, v149\n"
      "v_cvt_pk_bf16_f32 v219, v150, v151\n"
      "v_cvt_pk_bf16_f32 v222, v156, v157\n"
      "v_cvt_pk_bf16_f32 v223, v158, v159\n"
      "v_cvt_pk_bf16_f32 v226, v164, v165\n"
      "v_cvt_pk_bf16_f32 v227, v166, v167\n"
      "v_cvt_pk_bf16_f32 v230, v172, v173\n"
      "v_cvt_pk_bf16_f32 v231, v174, v175\n"
      "buffer_store_dwordx4 v[208:211], v242, s[56:59], s85 offen offset:0 sc1\n"
      "buffer_store_dwordx4 v[212:215], v242, s[56:59], s85 offen offset:1024 sc1\n"
      "buffer_store_dwordx4 v[216:219], v242, s[56:59], s85 offen offset:2048 sc1\n"
      "buffer_store_dwordx4 v[220:223], v242, s[56:59], s85 offen offset:3072 sc1\n"
      "s_add_u32 s85, s85, 4096\n"
      "buffer_store_dwordx4 v[224:227], v242, s[56:59], s85 offen offset:0 sc1\n"
      "buffer_store_dwordx4 v[228:231], v242, s[56:59], s85 offen offset:1024 sc1\n"
      "s_add_u32 s90, %8, 8\n"
      "s_min_u32 s90, s90, 14\n"
      "s_mul_i32 s88, s90, 43\n"
      "s_lshr_b32 s88, s88, 7\n"
      "s_mul_i32 s79, s88, 3\n"
      "s_sub_u32 s90, s90, s79\n"
      "s_mul_i32 s79, %13, 3\n"
      "s_add_u32 s90, s90, s79\n"
      "s_add_u32 s88, s88, %11\n"
      "s_sub_u32 s79, s88, 5\n"
      "s_min_u32 s88, s88, s79\n"
      "s_mul_i32 s79, s90, 40960\n"
      "s_lshl_b32 s90, s90, 10\n"
      "s_mul_i32 s78, s88, 24576\n"
      "s_add_u32 s90, s90, s78\n"
      "s_lshl_b32 s88, s88, 8\n"
      "s_add_u32 s79, s79, s88\n"
      "s_lshr_b32 s88, %13, 1\n"
      "s_mul_i32 s88, s88, 245696\n"
      "s_sub_u32 s79, s79, s88\n"
      "s_waitcnt vmcnt(6)\n"
      "v_cmp_gt_u32 vcc, 4, v240\n"
      "s_nop 0\n"
      "s_cbranch_vccnz CP4_%=\n"
      "BP4_%=:\n"
      "buffer_load_dwordx4 v[0:3], v242, s[56:59], s90 offen offset:0 sc1\n"
      "s_add_u32 s90, s90, 122880\n"
      "buffer_load_dwordx4 v[4:7], v242, s[56:59], s90 offen offset:0 sc1\n"
      "s_add_u32 s90, s90, 122880\n"
      "buffer_load_dwordx4 v[8:11], v242, s[56:59], s90 offen offset:0 sc1\n"
      "s_add_u32 s90, s90, 122880\n"
      "buffer_load_dwordx4 v[12:15], v242, s[56:59], s90 offen offset:0 sc1\n"
      "s_add_u32 s90, s90, 122880\n"
      "buffer_load_dwordx4 v[16:19], v242, s[56:59], s90 offen offset:0 sc1\n"
      "s_add_u32 s90, s90, 122880\n"
      "buffer_load_dwordx4 v[20:23], v242, s[56:59], s90 offen offset:0 sc1\n"
      "s_add_u32 s90, s90, 122880\n"
      "buffer_load_dwordx4 v[24:27], v242, s[56:59], s90 offen offset:0 sc1\n"
      "s_add_u32 s90, s90, 122880\n"
      "buffer_load_dwordx4 v[28:31], v242, s[56:59], s90 offen offset:0 sc1\n"
      "s_waitcnt vmcnt(8)\n"
      "s_barrier\n"
      "v_mov_b32 v241, 5\n"
      "buffer_store_dword v241, v254, s[64:67], 0 offen offset:0 sc1\n"
      "s_waitcnt vmcnt(8)\n"
      "v_lshlrev_b32 v240, 16, v0\n"
      "v_and_b32 v0, 0xffff0000, v0\n"
      "s_waitcnt vmcnt(7)\n"
      "v_lshlrev_b32 v241, 16, v4\n"
      "v_and_b32 v4, 0xffff0000, v4\n"
      "v_add_f32 v240, v240, v241\n"
      "v_add_f32 v0, v0, v4\n"
      "s_waitcnt vmcnt(6)\n"
      "v_lshlrev_b32 v241, 16, v8\n"
      "v_and_b32 v8, 0xffff0000, v8\n"
      "v_add_f32 v240, v240, v241\n"
      "v_add_f32 v0, v0, v8\n"
      "s_waitcnt vmcnt(5)\n"
      "v_lshlrev_b32 v241, 16, v12\n"
      "v_and_b32 v12, 0xffff0000, v12\n"
      "v_add_f32 v240, v240, v241\n"
      "v_add_f32 v0, v0, v12\n"
      "s_waitcnt vmcnt(4)\n"
      "v_lshlrev_b32 v241, 16, v16\n"
      "v_and_b32 v16, 0xffff0000, v16\n"
      "v_add_f32 v240, v240, v241\n"
      "v_add_f32 v0, v0, v16\n"
      "s_waitcnt vmcnt(3)\n"
      "v_lshlrev_b32 v241, 16, v20\n"
      "v_and_b32 v20, 0xffff0000, v20\n"
      "v_add_f32 v240, v240, v241\n"
      "v_add_f32 v0, v0, v20\n"
      "s_waitcnt vmcnt(2)\n"
      "v_lshlrev_b32 v241, 16, v24\n"
      "v_and_b32 v24, 0xffff0000, v24\n"
      "v_add_f32 v240, v240, v241\n"
      "v_add_f32 v0, v0, v24\n"
      "s_waitcnt vmcnt(1)\n"
      "v_lshlrev_b32 v241, 16, v28\n"
      "v_and_b32 v28, 0xffff0000, v28\n"
      "v_add_f32 v240, v240, v241\n"
      "v_add_f32 v0, v0, v28\n"
      "v_cvt_pk_bf16_f32 v0, v240, v0\n"
      "v_lshlrev_b32 v240, 16, v2\n"
      "v_and_b32 v2, 0xffff0000, v2\n"
      "v_lshlrev_b32 v241, 16, v6\n"
      "v_and_b32 v6, 0xffff0000, v6\n"
      "v_add_f32 v240, v240, v241\n"
      "v_add_f32 v2, v2, v6\n"
      "v_lshlrev_b32 v241, 16, v10\n"
      "v_and_b32 v10, 0xffff0000, v10\n"
      "v_add_f32 v240, v240, v241\n"
      "v_add_f32 v2, v2, v10\n"
      "v_lshlrev_b32 v241, 16, v14\n"
      "v_and_b32 v14, 0xffff0000, v14\n"
      "v_add_f32 v240, v240, v241\n"
      "v_add_f32 v2, v2, v14\n"
      "v_lshlrev_b32 v241, 16, v18\n"
      "v_and_b32 v18, 0xffff0000, v18\n"
      "v_add_f32 v240, v240, v241\n"
      "v_add_f32 v2, v2, v18\n"
      "v_lshlrev_b32 v241, 16, v22\n"
      "v_and_b32 v22, 0xffff0000, v22\n"
      "v_add_f32 v240, v240, v241\n"
      "v_add_f32 v2, v2, v22\n"
      "v_lshlrev_b32 v241, 16, v26\n"
      "v_and_b32 v26, 0xffff0000, v26\n"
      "v_add_f32 v240, v240, v241\n"
      "v_add_f32 v2, v2, v26\n"
      "v_lshlrev_b32 v241, 16, v30\n"
      "v_and_b32 v30, 0xffff0000, v30\n"
      "v_add_f32 v240, v240, v241\n"
      "v_add_f32 v2, v2, v30\n"
      "v_cvt_pk_bf16_f32 v2, v240, v2\n"
      "v_lshlrev_b32 v240, 16, v1\n"
      "v_and_b32 v1, 0xffff0000, v1\n"
      "v_lshlrev_b32 v241, 16, v5\n"
      "v_and_b32 v5, 0xffff0000, v5\n"
      "v_add_f32 v240, v240, v241\n"
      "v_add_f32 v1, v1, v5\n"
      "v_lshlrev_b32 v241, 16, v9\n"
      "v_and_b32 v9, 0xffff0000, v9\n"
      "v_add_f32 v240, v240, v241\n"
      "v_add_f32 v1, v1, v9\n"
      "v_lshlrev_b32 v241, 16, v13\n"
      "v_and_b32 v13, 0xffff0000, v13\n"
      "v_add_f32 v240, v240, v241\n"
      "v_add_f32 v1, v1, v13\n"
      "v_lshlrev_b32 v241, 16, v17\n"
      "v_and_b32 v17, 0xffff0000, v17\n"
      "v_add_f32 v240, v240, v241\n"
      "v_add_f32 v1, v1, v17\n"
      "v_lshlrev_b32 v241, 16, v21\n"
      "v_and_b32 v21, 0xffff0000, v21\n"
      "v_add_f32 v240, v240, v241\n"
      "v_add_f32 v1, v1, v21\n"
      "v_lshlrev_b32 v241, 16, v25\n"
      "v_and_b32 v25, 0xffff0000, v25\n"
      "v_add_f32 v240, v240, v241\n"
      "v_add_f32 v1, v1, v25\n"
      "v_lshlrev_b32 v241, 16, v29\n"
      "v_and_b32 v29, 0xffff0000, v29\n"
      "v_add_f32 v240, v240, v241\n"
      "v_add_f32 v1, v1, v29\n"
      "v_cvt_pk_bf16_f32 v1, v240, v1\n"
      "v_lshlrev_b32 v240, 16, v3\n"
      "v_and_b32 v3, 0xffff0000, v3\n"
      "v_lshlrev_b32 v241, 16, v7\n"
      "v_and_b32 v7, 0xffff0000, v7\n"
      "v_add_f32 v240, v240, v241\n"
      "v_add_f32 v3, v3, v7\n"
      "v_lshlrev_b32 v241, 16, v11\n"
      "v_and_b32 v11, 0xffff0000, v11\n"
      "v_add_f32 v240, v240, v241\n"
      "v_add_f32 v3, v3, v11\n"
      "v_lshlrev_b32 v241, 16, v15\n"
      "v_and_b32 v15, 0xffff0000, v15\n"
      "v_add_f32 v240, v240, v241\n"
      "v_add_f32 v3, v3, v15\n"
      "v_lshlrev_b32 v241, 16, v19\n"
      "v_and_b32 v19, 0xffff0000, v19\n"
      "v_add_f32 v240, v240, v241\n"
      "v_add_f32 v3, v3, v19\n"
      "v_lshlrev_b32 v241, 16, v23\n"
      "v_and_b32 v23, 0xffff0000, v23\n"
      "v_add_f32 v240, v240, v241\n"
      "v_add_f32 v3, v3, v23\n"
      "v_lshlrev_b32 v241, 16, v27\n"
      "v_and_b32 v27, 0xffff0000, v27\n"
      "v_add_f32 v240, v240, v241\n"
      "v_add_f32 v3, v3, v27\n"
      "v_lshlrev_b32 v241, 16, v31\n"
      "v_and_b32 v31, 0xffff0000, v31\n"
      "v_add_f32 v240, v240, v241\n"
      "v_add_f32 v3, v3, v31\n"
      "v_cvt_pk_bf16_f32 v3, v240, v3\n"
      "v_permlane16_swap_b32 v0, v2\n"
      "s_nop 0\n"
      "v_permlane16_swap_b32 v1, v3\n"
      "buffer_store_dwordx4 v[0:3], v252, s[60:63], s79 offen offset:0 \n"
      "buffer_load_dword v240, v253, s[64:67], 0 offen offset:0 sc1\n"
      "s_add_u32 s90, %8, 12\n"
      "s_min_u32 s90, s90, 14\n"
      "s_mul_i32 s88, s90, 43\n"
      "s_lshr_b32 s88, s88, 7\n"
      "s_mul_i32 s79, s88, 3\n"
      "s_sub_u32 s90, s90, s79\n"
      "s_mul_i32 s79, %13, 3\n"
      "s_add_u32 s90, s90, s79\n"
      "s_add_u32 s88, s88, %11\n"
      "s_sub_u32 s79, s88, 5\n"
      "s_min_u32 s88, s88, s79\n"
      "s_mul_i32 s79, s90, 40960\n"
      "s_lshl_b32 s90, s90, 10\n"
      "s_mul_i32 s78, s88, 24576\n"
      "s_add_u32 s90, s90, s78\n"
      "s_lshl_b32 s88, s88, 8\n"
      "s_add_u32 s79, s79, s88\n"
      "s_lshr_b32 s88, %13, 1\n"
      "s_mul_i32 s88, s88, 245696\n"
      "s_sub_u32 s79, s79, s88\n"
      "s_waitcnt vmcnt(0)\n"
      "v_cmp_gt_u32 vcc, 5, v240\n"
      "s_nop 0\n"
      "s_cbranch_vccnz CP5_%=\n"
      "BP5_%=:\n"
      "buffer_load_dwordx4 v[32:35], v242, s[56:59], s90 offen offset:0 sc1\n"
      "s_add_u32 s90, s90, 122880\n"
      "buffer_load_dwordx4 v[36:39], v242, s[56:59], s90 offen offset:0 sc1\n"
      "s_add_u32 s90, s90, 122880\n"
      "buffer_load_dwordx4 v[40:43], v242, s[56:59], s90 offen offset:0 sc1\n"
      "s_add_u32 s90, s90, 122880\n"
      "buffer_load_dwordx4 v[44:47], v242, s[56:59], s90 offen offset:0 sc1\n"
      "s_add_u32 s90, s90, 122880\n"
      "buffer_load_dwordx4 v[48:51], v242, s[56:59], s90 offen offset:0 sc1\n"
      "s_add_u32 s90, s90, 122880\n"
      "buffer_load_dwordx4 v[52:55], v242, s[56:59], s90 offen offset:0 sc1\n"
      "s_add_u32 s90, s90, 122880\n"
      "buffer_load_dwordx4 v[56:59], v242, s[56:59], s90 offen offset:0 sc1\n"
      "s_add_u32 s90, s90, 122880\n"
      "buffer_load_dwordx4 v[60:63], v242, s[56:59], s90 offen offset:0 sc1\n"
      "s_waitcnt vmcnt(7)\n"
      "v_lshlrev_b32 v240, 16, v32\n"
      "v_and_b32 v32, 0xffff0000, v32\n"
      "s_waitcnt vmcnt(6)\n"
      "v_lshlrev_b32 v241, 16, v36\n"
      "v_and_b32 v36, 0xffff0000, v36\n"
      "v_add_f32 v240, v240, v241\n"
      "v_add_f32 v32, v32, v36\n"
      "s_waitcnt vmcnt(5)\n"
      "v_lshlrev_b32 v241, 16, v40\n"
      "v_and_b32 v40, 0xffff0000, v40\n"
      "v_add_f32 v240, v240, v241\n"
      "v_add_f32 v32, v32, v40\n"
      "s_waitcnt vmcnt(4)\n"
      "v_lshlrev_b32 v241, 16, v44\n"
      "v_and_b32 v44, 0xffff0000, v44\n"
      "v_add_f32 v240, v240, v241\n"
      "v_add_f32 v32, v32, v44\n"
      "s_waitcnt vmcnt(3)\n"
      "v_lshlrev_b32 v241, 16, v48\n"
      "v_and_b32 v48, 0xffff0000, v48\n"
      "v_add_f32 v240, v240, v241\n"
      "v_add_f32 v32, v32, v48\n"
      "s_waitcnt vmcnt(2)\n"
      "v_lshlrev_b32 v241, 16, v52\n"
      "v_and_b32 v52, 0xffff0000, v52\n"
      "v_add_f32 v240, v240, v241\n"
      "v_add_f32 v32, v32, v52\n"
      "s_waitcnt vmcnt(1)\n"
      "v_lshlrev_b32 v241, 16, v56\n"
      "v_and_b32 v56, 0xffff0000, v56\n"
      "v_add_f32 v240, v240, v241\n"
      "v_add_f32 v32, v32, v56\n"
      "s_waitcnt vmcnt(0)\n"
      "v_lshlrev_b32 v241, 16, v60\n"
      "v_and_b32 v60, 0xffff0000, v60\n"
      "v_add_f32 v240, v240, v241\n"
      "v_add_f32 v32, v32, v60\n"
      "v_cvt_pk_bf16_f32 v32, v240, v32\n"
      "v_lshlrev_b32 v240, 16, v34\n"
      "v_and_b32 v34, 0xffff0000, v34\n"
      "v_lshlrev_b32 v241, 16, v38\n"
      "v_and_b32 v38, 0xffff0000, v38\n"
      "v_add_f32 v240, v240, v241\n"
      "v_add_f32 v34, v34, v38\n"
      "v_lshlrev_b32 v241, 16, v42\n"
      "v_and_b32 v42, 0xffff0000, v42\n"
      "v_add_f32 v240, v240, v241\n"
      "v_add_f32 v34, v34, v42\n"
      "v_lshlrev_b32 v241, 16, v46\n"
      "v_and_b32 v46, 0xffff0000, v46\n"
      "v_add_f32 v240, v240, v241\n"
      "v_add_f32 v34, v34, v46\n"
      "v_lshlrev_b32 v241, 16, v50\n"
      "v_and_b32 v50, 0xffff0000, v50\n"
      "v_add_f32 v240, v240, v241\n"
      "v_add_f32 v34, v34, v50\n"
      "v_lshlrev_b32 v241, 16, v54\n"
      "v_and_b32 v54, 0xffff0000, v54\n"
      "v_add_f32 v240, v240, v241\n"
      "v_add_f32 v34, v34, v54\n"
      "v_lshlrev_b32 v241, 16, v58\n"
      "v_and_b32 v58, 0xffff0000, v58\n"
      "v_add_f32 v240, v240, v241\n"
      "v_add_f32 v34, v34, v58\n"
      "v_lshlrev_b32 v241, 16, v62\n"
      "v_and_b32 v62, 0xffff0000, v62\n"
      "v_add_f32 v240, v240, v241\n"
      "v_add_f32 v34, v34, v62\n"
      "v_cvt_pk_bf16_f32 v34, v240, v34\n"
      "v_lshlrev_b32 v240, 16, v33\n"
      "v_and_b32 v33, 0xffff0000, v33\n"
      "v_lshlrev_b32 v241, 16, v37\n"
      "v_and_b32 v37, 0xffff0000, v37\n"
      "v_add_f32 v240, v240, v241\n"
      "v_add_f32 v33, v33, v37\n"
      "v_lshlrev_b32 v241, 16, v41\n"
      "v_and_b32 v41, 0xffff0000, v41\n"
      "v_add_f32 v240, v240, v241\n"
      "v_add_f32 v33, v33, v41\n"
      "v_lshlrev_b32 v241, 16, v45\n"
      "v_and_b32 v45, 0xffff0000, v45\n"
      "v_add_f32 v240, v240, v241\n"
      "v_add_f32 v33, v33, v45\n"
      "v_lshlrev_b32 v241, 16, v49\n"
      "v_and_b32 v49, 0xffff0000, v49\n"
      "v_add_f32 v240, v240, v241\n"
      "v_add_f32 v33, v33, v49\n"
      "v_lshlrev_b32 v241, 16, v53\n"
      "v_and_b32 v53, 0xffff0000, v53\n"
      "v_add_f32 v240, v240, v241\n"
      "v_add_f32 v33, v33, v53\n"
      "v_lshlrev_b32 v241, 16, v57\n"
      "v_and_b32 v57, 0xffff0000, v57\n"
      "v_add_f32 v240, v240, v241\n"
      "v_add_f32 v33, v33, v57\n"
      "v_lshlrev_b32 v241, 16, v61\n"
      "v_and_b32 v61, 0xffff0000, v61\n"
      "v_add_f32 v240, v240, v241\n"
      "v_add_f32 v33, v33, v61\n"
      "v_cvt_pk_bf16_f32 v33, v240, v33\n"
      "v_lshlrev_b32 v240, 16, v35\n"
      "v_and_b32 v35, 0xffff0000, v35\n"
      "v_lshlrev_b32 v241, 16, v39\n"
      "v_and_b32 v39, 0xffff0000, v39\n"
      "v_add_f32 v240, v240, v241\n"
      "v_add_f32 v35, v35, v39\n"
      "v_lshlrev_b32 v241, 16, v43\n"
      "v_and_b32 v43, 0xffff0000, v43\n"
      "v_add_f32 v240, v240, v241\n"
      "v_add_f32 v35, v35, v43\n"
      "v_lshlrev_b32 v241, 16, v47\n"
      "v_and_b32 v47, 0xffff0000, v47\n"
      "v_add_f32 v240, v240, v241\n"
      "v_add_f32 v35, v35, v47\n"
      "v_lshlrev_b32 v241, 16, v51\n"
      "v_and_b32 v51, 0xffff0000, v51\n"
      "v_add_f32 v240, v240, v241\n"
      "v_add_f32 v35, v35, v51\n"
      "v_lshlrev_b32 v241, 16, v55\n"
      "v_and_b32 v55, 0xffff0000, v55\n"
      "v_add_f32 v240, v240, v241\n"
      "v_add_f32 v35, v35, v55\n"
      "v_lshlrev_b32 v241, 16, v59\n"
      "v_and_b32 v59, 0xffff0000, v59\n"
      "v_add_f32 v240, v240, v241\n"
      "v_add_f32 v35, v35, v59\n"
      "v_lshlrev_b32 v241, 16, v63\n"
      "v_and_b32 v63, 0xffff0000, v63\n"
      "v_add_f32 v240, v240, v241\n"
      "v_add_f32 v35, v35, v63\n"
      "v_cvt_pk_bf16_f32 v35, v240, v35\n"
      "v_permlane16_swap_b32 v32, v34\n"
      "s_nop 0\n"
      "v_permlane16_swap_b32 v33, v35\n"
      "buffer_store_dwordx4 v[32:35], v252, s[60:63], s79 offen offset:0 \n"
      "s_branch LEND_%=\n"
      "CRx_0_%=:\n"
      "s_call_b64 s[92:93], RARE0_%=\n"
      "s_branch BRx_0_%=\n"
      "CRx_1_%=:\n"
      "s_call_b64 s[92:93], RARE1_%=\n"
      "s_branch BRx_1_%=\n"
      "CRx_2_%=:\n"
      "s_call_b64 s[92:93], RARE2_%=\n"
      "s_branch BRx_2_%=\n"
      "CRx_3_%=:\n"
      "s_call_b64 s[92:93], RARE3_%=\n"
      "s_branch BRx_3_%=\n"
      "CRx_4_%=:\n"
      "s_call_b64 s[92:93], RARE4_%=\n"
      "s_branch BRx_4_%=\n"
      "CRx_5_%=:\n"
      "s_call_b64 s[92:93], RARE5_%=\n"
      "s_branch BRx_5_%=\n"
      "CRy_0_%=:\n"
      "s_call_b64 s[92:93], RARE0_%=\n"
      "s_branch BRy_0_%=\n"
      "CRy_1_%=:\n"
      "s_call_b64 s[92:93], RARE1_%=\n"
      "s_branch BRy_1_%=\n"
      "CRy_2_%=:\n"
      "s_call_b64 s[92:93], RARE2_%=\n"
      "s_branch BRy_2_%=\n"
      "CRy_3_%=:\n"
      "s_call_b64 s[92:93], RARE3_%=\n"
      "s_branch BRy_3_%=\n"
      "CRy_4_%=:\n"
      "s_call_b64 s[92:93], RARE4_%=\n"
      "s_branch BRy_4_%=\n"
      "CRy_5_%=:\n"
      "s_call_b64 s[92:93], RARE5_%=\n"
      "s_branch BRy_5_%=\n"
      "CRz_0_%=:\n"
      "s_call_b64 s[92:93], RARE0_%=\n"
      "s_branch BRz_0_%=\n"
      "CRz_1_%=:\n"
      "s_call_b64 s[92:93], RARE1_%=\n"
      "s_branch BRz_1_%=\n"
      "CRz_2_%=:\n"
      "s_call_b64 s[92:93], RARE2_%=\n"
      "s_branch BRz_2_%=\n"
      "CRz_3_%=:\n"
      "s_call_b64 s[92:93], RARE3_%=\n"
      "s_branch BRz_3_%=\n"
      "CRz_4_%=:\n"
      "s_call_b64 s[92:93], RARE4_%=\n"
      "s_branch BRz_4_%=\n"
      "CRz_5_%=:\n"
      "s_call_b64 s[92:93], RARE5_%=\n"
      "s_branch BRz_5_%=\n"
      "CRa_0_%=:\n"
      "s_call_b64 s[92:93], RARE0_%=\n"
      "s_branch BRa_0_%=\n"
      "CRa_1_%=:\n"
      "s_call_b64 s[92:93], RARE1_%=\n"
      "s_branch BRa_1_%=\n"
      "CRa_2_%=:\n"
      "s_call_b64 s[92:93], RARE2_%=\n"
      "s_branch BRa_2_%=\n"
      "CRa_3_%=:\n"
      "s_call_b64 s[92:93], RARE3_%=\n"
      "s_branch BRa_3_%=\n"
      "CRa_4_%=:\n"
      "s_call_b64 s[92:93], RARE4_%=\n"
      "s_branch BRa_4_%=\n"
      "CRa_5_%=:\n"
      "s_call_b64 s[92:93], RARE5_%=\n"
      "s_branch BRa_5_%=\n"
      "CRb_0_%=:\n"
      "s_call_b64 s[92:93], RARE0_%=\n"
      "s_branch BRb_0_%=\n"
      "CRb_1_%=:\n"
      "s_call_b64 s[92:93], RARE1_%=\n"
      "s_branch BRb_1_%=\n"
      "CRb_2_%=:\n"
      "s_call_b64 s[92:93], RARE2_%=\n"
      "s_branch BRb_2_%=\n"
      "CRb_3_%=:\n"
      "s_call_b64 s[92:93], RARE3_%=\n"
      "s_branch BRb_3_%=\n"
      "CRb_4_%=:\n"
      "s_call_b64 s[92:93], RARE4_%=\n"
      "s_branch BRb_4_%=\n"
      "CRb_5_%=:\n"
      "s_call_b64 s[92:93], RARE5_%=\n"
      "s_branch BRb_5_%=\n"
      "CRc_0_%=:\n"
      "s_call_b64 s[92:93], RARE0_%=\n"
      "s_branch BRc_0_%=\n"
      "CRc_1_%=:\n"
      "s_call_b64 s[92:93], RARE1_%=\n"
      "s_branch BRc_1_%=\n"
      "CRc_2_%=:\n"
      "s_call_b64 s[92:93], RARE2_%=\n"
      "s_branch BRc_2_%=\n"
      "CRc_3_%=:\n"
      "s_call_b64 s[92:93], RARE3_%=\n"
      "s_branch BRc_3_%=\n"
      "CRc_4_%=:\n"
      "s_call_b64 s[92:93], RARE4_%=\n"
      "s_branch BRc_4_%=\n"
      "CRc_5_%=:\n"
      "s_call_b64 s[92:93], RARE5_%=\n"
      "s_branch BRc_5_%=\n"
      "RARE0_%=:\n"
      "s_nop 7\n"
      "v_mov_b32 v229, v228\n"
      "s_nop 1\n"
      "v_permlane16_swap_b32 v228, v229\n"
      "v_max_f32 v228, v228, v229\n"
      "v_mov_b32 v229, v228\n"
      "s_nop 1\n"
      "v_permlane32_swap_b32 v228, v229\n"
      "v_max_f32 v228, v228, v229\n"
      "v_max_f32 v228, 0, v228\n"
      "v_exp_f32 v230, -v228\n"
      "v_accvgpr_read_b32 v229, a224\n"
      "v_mul_f32 v229, v229, v230\n"
      "v_accvgpr_write_b32 a224, v229\n"
      "v_mul_f32 v0, v0, v230\n"
      "v_mul_f32 v1, v1, v230\n"
      "v_mul_f32 v2, v2, v230\n"
      "v_mul_f32 v3, v3, v230\n"
      "v_mul_f32 v4, v4, v230\n"
      "v_mul_f32 v5, v5, v230\n"
      "v_mul_f32 v6, v6, v230\n"
      "v_mul_f32 v7, v7, v230\n"
      "v_mul_f32 v8, v8, v230\n"
      "v_mul_f32 v9, v9, v230\n"
      "v_mul_f32 v10, v10, v230\n"
      "v_mul_f32 v11, v11, v230\n"
      "v_mul_f32 v12, v12, v230\n"
      "v_mul_f32 v13, v13, v230\n"
      "v_mul_f32 v14, v14, v230\n"
      "v_mul_f32 v15, v15, v230\n"
      "v_mul_f32 v16, v16, v230\n"
      "v_mul_f32 v17, v17, v230\n"
      "v_mul_f32 v18, v18, v230\n"
      "v_mul_f32 v19, v19, v230\n"
      "v_mul_f32 v20, v20, v230\n"
      "v_mul_f32 v21, v21, v230\n"
      "v_mul_f32 v22, v22, v230\n"
      "v_mul_f32 v23, v23, v230\n"
      "v_mul_f32 v24, v24, v230\n"
      "v_mul_f32 v25, v25, v230\n"
      "v_mul_f32 v26, v26, v230\n"
      "v_mul_f32 v27, v27, v230\n"
      "v_mul_f32 v28, v28, v230\n"
      "v_mul_f32 v29, v29, v230\n"
      "v_mul_f32 v30, v30, v230\n"
      "v_mul_f32 v31, v31, v230\n"
      "v_add_f32 v216, v216, v228\n"
      "v_sub_f32 v192, v192, v228\n"
      "v_sub_f32 v193, v193, v228\n"
      "v_sub_f32 v194, v194, v228\n"
      "v_sub_f32 v195, v195, v228\n"
      "v_sub_f32 v196, v196, v228\n"
      "v_sub_f32 v197, v197, v228\n"
      "v_sub_f32 v198, v198, v228\n"
      "v_sub_f32 v199, v199, v228\n"
      "s_nop 1\n"
      "s_setpc_b64 s[92:93]\n"
      "RARE1_%=:\n"
      "s_nop 7\n"
      "v_mov_b32 v229, v228\n"
      "s_nop 1\n"
      "v_permlane16_swap_b32 v228, v229\n"
      "v_max_f32 v228, v228, v229\n"
      "v_mov_b32 v229, v228\n"
      "s_nop 1\n"
      "v_permlane32_swap_b32 v228, v229\n"
      "v_max_f32 v228, v228, v229\n"
      "v_max_f32 v228, 0, v228\n"
      "v_exp_f32 v230, -v228\n"
      "v_accvgpr_read_b32 v229, a228\n"
      "v_mul_f32 v229, v229, v230\n"
      "v_accvgpr_write_b32 a228, v229\n"
      "v_mul_f32 v32, v32, v230\n"
      "v_mul_f32 v33, v33, v230\n"
      "v_mul_f32 v34, v34, v230\n"
      "v_mul_f32 v35, v35, v230\n"
      "v_mul_f32 v36, v36, v230\n"
      "v_mul_f32 v37, v37, v230\n"
      "v_mul_f32 v38, v38, v230\n"
      "v_mul_f32 v39, v39, v230\n"
      "v_mul_f32 v40, v40, v230\n"
      "v_mul_f32 v41, v41, v230\n"
      "v_mul_f32 v42, v42, v230\n"
      "v_mul_f32 v43, v43, v230\n"
      "v_mul_f32 v44, v44, v230\n"
      "v_mul_f32 v45, v45, v230\n"
      "v_mul_f32 v46, v46, v230\n"
      "v_mul_f32 v47, v47, v230\n"
      "v_mul_f32 v48, v48, v230\n"
      "v_mul_f32 v49, v49, v230\n"
      "v_mul_f32 v50, v50, v230\n"
      "v_mul_f32 v51, v51, v230\n"
      "v_mul_f32 v52, v52, v230\n"
      "v_mul_f32 v53, v53, v230\n"
      "v_mul_f32 v54, v54, v230\n"
      "v_mul_f32 v55, v55, v230\n"
      "v_mul_f32 v56, v56, v230\n"
      "v_mul_f32 v57, v57, v230\n"
      "v_mul_f32 v58, v58, v230\n"
      "v_mul_f32 v59, v59, v230\n"
      "v_mul_f32 v60, v60, v230\n"
      "v_mul_f32 v61, v61, v230\n"
      "v_mul_f32 v62, v62, v230\n"
      "v_mul_f32 v63, v63, v230\n"
      "v_add_f32 v217, v217, v228\n"
      "v_sub_f32 v200, v200, v228\n"
      "v_sub_f32 v201, v201, v228\n"
      "v_sub_f32 v202, v202, v228\n"
      "v_sub_f32 v203, v203, v228\n"
      "v_sub_f32 v204, v204, v228\n"
      "v_sub_f32 v205, v205, v228\n"
      "v_sub_f32 v206, v206, v228\n"
      "v_sub_f32 v207, v207, v228\n"
      "s_nop 1\n"
      "s_setpc_b64 s[92:93]\n"
      "RARE2_%=:\n"
      "s_nop 7\n"
      "v_mov_b32 v229, v228\n"
      "s_nop 1\n"
      "v_permlane16_swap_b32 v228, v229\n"
      "v_max_f32 v228, v228, v229\n"
      "v_mov_b32 v229, v228\n"
      "s_nop 1\n"
      "v_permlane32_swap_b32 v228, v229\n"
      "v_max_f32 v228, v228, v229\n"
      "v_max_f32 v228, 0, v228\n"
      "v_exp_f32 v230, -v228\n"
      "v_accvgpr_read_b32 v229, a232\n"
      "v_mul_f32 v229, v229, v230\n"
      "v_accvgpr_write_b32 a232, v229\n"
      "v_mul_f32 v64, v64, v230\n"
      "v_mul_f32 v65, v65, v230\n"
      "v_mul_f32 v66, v66, v230\n"
      "v_mul_f32 v67, v67, v230\n"
      "v_mul_f32 v68, v68, v230\n"
      "v_mul_f32 v69, v69, v230\n"
      "v_mul_f32 v70, v70, v230\n"
      "v_mul_f32 v71, v71, v230\n"
      "v_mul_f32 v72, v72, v230\n"
      "v_mul_f32 v73, v73, v230\n"
      "v_mul_f32 v74, v74, v230\n"
      "v_mul_f32 v75, v75, v230\n"
      "v_mul_f32 v76, v76, v230\n"
      "v_mul_f32 v77, v77, v230\n"
      "v_mul_f32 v78, v78, v230\n"
      "v_mul_f32 v79, v79, v230\n"
      "v_mul_f32 v80, v80, v230\n"
      "v_mul_f32 v81, v81, v230\n"
      "v_mul_f32 v82, v82, v230\n"
      "v_mul_f32 v83, v83, v230\n"
      "v_mul_f32 v84, v84, v230\n"
      "v_mul_f32 v85, v85, v230\n"
      "v_mul_f32 v86, v86, v230\n"
      "v_mul_f32 v87, v87, v230\n"
      "v_mul_f32 v88, v88, v230\n"
      "v_mul_f32 v89, v89, v230\n"
      "v_mul_f32 v90, v90, v230\n"
      "v_mul_f32 v91, v91, v230\n"
      "v_mul_f32 v92, v92, v230\n"
      "v_mul_f32 v93, v93, v230\n"
      "v_mul_f32 v94, v94, v230\n"
      "v_mul_f32 v95, v95, v230\n"
      "v_add_f32 v218, v218, v228\n"
      "v_sub_f32 v192, v192, v228\n"
      "v_sub_f32 v193, v193, v228\n"
      "v_sub_f32 v194, v194, v228\n"
      "v_sub_f32 v195, v195, v228\n"
      "v_sub_f32 v196, v196, v228\n"
      "v_sub_f32 v197, v197, v228\n"
      "v_sub_f32 v198, v198, v228\n"
      "v_sub_f32 v199, v199, v228\n"
      "s_nop 1\n"
      "s_setpc_b64 s[92:93]\n"
      "RARE3_%=:\n"
      "s_nop 7\n"
      "v_mov_b32 v229, v228\n"
      "s_nop 1\n"
      "v_permlane16_swap_b32 v228, v229\n"
      "v_max_f32 v228, v228, v229\n"
      "v_mov_b32 v229, v228\n"
      "s_nop 1\n"
      "v_permlane32_swap_b32 v228, v229\n"
      "v_max_f32 v228, v228, v229\n"
      "v_max_f32 v228, 0, v228\n"
      "v_exp_f32 v230, -v228\n"
      "v_accvgpr_read_b32 v229, a236\n"
      "v_mul_f32 v229, v229, v230\n"
      "v_accvgpr_write_b32 a236, v229\n"
      "v_mul_f32 v96, v96, v230\n"
      "v_mul_f32 v97, v97, v230\n"
      "v_mul_f32 v98, v98, v230\n"
      "v_mul_f32 v99, v99, v230\n"
      "v_mul_f32 v100, v100, v230\n"
      "v_mul_f32 v101, v101, v230\n"
      "v_mul_f32 v102, v102, v230\n"
      "v_mul_f32 v103, v103, v230\n"
      "v_mul_f32 v104, v104, v230\n"
      "v_mul_f32 v105, v105, v230\n"
      "v_mul_f32 v106, v106, v230\n"
      "v_mul_f32 v107, v107, v230\n"
      "v_mul_f32 v108, v108, v230\n"
      "v_mul_f32 v109, v109, v230\n"
      "v_mul_f32 v110, v110, v230\n"
      "v_mul_f32 v111, v111, v230\n"
      "v_mul_f32 v112, v112, v230\n"
      "v_mul_f32 v113, v113, v230\n"
      "v_mul_f32 v114, v114, v230\n"
      "v_mul_f32 v115, v115, v230\n"
      "v_mul_f32 v116, v116, v230\n"
      "v_mul_f32 v117, v117, v230\n"
      "v_mul_f32 v118, v118, v230\n"
      "v_mul_f32 v119, v119, v230\n"
      "v_mul_f32 v120, v120, v230\n"
      "v_mul_f32 v121, v121, v230\n"
      "v_mul_f32 v122, v122, v230\n"
      "v_mul_f32 v123, v123, v230\n"
      "v_mul_f32 v124, v124, v230\n"
      "v_mul_f32 v125, v125, v230\n"
      "v_mul_f32 v126, v126, v230\n"
      "v_mul_f32 v127, v127, v230\n"
      "v_add_f32 v219, v219, v228\n"
      "v_sub_f32 v200, v200, v228\n"
      "v_sub_f32 v201, v201, v228\n"
      "v_sub_f32 v202, v202, v228\n"
      "v_sub_f32 v203, v203, v228\n"
      "v_sub_f32 v204, v204, v228\n"
      "v_sub_f32 v205, v205, v228\n"
      "v_sub_f32 v206, v206, v228\n"
      "v_sub_f32 v207, v207, v228\n"
      "s_nop 1\n"
      "s_setpc_b64 s[92:93]\n"
      "RARE4_%=:\n"
      "s_nop 7\n"
      "v_mov_b32 v229, v228\n"
      "s_nop 1\n"
      "v_permlane16_swap_b32 v228, v229\n"
      "v_max_f32 v228, v228, v229\n"
      "v_mov_b32 v229, v228\n"
      "s_nop 1\n"
      "v_permlane32_swap_b32 v228, v229\n"
      "v_max_f32 v228, v228, v229\n"
      "v_max_f32 v228, 0, v228\n"
      "v_exp_f32 v230, -v228\n"
      "v_accvgpr_read_b32 v229, a240\n"
      "v_mul_f32 v229, v229, v230\n"
      "v_accvgpr_write_b32 a240, v229\n"
      "v_mul_f32 v128, v128, v230\n"
      "v_mul_f32 v129, v129, v230\n"
      "v_mul_f32 v130, v130, v230\n"
      "v_mul_f32 v131, v131, v230\n"
      "v_mul_f32 v132, v132, v230\n"
      "v_mul_f32 v133, v133, v230\n"
      "v_mul_f32 v134, v134, v230\n"
      "v_mul_f32 v135, v135, v230\n"
      "v_mul_f32 v136, v136, v230\n"
      "v_mul_f32 v137, v137, v230\n"
      "v_mul_f32 v138, v138, v230\n"
      "v_mul_f32 v139, v139, v230\n"
      "v_mul_f32 v140, v140, v230\n"
      "v_mul_f32 v141, v141, v230\n"
      "v_mul_f32 v142, v142, v230\n"
      "v_mul_f32 v143, v143, v230\n"
      "v_mul_f32 v144, v144, v230\n"
      "v_mul_f32 v145, v145, v230\n"
      "v_mul_f32 v146, v146, v230\n"
      "v_mul_f32 v147, v147, v230\n"
      "v_mul_f32 v148, v148, v230\n"
      "v_mul_f32 v149, v149, v230\n"
      "v_mul_f32 v150, v150, v230\n"
      "v_mul_f32 v151, v151, v230\n"
      "v_mul_f32 v152, v152, v230\n"
      "v_mul_f32 v153, v153, v230\n"
      "v_mul_f32 v154, v154, v230\n"
      "v_mul_f32 v155, v155, v230\n"
      "v_mul_f32 v156, v156, v230\n"
      "v_mul_f32 v157, v157, v230\n"
      "v_mul_f32 v158, v158, v230\n"
      "v_mul_f32 v159, v159, v230\n"
      "v_add_f32 v220, v220, v228\n"
      "v_sub_f32 v192, v192, v228\n"
      "v_sub_f32 v193, v193, v228\n"
      "v_sub_f32 v194, v194, v228\n"
      "v_sub_f32 v195, v195, v228\n"
      "v_sub_f32 v196, v196, v228\n"
      "v_sub_f32 v197, v197, v228\n"
      "v_sub_f32 v198, v198, v228\n"
      "v_sub_f32 v199, v199, v228\n"
      "s_nop 1\n"
      "s_setpc_b64 s[92:93]\n"
      "RARE5_%=:\n"
      "s_nop 7\n"
      "v_mov_b32 v229, v228\n"
      "s_nop 1\n"
      "v_permlane16_swap_b32 v228, v229\n"
      "v_max_f32 v228, v228, v229\n"
      "v_mov_b32 v229, v228\n"
      "s_nop 1\n"
      "v_permlane32_swap_b32 v228, v229\n"
      "v_max_f32 v228, v228, v229\n"
      "v_max_f32 v228, 0, v228\n"
      "v_exp_f32 v230, -v228\n"
      "v_accvgpr_read_b32 v229, a244\n"
      "v_mul_f32 v229, v229, v230\n"
      "v_accvgpr_write_b32 a244, v229\n"
      "v_mul_f32 v160, v160, v230\n"
      "v_mul_f32 v161, v161, v230\n"
      "v_mul_f32 v162, v162, v230\n"
      "v_mul_f32 v163, v163, v230\n"
      "v_mul_f32 v164, v164, v230\n"
      "v_mul_f32 v165, v165, v230\n"
      "v_mul_f32 v166, v166, v230\n"
      "v_mul_f32 v167, v167, v230\n"
      "v_mul_f32 v168, v168, v230\n"
      "v_mul_f32 v169, v169, v230\n"
      "v_mul_f32 v170, v170, v230\n"
      "v_mul_f32 v171, v171, v230\n"
      "v_mul_f32 v172, v172, v230\n"
      "v_mul_f32 v173, v173, v230\n"
      "v_mul_f32 v174, v174, v230\n"
      "v_mul_f32 v175, v175, v230\n"
      "v_mul_f32 v176, v176, v230\n"
      "v_mul_f32 v177, v177, v230\n"
      "v_mul_f32 v178, v178, v230\n"
      "v_mul_f32 v179, v179, v230\n"
      "v_mul_f32 v180, v180, v230\n"
      "v_mul_f32 v181, v181, v230\n"
      "v_mul_f32 v182, v182, v230\n"
      "v_mul_f32 v183, v183, v230\n"
      "v_mul_f32 v184, v184, v230\n"
      "v_mul_f32 v185, v185, v230\n"
      "v_mul_f32 v186, v186, v230\n"
      "v_mul_f32 v187, v187, v230\n"
      "v_mul_f32 v188, v188, v230\n"
      "v_mul_f32 v189, v189, v230\n"
      "v_mul_f32 v190, v190, v230\n"
      "v_mul_f32 v191, v191, v230\n"
      "v_add_f32 v221, v221, v228\n"
      "v_sub_f32 v200, v200, v228\n"
      "v_sub_f32 v201, v201, v228\n"
      "v_sub_f32 v202, v202, v228\n"
      "v_sub_f32 v203, v203, v228\n"
      "v_sub_f32 v204, v204, v228\n"
      "v_sub_f32 v205, v205, v228\n"
      "v_sub_f32 v206, v206, v228\n"
      "v_sub_f32 v207, v207, v228\n"
      "s_nop 1\n"
      "s_setpc_b64 s[92:93]\n"
      "CP2_%=:\n"
      "s_mov_b32 s91, 262144\n"
      "LP2_%=:\n"
      "s_sleep 1\n"
      "buffer_load_dword v240, v253, s[64:67], 0 offen offset:0 sc1\n"
      "s_waitcnt vmcnt(0)\n"
      "v_cmp_gt_u32 vcc, 2, v240\n"
      "s_sub_u32 s91, s91, 1\n"
      "s_cmp_eq_u32 s91, 0\n"
      "s_cbranch_scc1 BP2_%=\n"
      "s_cbranch_vccnz LP2_%=\n"
      "s_branch BP2_%=\n"
      "CP3_%=:\n"
      "s_mov_b32 s91, 262144\n"
      "LP3_%=:\n"
      "s_sleep 1\n"
      "buffer_load_dword v240, v253, s[64:67], 0 offen offset:0 sc1\n"
      "s_waitcnt vmcnt(0)\n"
      "v_cmp_gt_u32 vcc, 3, v240\n"
      "s_sub_u32 s91, s91, 1\n"
      "s_cmp_eq_u32 s91, 0\n"
      "s_cbranch_scc1 BP3_%=\n"
      "s_cbranch_vccnz LP3_%=\n"
      "s_branch BP3_%=\n"
      "CP4_%=:\n"
      "s_mov_b32 s91, 262144\n"
      "LP4_%=:\n"
      "s_sleep 1\n"
      "buffer_load_dword v240, v253, s[64:67], 0 offen offset:0 sc1\n"
      "s_waitcnt vmcnt(0)\n"
      "v_cmp_gt_u32 vcc, 4, v240\n"
      "s_sub_u32 s91, s91, 1\n"
      "s_cmp_eq_u32 s91, 0\n"
      "s_cbranch_scc1 BP4_%=\n"
      "s_cbranch_vccnz LP4_%=\n"
      "s_branch BP4_%=\n"
      "CP5_%=:\n"
      "s_mov_b32 s91, 262144\n"
      "LP5_%=:\n"
      "s_sleep 1\n"
      "buffer_load_dword v240, v253, s[64:67], 0 offen offset:0 sc1\n"
      "s_waitcnt vmcnt(0)\n"
      "v_cmp_gt_u32 vcc, 5, v240\n"
      "s_sub_u32 s91, s91, 1\n"
      "s_cmp_eq_u32 s91, 0\n"
      "s_cbranch_scc1 BP5_%=\n"
      "s_cbranch_vccnz LP5_%=\n"
      "s_branch BP5_%=\n"
      "HSLOOP_%=:\n"
      "s_mov_b32 s91, 262144\n"
      "v_lshrrev_b32 v192, 4, v242\n"
      "HSL_%=:\n"
      "s_sleep 4\n"
      "s_lshl_b32 s90, %13, 2\n"
      "s_add_u32 s90, s90, 256\n"
      "v_and_b32 v199, 7, v192\n"
      "v_lshl_add_u32 v199, v199, 6, s90\n"
      "buffer_load_dword v198, v199, s[64:67], 0 offen offset:0 sc1\n"
      "s_waitcnt vmcnt(0)\n"
      "v_cmp_ne_u32 vcc, s89, v198\n"
      "s_nop 0\n"
      "s_mov_b64 s[94:95], vcc\n"
      "v_and_b32 v199, 7, v192\n"
      "v_lshlrev_b32 v199, 2, v199\n"
      "buffer_load_dword v198, v199, s[64:67], 0 offen offset:0 sc1\n"
      "s_lshl_b32 s90, %13, 6\n"
      "s_add_u32 s90, s90, 256\n"
      "v_lshl_add_u32 v199, v192, 2, s90\n"
      "v_add_u32 v196, s72, v192\n"
      "v_cmp_gt_u32 vcc, 8, v196\n"
      "v_mov_b32 v196, 0x40000000\n"
      "s_nop 0\n"
      "v_cndmask_b32 v199, v196, v199, vcc\n"
      "s_waitcnt vmcnt(0)\n"
      "buffer_store_dword v198, v199, s[64:67], 0 offen offset:0 sc1\n"
      "s_waitcnt vmcnt(0)\n"
      "s_mov_b64 vcc, s[94:95]\n"
      "s_sub_u32 s91, s91, 1\n"
      "s_cmp_eq_u32 s91, 0\n"
      "s_cbranch_scc1 HSDONE_%=\n"
      "s_cbranch_vccnz HSL_%=\n"
      "s_branch HSDONE_%=\n"
      "LEND_%=:\n"
      :
      : "s"(px), "s"(pw), "s"(pk), "s"(pv), "s"(po), "s"(pg), "s"(pout), "s"(pc), "s"(w), "s"(rot_c), "s"(rot_kv), "s"(rot_b),
        "s"(rot_h), "s"(hg), "s"(pd), "s"(loc * 4 + w), "s"(str_kv), "s"((unsigned)(uintptr_t)smem)
      : "v0", "v1", "v2", "v3", "v4", "v5", "v6", "v7", "v8", "v9", "v10", "v11", "v12", "v13", "v14", "v15", "v16", "v17", "v18", "v19", "v20", "v21", "v22", "v23", "v24", "v25", "v26", "v27", "v28", "v29", "v30", "v31", "v32", "v33", "v34", "v35", "v36", "v37", "v38", "v39", "v40", "v41", "v42", "v43", "v44", "v45", "v46", "v47", "v48", "v49", "v50", "v51", "v52", "v53", "v54", "v55", "v56", "v57", "v58", "v59", "v60", "v61", "v62", "v63", "v64", "v65", "v66", "v67", "v68", "v69", "v70", "v71", "v72", "v73", "v74", "v75", "v76", "v77", "v78", "v79", "v80", "v81", "v82", "v83", "v84", "v85", "v86", "v87", "v88", "v89", "v90", "v91", "v92", "v93", "v94", "v95", "v96", "v97", "v98", "v99", "v100", "v101", "v102", "v103", "v104", "v105", "v106", "v107", "v108", "v109", "v110", "v111", "v112", "v113", "v114", "v115", "v116", "v117", "v118", "v119", "v120", "v121", "v122", "v123", "v124", "v125", "v126", "v127", "v128", "v129", "v130", "v131", "v132", "v133", "v134", "v135", "v136", "v137", "v138", "v139", "v140", "v141", "v142", "v143", "v144", "v145", "v146", "v147", "v148", "v149", "v150", "v151", "v152", "v153", "v154", "v155", "v156", "v157", "v158", "v159", "v160", "v161", "v162", "v163", "v164", "v165", "v166", "v167", "v168", "v169", "v170", "v171", "v172", "v173", "v174", "v175", "v176", "v177", "v178", "v179", "v180", "v181", "v182", "v183", "v184", "v185", "v186", "v187", "v188", "v189", "v190", "v191", "v192", "v193", "v194", "v195", "v196", "v197", "v198", "v199", "v200", "v201", "v202", "v203", "v204", "v205", "v206", "v207", "v208", "v209", "v210", "v211", "v212", "v213", "v214", "v215", "v216", "v217", "v218", "v219", "v220", "v221", "v222", "v223", "v224", "v225", "v226", "v227", "v228", "v229", "v230", "v231", "v232", "v233", "v234", "v235", "v236", "v237", "v238", "v239", "v240", "v241", "v242", "v243", "v244", "v245", "v246", "v247", "v248", "v249", "v250", "v251", "v252", "v253", "v254", "v255",
      "a0", "a1", "a2", "a3", "a4", "a5", "a6", "a7", "a8", "a9", "a10", "a11", "a12", "a13", "a14", "a15", "a16", "a17", "a18", "a19", "a20", "a21", "a22", "a23", "a24", "a25", "a26", "a27", "a28", "a29", "a30", "a31", "a32", "a33", "a34", "a35", "a36", "a37", "a38", "a39", "a40", "a41", "a42", "a43", "a44", "a45", "a46", "a47", "a48", "a49", "a50", "a51", "a52", "a53", "a54", "a55", "a56", "a57", "a58", "a59", "a60", "a61", "a62", "a63", "a64", "a65", "a66", "a67", "a68", "a69", "a70", "a71", "a72", "a73", "a74", "a75", "a76", "a77", "a78", "a79", "a80", "a81", "a82", "a83", "a84", "a85", "a86", "a87", "a88", "a89", "a90", "a91", "a92", "a93", "a94", "a95", "a96", "a97", "a98", "a99", "a100", "a101", "a102", "a103", "a104", "a105", "a106", "a107", "a108", "a109", "a110", "a111", "a112", "a113", "a114", "a115", "a116", "a117", "a118", "a119", "a120", "a121", "a122", "a123", "a124", "a125", "a126", "a127", "a128", "a129", "a130", "a131", "a132", "a133", "a134", "a135", "a136", "a137", "a138", "a139", "a140", "a141", "a142", "a143", "a144", "a145", "a146", "a147", "a148", "a149", "a150", "a151", "a152", "a153", "a154", "a155", "a156", "a157", "a158", "a159", "a160", "a161", "a162", "a163", "a164", "a165", "a166", "a167", "a168", "a169", "a170", "a171", "a172", "a173", "a174", "a175", "a176", "a177", "a178", "a179", "a180", "a181", "a182", "a183", "a184", "a185", "a186", "a187", "a188", "a189", "a190", "a191", "a192", "a193", "a194", "a195", "a196", "a197", "a198", "a199", "a200", "a201", "a202", "a203", "a204", "a205", "a206", "a207", "a208", "a209", "a210", "a211", "a212", "a213", "a214", "a215", "a216", "a217", "a218", "a219", "a220", "a221", "a222", "a223", "a224", "a225", "a226", "a227", "a228", "a229", "a230", "a231", "a232", "a233", "a234", "a235", "a236", "a237", "a238", "a239", "a240", "a241", "a242", "a243", "a244", "a245", "a246", "a247", "a248", "a249", "a250", "a251", "a252", "a253", "a254", "a255",
      "s36", "s37", "s38", "s39", "s40", "s41", "s42", "s43", "s44", "s45", "s46", "s47", "s48", "s49", "s50", "s51", "s52", "s53", "s54", "s55", "s56", "s57", "s58", "s59", "s60", "s61", "s62", "s63", "s64", "s65", "s66", "s67", "s68", "s69", "s70", "s71", "s72", "s73", "s74", "s75", "s76", "s77", "s78", "s79", "s80", "s81", "s82", "s83", "s84", "s85", "s86", "s87", "s88", "s89", "s90", "s91", "s92", "s93", "s94", "s95", "s96", "s97", "s98", "s99", "vcc", "scc", "memory");
}

extern "C" void kernel_launch(void* const* d_in, const int* in_sizes, int n_in, void* d_out, int out_size,
                              void* d_ws, size_t ws_size, hipStream_t stream) {
  Args a;
  for (int br = 0; br < 2; ++br) {
    a.x[br] = (const char*)d_in[5 * br + 0];
    a.k[br] = (const char*)d_in[5 * br + 1];
    a.v[br] = (const char*)d_in[5 * br + 2];
    a.wq[br] = (const char*)d_in[5 * br + 3];
    a.wo[br] = (const char*)d_in[5 * br + 4];
  }
  a.out = (char*)d_out;
  a.ws = (char*)d_ws;
  fused_k<<<256, 256, 0, stream>>>(a);
}
```
